```python
import torch

def _softmax(x, axis=-1):
    return torch.softmax(x, dim=axis)
def _gelu(x):
    return torch.nn.functional.gelu(x, approximate="tanh")
def _silu(x):
    return torch.nn.functional.silu(x)
def _sigmoid(x):
    return torch.sigmoid(x)
def _layernorm(x, axis=-1, eps=1e-5):
    m = x.mean(dim=axis, keepdim=True)
    v = x.var(dim=axis, keepdim=True, unbiased=False)
    return (x - m) / torch.sqrt(v + eps)
def _scatter_add(tgt, idx, src, axis=0):
    return tgt.clone().index_add_(axis, idx, src)
def _segment_sum(src, seg, n):
    out = torch.zeros((n,) + src.shape[1:], dtype=src.dtype)
    return out.index_add_(0, seg, src)
def _pad(x, pw):
    flat = tuple(v for pair in reversed(pw) for v in pair)
    return torch.nn.functional.pad(x, flat)

kernel_name = 'standalone__gqa_softmax__near_ridge'

def setup_inputs():

    _w = (lambda a: (lambda t: t.to(torch.bfloat16) if t.is_floating_point() else t)(torch.as_tensor(a)))
    B, Hq, Hkv, Sq, Sk, D = 8, 32, 8, 112, 1024, 128
    return {
        "q": _w((lambda *s: torch.randn(*s, dtype=torch.bfloat16) * 0.8)(B, Hq, Sq, D)),
        "k": _w((lambda *s: torch.randn(*s, dtype=torch.bfloat16) * 0.8)(B, Hkv, Sk, D)),
        "v": _w((lambda *s: torch.randn(*s, dtype=torch.bfloat16) * 0.05)(B, Hkv, Sk, D)),
    }

def reference(q, k, v):
    B, Hq, Sq, D = q.shape
    Hkv = k.shape[1]
    rep = Hq // Hkv
    k = (lambda x, n, axis: torch.repeat_interleave(x, n, dim=axis))(k, rep, axis=1)
    v = (lambda x, n, axis: torch.repeat_interleave(x, n, dim=axis))(v, rep, axis=1)
    scale = D ** -0.5
    scores = torch.einsum("bhqd,bhkd->bhqk", q, k) * scale
    p = _softmax(scores, axis=-1)
    return torch.einsum("bhqk,bhkd->bhqd", p, v)
```

```cpp
#include <hip/hip_runtime.h>
#include <hip/hip_bf16.h>

typedef short bf16x8 __attribute__((ext_vector_type(8)));
typedef short s16x4 __attribute__((ext_vector_type(4)));
typedef float f32x16 __attribute__((ext_vector_type(16)));
typedef float f32x2 __attribute__((ext_vector_type(2)));
typedef __bf16 bf16x2 __attribute__((ext_vector_type(2)));
typedef unsigned u32x4 __attribute__((ext_vector_type(4)));
typedef unsigned u32x2 __attribute__((ext_vector_type(2)));
#define LDS_AS __attribute__((address_space(3)))
#define GLB_AS __attribute__((address_space(1)))

constexpr int B = 8, HQ = 32, HKV = 8, SQ = 112, SK = 1024, D = 128;
constexpr int NT = SK / 64;
constexpr float C2 = 0.088388347648318447f * 1.4426950408889634f;

__device__ __forceinline__ unsigned cvtpk(float lo, float hi) {
  f32x2 v = {lo, hi};
  bf16x2 b = __builtin_convertvector(v, bf16x2);
  return __builtin_bit_cast(unsigned, b);
}

__global__ __launch_bounds__(256, 1) void gqa_v0(const short* __restrict__ Q, const short* __restrict__ K,
                                                 const short* __restrict__ V, short* __restrict__ O) {
  __shared__ __attribute__((aligned(16))) char lds[65536];
  const int tid = threadIdx.x, lane = tid & 63, w = __builtin_amdgcn_readfirstlane(tid >> 6);
  const int r32 = lane & 31, hi = lane >> 5;
  const int bid = blockIdx.x;
  const int pair = (bid & 7) * 8 + (bid >> 5), g = (bid >> 3) & 3;
  const int b = pair >> 3, kvh = pair & 7, hq = kvh * 4 + g;
  const short* Qh = Q + (size_t)(b * HQ + hq) * SQ * D;
  const char* Kh = (const char*)(K + (size_t)(b * HKV + kvh) * SK * D);
  const char* Vh = (const char*)(V + (size_t)(b * HKV + kvh) * SK * D);
  short* Oh = O + (size_t)(b * HQ + hq) * SQ * D;

  int qrow = w * 32 + r32;
  int qr_c = qrow < SQ ? qrow : SQ - 1;
  bf16x8 qf[8];
#pragma unroll
  for (int s = 0; s < 8; ++s) qf[s] = *(const bf16x8*)(Qh + qr_c * D + s * 16 + hi * 8);

  int src[2];
#pragma unroll
  for (int par = 0; par < 2; ++par)
    src[par] = ((lane >> 2) & 7) * 256 + (lane >> 5) * 64 + 16 * ((lane & 3) ^ (2 * par + ((lane >> 4) & 1)));

  auto stage = [&](int t, int buf) {
#pragma unroll
    for (int i = 0; i < 4; ++i) {
      const int p = w * 4 + i, g8 = p >> 1, c2 = p & 1;
      const int goff = t * 16384 + g8 * 2048 + c2 * 128 + src[g8 & 1];
      __builtin_amdgcn_global_load_lds((const GLB_AS void*)(Kh + goff), (LDS_AS void*)(lds + buf * 32768 + p * 1024), 16, 0, 0);
      __builtin_amdgcn_global_load_lds((const GLB_AS void*)(Vh + goff), (LDS_AS void*)(lds + buf * 32768 + 16384 + p * 1024), 16, 0, 0);
    }
  };

  int kaddr[2];
#pragma unroll
  for (int e = 0; e < 2; ++e)
    kaddr[e] = 2048 * (r32 >> 3) + 64 * (r32 & 7) + 16 * ((2 * e + hi) ^ ((r32 >> 2) & 3));
  int vaddr[2];
  {
    const int blk = (lane >> 4) & 1, q = (lane & 15) >> 2, p = lane & 3;
#pragma unroll
    for (int t2 = 0; t2 < 2; ++t2) {
      const int row = 8 * t2 + 4 * hi + q;
      const int ch = 2 * blk + (p >> 1);
      vaddr[t2] = 2048 * (row >> 3) + 64 * (row & 7) + 16 * ((ch & 3) ^ ((row >> 2) & 3)) + 8 * (p & 1);
    }
  }

  f32x16 o[4] = {};
  float m = -1e30f, l = 0.f;
  stage(0, 0);
  __syncthreads();
  for (int t = 0; t < NT; ++t) {
    const int buf = t & 1;
    if (t + 1 < NT) stage(t + 1, buf ^ 1);
    const char* kb_ = lds + buf * 32768;
    const char* vb_ = kb_ + 16384;
    f32x16 s0 = {}, s1 = {};
#pragma unroll
    for (int s = 0; s < 8; ++s) {
      bf16x8 a0 = *(const bf16x8*)(kb_ + kaddr[s & 1] + 512 * (s >> 1));
      bf16x8 a1 = *(const bf16x8*)(kb_ + kaddr[s & 1] + 512 * (s >> 1) + 8192);
      s0 = __builtin_amdgcn_mfma_f32_32x32x16_bf16(a0, qf[s], s0, 0, 0, 0);
      s1 = __builtin_amdgcn_mfma_f32_32x32x16_bf16(a1, qf[s], s1, 0, 0, 0);
    }
    float pm = s0[0];
#pragma unroll
    for (int r = 1; r < 16; ++r) pm = fmaxf(pm, s0[r]);
#pragma unroll
    for (int r = 0; r < 16; ++r) pm = fmaxf(pm, s1[r]);
    {
      auto rr = __builtin_amdgcn_permlane32_swap(__float_as_uint(pm), __float_as_uint(pm), false, false);
      pm = fmaxf(__uint_as_float(rr[0]), __uint_as_float(rr[1]));
    }
    const float mn = fmaxf(m, pm);
    const float alpha = __builtin_amdgcn_exp2f((m - mn) * C2);
    m = mn;
    const float nm = -mn * C2;
    float ps = 0.f;
#pragma unroll
    for (int r = 0; r < 16; ++r) { s0[r] = __builtin_amdgcn_exp2f(fmaf(s0[r], C2, nm)); ps += s0[r]; }
#pragma unroll
    for (int r = 0; r < 16; ++r) { s1[r] = __builtin_amdgcn_exp2f(fmaf(s1[r], C2, nm)); ps += s1[r]; }
    l = l * alpha + ps;
#pragma unroll
    for (int d = 0; d < 4; ++d)
#pragma unroll
      for (int r = 0; r < 16; ++r) o[d][r] *= alpha;
    bf16x8 pf[4];
#pragma unroll
    for (int ks = 0; ks < 2; ++ks) {
      u32x4 a = {cvtpk(s0[8 * ks], s0[8 * ks + 1]), cvtpk(s0[8 * ks + 2], s0[8 * ks + 3]), cvtpk(s0[8 * ks + 4], s0[8 * ks + 5]), cvtpk(s0[8 * ks + 6], s0[8 * ks + 7])};
      u32x4 c = {cvtpk(s1[8 * ks], s1[8 * ks + 1]), cvtpk(s1[8 * ks + 2], s1[8 * ks + 3]), cvtpk(s1[8 * ks + 4], s1[8 * ks + 5]), cvtpk(s1[8 * ks + 6], s1[8 * ks + 7])};
      pf[ks] = __builtin_bit_cast(bf16x8, a);
      pf[2 + ks] = __builtin_bit_cast(bf16x8, c);
    }
#pragma unroll
    for (int ks = 0; ks < 4; ++ks)
#pragma unroll
      for (int d = 0; d < 4; ++d) {
        s16x4 lo = __builtin_amdgcn_ds_read_tr16_b64_v4i16((LDS_AS s16x4*)(vb_ + vaddr[0] + 4096 * ks + 512 * d));
        s16x4 up = __builtin_amdgcn_ds_read_tr16_b64_v4i16((LDS_AS s16x4*)(vb_ + vaddr[1] + 4096 * ks + 512 * d));
        bf16x8 a = __builtin_shufflevector(lo, up, 0, 1, 2, 3, 4, 5, 6, 7);
        o[d] = __builtin_amdgcn_mfma_f32_32x32x16_bf16(a, pf[ks], o[d], 0, 0, 0);
      }
    __syncthreads();
  }
  {
    auto rr = __builtin_amdgcn_permlane32_swap(__float_as_uint(l), __float_as_uint(l), false, false);
    l = __uint_as_float(rr[0]) + __uint_as_float(rr[1]);
  }
  const float il = 1.f / l;
  if (qrow < SQ) {
#pragma unroll
    for (int d = 0; d < 4; ++d)
#pragma unroll
      for (int gq = 0; gq < 4; ++gq) {
        u32x2 wv = {cvtpk(o[d][4 * gq] * il, o[d][4 * gq + 1] * il), cvtpk(o[d][4 * gq + 2] * il, o[d][4 * gq + 3] * il)};
        *(u32x2*)(Oh + qrow * D + 32 * d + 8 * gq + 4 * hi) = wv;
      }
  }
}

extern "C" void kernel_launch(void* const* d_in, const int* in_sizes, int n_in, void* d_out, int out_size, void* d_ws,
                              size_t ws_size, hipStream_t stream) {
  gqa_v0<<<256, 256, 0, stream>>>((const short*)d_in[0], (const short*)d_in[1], (const short*)d_in[2], (short*)d_out);
}
```

```cpp
#include <hip/hip_runtime.h>
#include <hip/hip_bf16.h>
typedef short bf16x8_t __attribute__((ext_vector_type(8)));
typedef short s16x4_t __attribute__((ext_vector_type(4)));
typedef float f32x16_t __attribute__((ext_vector_type(16)));
typedef float f32x2_t __attribute__((ext_vector_type(2)));
typedef __bf16 bf16x2_t __attribute__((ext_vector_type(2)));
typedef unsigned u32x4_t __attribute__((ext_vector_type(4)));
typedef unsigned u32x2_t __attribute__((ext_vector_type(2)));
#define LDS_AS __attribute__((address_space(3)))
#define GLB_AS __attribute__((address_space(1)))

__device__ __forceinline__ unsigned pack_bf16(float lo, float hi) {
  f32x2_t v = {lo, hi};
  return __builtin_bit_cast(unsigned, __builtin_convertvector(v, bf16x2_t));
}

__device__ __forceinline__ void slow_block(const char* Q, const char* K, const char* V, char* O, unsigned bid, unsigned tid, char* lds) {
  asm volatile("s_waitcnt vmcnt(0) lgkmcnt(0)" ::: "memory");
  const int lane = tid & 63, w = __builtin_amdgcn_readfirstlane(tid >> 6), qh = w & 1, kh = w >> 1;
  const int r32 = lane & 31, hi = lane >> 5;
  const int pair = (bid & 7) * 8 + (bid >> 5), g = (bid >> 3) & 3, head = pair * 4 + g;
  const char* Qh = Q + (size_t)head * 28672;
  const char* Kh = K + (size_t)pair * 262144;
  const char* Vh = V + (size_t)pair * 262144;
  char* Oh = O + (size_t)head * 28672;
  char* buf = lds + kh * 81920 + 40960 + qh * 16384;
  const float C2 = 0.088388347648318447f * 1.4426950408889634f;
  int src[2], kaddr[2], vaddr[2];
#pragma unroll
  for (int e = 0; e < 2; ++e) {
    src[e] = ((lane >> 2) & 7) * 256 + (lane >> 5) * 64 + 16 * ((lane & 3) ^ (2 * e + ((lane >> 4) & 1)));
    kaddr[e] = 2048 * (r32 >> 3) + 64 * (r32 & 7) + 16 * ((2 * e + hi) ^ ((r32 >> 2) & 3));
    const int blk = (lane >> 4) & 1, q = (lane & 15) >> 2, p = lane & 3, row = 8 * e + 4 * hi + q, ch = 2 * blk + (p >> 1);
    vaddr[e] = 2048 * (row >> 3) + 64 * (row & 7) + 16 * ((ch & 3) ^ ((row >> 2) & 3)) + 8 * (p & 1);
  }
  for (int pass = 0; pass < 2; ++pass) {
    const int slot = 2 * pass + (r32 >> 4), blk16 = slot == 3 ? 3 : 4 * qh + slot, qrow = blk16 * 16 + (r32 & 15);
    bf16x8_t qf[8];
#pragma unroll
    for (int s = 0; s < 8; ++s) qf[s] = *(const bf16x8_t*)(Qh + qrow * 256 + s * 32 + hi * 16);
    f32x16_t o[4] = {};
    float m = -__builtin_inff(), l = 0.f;
    for (int t = 0; t < 32; ++t) {
      asm volatile("s_waitcnt lgkmcnt(0)" ::: "memory");
#pragma unroll
      for (int p = 0; p < 8; ++p) {
        const int goff = t * 8192 + (p >> 1) * 2048 + (p & 1) * 128 + src[(p >> 1) & 1];
        __builtin_amdgcn_global_load_lds((const GLB_AS void*)(Kh + goff), (LDS_AS void*)(buf + p * 1024), 16, 0, 0);
        __builtin_amdgcn_global_load_lds((const GLB_AS void*)(Vh + goff), (LDS_AS void*)(buf + 8192 + p * 1024), 16, 0, 0);
      }
      asm volatile("s_waitcnt vmcnt(0)" ::: "memory");
      f32x16_t s0 = {};
#pragma unroll
      for (int s = 0; s < 8; ++s)
        s0 = __builtin_amdgcn_mfma_f32_32x32x16_bf16(*(const bf16x8_t*)(buf + kaddr[s & 1] + 512 * (s >> 1)), qf[s], s0, 0, 0, 0);
      float pm = s0[0];
#pragma unroll
      for (int r = 1; r < 16; ++r) pm = fmaxf(pm, s0[r]);
      pm = fmaxf(pm, __shfl_xor(pm, 32));
      const float mn = fmaxf(m, pm);
      const float ms = mn == -__builtin_inff() ? 0.f : mn;
      const float alpha = __builtin_amdgcn_exp2f((m - ms) * C2);
      m = mn;
      float ps = 0.f;
#pragma unroll
      for (int r = 0; r < 16; ++r) { s0[r] = __builtin_amdgcn_exp2f((s0[r] - ms) * C2); ps += s0[r]; }
      l = l * alpha + ps;
#pragma unroll
      for (int d = 0; d < 4; ++d)
#pragma unroll
        for (int r = 0; r < 16; ++r) o[d][r] *= alpha;
#pragma unroll
      for (int ks = 0; ks < 2; ++ks) {
        const u32x4_t a = {pack_bf16(s0[8 * ks], s0[8 * ks + 1]), pack_bf16(s0[8 * ks + 2], s0[8 * ks + 3]),
                           pack_bf16(s0[8 * ks + 4], s0[8 * ks + 5]), pack_bf16(s0[8 * ks + 6], s0[8 * ks + 7])};
        const bf16x8_t pf = __builtin_bit_cast(bf16x8_t, a);
#pragma unroll
        for (int d = 0; d < 4; ++d) {
          const s16x4_t lo = __builtin_amdgcn_ds_read_tr16_b64_v4i16((LDS_AS s16x4_t*)(buf + 8192 + vaddr[0] + 4096 * ks + 512 * d));
          const s16x4_t up = __builtin_amdgcn_ds_read_tr16_b64_v4i16((LDS_AS s16x4_t*)(buf + 8192 + vaddr[1] + 4096 * ks + 512 * d));
          o[d] = __builtin_amdgcn_mfma_f32_32x32x16_bf16(__builtin_shufflevector(lo, up, 0, 1, 2, 3, 4, 5, 6, 7), pf, o[d], 0, 0, 0);
        }
      }
    }
    l += __shfl_xor(l, 32);
    const float il = 1.f / l;
#pragma unroll
    for (int d = 0; d < 4; ++d) {
      const bool own = slot == 3 ? d == w : (d >> 1) == kh;
      if (own) {
#pragma unroll
        for (int gq = 0; gq < 4; ++gq) {
          const u32x2_t wv = {pack_bf16(o[d][4 * gq] * il, o[d][4 * gq + 1] * il), pack_bf16(o[d][4 * gq + 2] * il, o[d][4 * gq + 3] * il)};
          *(u32x2_t*)(Oh + qrow * 256 + 64 * d + 16 * gq + 8 * hi) = wv;
        }
      }
    }
  }
}

__global__ __launch_bounds__(256, 1) void gqa_fwd(const char* __restrict__ Q, const char* __restrict__ K, const char* __restrict__ V, char* __restrict__ O) {
  __shared__ __attribute__((aligned(16))) char lds[163840];

  const unsigned tid = threadIdx.x, bid = blockIdx.x;
  const unsigned ldsb = __builtin_amdgcn_readfirstlane((unsigned)(size_t)lds);
  unsigned bad;
  asm volatile(
    "s_cmp_lt_u32 %5, 8\n\t"
    "s_cbranch_scc0 L_nopf_%=\n\t"
    "s_getpc_b64 s[64:65]\n\t"
    "L_pc_%=:\n\t"
    "v_lshlrev_b32 v20, 7, %6\n\t"
    "global_load_dword v252, v20, s[64:65]\n\t"
    "v_add_u32 v20, 32768, v20\n\t"
    "v_min_u32 v20, L_end_%=-L_pc_%=-4, v20\n\t"
    "global_load_dword v252, v20, s[64:65]\n\t"
    "L_nopf_%=:\n\t"
    "s_load_dwordx8 s[40:47], %1, 0\n\t"
    "v_lshrrev_b32 v18, 6, %6\n\t"
    "v_and_b32 v1, 63, %6\n\t"
    "s_and_b32 s60, %5, 7\n\t"
    "v_readfirstlane_b32 s56, v18\n\t"
    "s_lshr_b32 s61, %5, 3\n\t"
    "s_lshl_b32 s60, s60, 5\n\t"
    "s_add_u32 s60, s60, s61\n\t"
    "s_mul_i32 s74, s60, 28672\n\t"
    "v_lshrrev_b32 v18, 4, v1\n\t"
    "v_and_b32 v19, 15, v1\n\t"
    "v_xor_b32 v20, v19, v18\n\t"
    "v_lshlrev_b32 v21, 8, v18\n\t"
    "v_lshl_or_b32 v2, v20, 4, v21\n\t"
    "s_and_b32 s57, s56, 1\n\t"
    "s_lshr_b32 s58, s56, 1\n\t"
    "v_xor_b32 v3, 80, v2\n\t"
    "v_xor_b32 v4, 144, v2\n\t"
    "v_xor_b32 v5, 192, v2\n\t"
    "s_mul_i32 s55, s58, 81920\n\t"
    "s_lshl_b32 s63, s57, 13\n\t"
    "s_add_u32 s59, s55, s63\n\t"
    "s_add_u32 s59, s59, 40960\n\t"
    "s_mov_b32 m0, s59\n\t"
    "s_lshl_b32 s64, s58, 13\n\t"
    "s_lshl_b32 s63, s63, 1\n\t"
    "s_add_u32 s64, s64, s63\n\t"
    "s_mov_b32 s38, 28672\n\t"
    "s_mov_b32 s39, 0x20000\n\t"
    "s_waitcnt lgkmcnt(0)\n\t"
    "s_add_u32 s36, s40, s74\n\t"
    "s_addc_u32 s37, s41, 0\n\t"
    "buffer_load_dwordx4 v2, s[36:39], s64 offen offset:0 nt lds\n\t"
    "buffer_load_dwordx4 v3, s[36:39], s64 offen offset:1024 nt lds\n\t"
    "buffer_load_dwordx4 v4, s[36:39], s64 offen offset:2048 nt lds\n\t"
    "buffer_load_dwordx4 v5, s[36:39], s64 offen offset:3072 nt lds\n\t"
    "s_add_u32 m0, s59, 4096\n\t"
    "s_add_u32 s65, s64, 4096\n\t"
    "buffer_load_dwordx4 v2, s[36:39], s65 offen offset:0 nt lds\n\t"
    "buffer_load_dwordx4 v3, s[36:39], s65 offen offset:1024 nt lds\n\t"
    "buffer_load_dwordx4 v4, s[36:39], s65 offen offset:2048 nt lds\n\t"
    "buffer_load_dwordx4 v5, s[36:39], s65 offen offset:3072 nt lds\n\t"
    "s_lshr_b32 s60, s60, 2\n\t"
    "s_lshl_b32 s54, s57, 12\n\t"
    "s_add_u32 s52, s55, s54\n\t"
    "s_add_u32 s53, s52, 40960\n\t"
    "s_lshl_b32 s60, s60, 18\n\t"
    "s_lshl_b32 s61, s58, 17\n\t"
    "s_add_u32 s60, s60, s61\n\t"
    "s_add_u32 s48, s46, s74\n\t"
    "s_addc_u32 s49, s47, 0\n\t"
    "s_add_u32 s40, s42, s60\n\t"
    "s_addc_u32 s41, s43, 0\n\t"
    "s_mov_b32 s42, 131072\n\t"
    "s_mov_b32 s43, 0x20000\n\t"
    "s_add_u32 m0, s52, 0\n\t"
    "s_add_u32 s61, s54, 0\n\t"
    "buffer_load_dwordx4 v2, s[40:43], s61 offen offset:0 nt lds\n\t"
    "buffer_load_dwordx4 v3, s[40:43], s61 offen offset:1024 nt lds\n\t"
    "buffer_load_dwordx4 v4, s[40:43], s61 offen offset:2048 nt lds\n\t"
    "buffer_load_dwordx4 v5, s[40:43], s61 offen offset:3072 nt lds\n\t"
    "s_add_u32 m0, s52, 8192\n\t"
    "s_add_u32 s62, s54, 8192\n\t"
    "buffer_load_dwordx4 v2, s[40:43], s62 offen offset:0 nt lds\n\t"
    "buffer_load_dwordx4 v3, s[40:43], s62 offen offset:1024 nt lds\n\t"
    "buffer_load_dwordx4 v4, s[40:43], s62 offen offset:2048 nt lds\n\t"
    "buffer_load_dwordx4 v5, s[40:43], s62 offen offset:3072 nt lds\n\t"
    "s_add_u32 s44, s44, s60\n\t"
    "s_addc_u32 s45, s45, 0\n\t"
    "s_sub_u32 s44, s44, 896\n\t"
    "s_subb_u32 s45, s45, 0\n\t"
    "s_and_b32 s45, s45, 0xffff\n\t"
    "s_mov_b32 s46, 131968\n\t"
    "s_mov_b32 s47, 0x20000\n\t"
    "v_lshrrev_b32 v22, 2, v1\n\t"
    "v_and_b32 v22, 7, v22\n\t"
    "v_lshlrev_b32 v22, 8, v22\n\t"
    "v_lshrrev_b32 v23, 5, v1\n\t"
    "v_lshl_add_u32 v22, v23, 6, v22\n\t"
    "v_bfe_u32 v23, v1, 4, 1\n\t"
    "v_and_b32 v24, 3, v1\n\t"
    "v_xor_b32 v24, v24, v23\n\t"
    "v_xor_b32 v25, 2, v24\n\t"
    "v_lshl_add_u32 v7, v24, 4, v22\n\t"
    "v_lshl_add_u32 v9, v25, 4, v22\n\t"
    "v_add_u32 v6, 896, v7\n\t"
    "v_add_u32 v8, 896, v9\n\t"
    "v_and_b32 v22, 3, v19\n\t"
    "v_bfe_u32 v23, v19, 2, 1\n\t"
    "v_bfe_u32 v24, v19, 3, 1\n\t"
    "v_lshl_add_u32 v22, v23, 3, v22\n\t"
    "v_lshl_add_u32 v22, v24, 2, v22\n\t"
    "v_xor_b32 v23, v23, v24\n\t"
    "v_xor_b32 v24, v22, v23\n\t"
    "v_xor_b32 v24, v24, v18\n\t"
    "v_lshlrev_b32 v22, 8, v22\n\t"
    "v_add_u32 v22, s55, v22\n\t"
    "v_xor_b32 v25, 0, v24\n\t"
    "v_lshl_add_u32 v10, v25, 4, v22\n\t"
    "v_xor_b32 v25, 4, v24\n\t"
    "v_lshl_add_u32 v11, v25, 4, v22\n\t"
    "v_xor_b32 v25, 8, v24\n\t"
    "v_lshl_add_u32 v12, v25, 4, v22\n\t"
    "v_xor_b32 v25, 12, v24\n\t"
    "v_lshl_add_u32 v13, v25, 4, v22\n\t"
    "v_xor_b32 v24, v19, v23\n\t"
    "v_xor_b32 v24, v24, v18\n\t"
    "v_lshlrev_b32 v22, 8, v19\n\t"
    "v_add_u32 v22, s59, v22\n\t"
    "v_xor_b32 v25, 0, v24\n\t"
    "v_lshl_add_u32 v26, v25, 4, v22\n\t"
    "v_xor_b32 v25, 4, v24\n\t"
    "v_lshl_add_u32 v27, v25, 4, v22\n\t"
    "v_xor_b32 v25, 8, v24\n\t"
    "v_lshl_add_u32 v28, v25, 4, v22\n\t"
    "v_xor_b32 v25, 12, v24\n\t"
    "v_lshl_add_u32 v29, v25, 4, v22\n\t"
    "v_and_b32 v22, 1, v18\n\t"
    "v_lshrrev_b32 v23, 1, v18\n\t"
    "v_lshl_add_u32 v24, v22, 1, v23\n\t"
    "v_lshlrev_b32 v25, 11, v22\n\t"
    "v_bfe_u32 v22, v1, 2, 2\n\t"
    "v_lshl_add_u32 v22, v23, 2, v22\n\t"
    "v_lshl_add_u32 v25, v22, 6, v25\n\t"
    "v_and_b32 v22, 1, v1\n\t"
    "v_lshl_add_u32 v25, v22, 3, v25\n\t"
    "v_bfe_u32 v22, v1, 1, 1\n\t"
    "v_xor_b32 v22, v22, v24\n\t"
    "v_xor_b32 v23, 2, v22\n\t"
    "s_lshl_b32 s61, s58, 10\n\t"
    "s_add_u32 s62, s55, 40960\n\t"
    "s_add_u32 s63, s62, s61\n\t"
    "s_sub_u32 s64, s62, s61\n\t"
    "v_lshl_add_u32 v22, v22, 4, v25\n\t"
    "v_lshl_add_u32 v23, v23, 4, v25\n\t"
    "v_add_u32 v14, s63, v22\n\t"
    "v_add_u32 v15, s63, v23\n\t"
    "v_add_u32 v16, s64, v22\n\t"
    "v_add_u32 v17, s64, v23\n\t"
    "v_mov_b32 v216, 0x3f803f80\n\t"
    "v_mov_b32 v217, 0x3f803f80\n\t"
    "v_mov_b32 v218, 0x3f803f80\n\t"
    "v_mov_b32 v219, 0x3f803f80\n\t"
    "s_mov_b32 s68, 0x3e0293ee\n\t"
    "s_mov_b32 s69, 0x3e0293ee\n\t"
    "v_lshlrev_b32 v30, 4, v1\n\t"
    "v_add_u32 v31, s59, v30\n\t"
    "s_mov_b32 s50, 28672\n\t"
    "s_mov_b32 s51, 0x20000\n\t"
    "s_mul_i32 s60, s57, 18432\n\t"
    "s_add_u32 s60, s60, s55\n\t"
    "v_add_u32 v220, s60, v30\n\t"
    "v_lshrrev_b32 v22, 2, v30\n\t"
    "s_lshl_b32 s61, s56, 10\n\t"
    "s_add_u32 s61, s61, %7\n\t"
    "s_add_u32 s61, s61, 36864\n\t"
    "v_add_u32 v221, s61, v22\n\t"
    "s_xor_b32 s62, s56, 1\n\t"
    "s_and_b32 s63, s62, 1\n\t"
    "s_mul_i32 s63, s63, 18432\n\t"
    "s_lshr_b32 s64, s62, 1\n\t"
    "s_mul_i32 s64, s64, 81920\n\t"
    "s_add_u32 s63, s63, s64\n\t"
    "s_add_u32 s63, s63, %7\n\t"
    "v_add_u32 v222, s63, v30\n\t"
    "s_lshl_b32 s62, s62, 10\n\t"
    "s_add_u32 s62, s62, %7\n\t"
    "s_add_u32 s62, s62, 36864\n\t"
    "v_add_u32 v225, s62, v22\n\t"
    "s_xor_b32 s62, s56, 2\n\t"
    "s_and_b32 s63, s62, 1\n\t"
    "s_mul_i32 s63, s63, 18432\n\t"
    "s_lshr_b32 s64, s62, 1\n\t"
    "s_mul_i32 s64, s64, 81920\n\t"
    "s_add_u32 s63, s63, s64\n\t"
    "s_add_u32 s63, s63, %7\n\t"
    "v_add_u32 v223, s63, v30\n\t"
    "s_lshl_b32 s62, s62, 10\n\t"
    "s_add_u32 s62, s62, %7\n\t"
    "s_add_u32 s62, s62, 36864\n\t"
    "v_add_u32 v226, s62, v22\n\t"
    "s_xor_b32 s62, s56, 3\n\t"
    "s_and_b32 s63, s62, 1\n\t"
    "s_mul_i32 s63, s63, 18432\n\t"
    "s_lshr_b32 s64, s62, 1\n\t"
    "s_mul_i32 s64, s64, 81920\n\t"
    "s_add_u32 s63, s63, s64\n\t"
    "s_add_u32 s63, s63, %7\n\t"
    "v_add_u32 v224, s63, v30\n\t"
    "s_lshl_b32 s62, s62, 10\n\t"
    "s_add_u32 s62, s62, %7\n\t"
    "s_add_u32 s62, s62, 36864\n\t"
    "v_add_u32 v227, s62, v22\n\t"
    "v_lshlrev_b32 v22, 8, v19\n\t"
    "v_and_b32 v23, 1, v18\n\t"
    "v_lshrrev_b32 v24, 1, v18\n\t"
    "v_lshl_add_u32 v22, v23, 5, v22\n\t"
    "v_lshl_add_u32 v22, v24, 4, v22\n\t"
    "s_lshl_b32 s62, s56, 6\n\t"
    "v_add_u32 v228, s62, v22\n\t"
    "v_lshrrev_b32 v25, 3, v19\n\t"
    "v_lshl_add_u32 v25, v25, 1, v23\n\t"
    "s_lshl_b32 s62, s57, 1\n\t"
    "v_xor_b32 v25, s62, v25\n\t"
    "v_and_b32 v22, 7, v19\n\t"
    "v_lshlrev_b32 v22, 8, v22\n\t"
    "v_lshl_add_u32 v22, v25, 5, v22\n\t"
    "v_lshl_add_u32 v22, v24, 4, v22\n\t"
    "s_lshl_b32 s62, s58, 7\n\t"
    "v_add_u32 v229, s62, v22\n\t"
    "s_waitcnt vmcnt(12)\n\t"
    "ds_read_b128 v[32:35], v26 offset:0\n\t"
    "ds_read_b128 v[36:39], v27 offset:0\n\t"
    "ds_read_b128 v[40:43], v28 offset:0\n\t"
    "ds_read_b128 v[44:47], v29 offset:0\n\t"
    "s_waitcnt lgkmcnt(2)\n\t"
    "v_lshlrev_b32 v22, 16, v32\n\t"
    "v_and_b32 v23, 0xffff0000, v32\n\t"
    "v_pk_mul_f32 v[22:23], v[22:23], s[68:69]\n\t"
    "v_cvt_pk_bf16_f32 v32, v22, v23\n\t"
    "v_lshlrev_b32 v24, 16, v33\n\t"
    "v_and_b32 v25, 0xffff0000, v33\n\t"
    "v_pk_mul_f32 v[24:25], v[24:25], s[68:69]\n\t"
    "v_cvt_pk_bf16_f32 v33, v24, v25\n\t"
    "v_lshlrev_b32 v22, 16, v34\n\t"
    "v_and_b32 v23, 0xffff0000, v34\n\t"
    "v_pk_mul_f32 v[22:23], v[22:23], s[68:69]\n\t"
    "v_cvt_pk_bf16_f32 v34, v22, v23\n\t"
    "v_lshlrev_b32 v24, 16, v35\n\t"
    "v_and_b32 v25, 0xffff0000, v35\n\t"
    "v_pk_mul_f32 v[24:25], v[24:25], s[68:69]\n\t"
    "v_cvt_pk_bf16_f32 v35, v24, v25\n\t"
    "ds_write_b128 v31, v[32:35] offset:0\n\t"
    "v_lshlrev_b32 v22, 16, v36\n\t"
    "v_and_b32 v23, 0xffff0000, v36\n\t"
    "v_pk_mul_f32 v[22:23], v[22:23], s[68:69]\n\t"
    "v_cvt_pk_bf16_f32 v36, v22, v23\n\t"
    "v_lshlrev_b32 v24, 16, v37\n\t"
    "v_and_b32 v25, 0xffff0000, v37\n\t"
    "v_pk_mul_f32 v[24:25], v[24:25], s[68:69]\n\t"
    "v_cvt_pk_bf16_f32 v37, v24, v25\n\t"
    "v_lshlrev_b32 v22, 16, v38\n\t"
    "v_and_b32 v23, 0xffff0000, v38\n\t"
    "v_pk_mul_f32 v[22:23], v[22:23], s[68:69]\n\t"
    "v_cvt_pk_bf16_f32 v38, v22, v23\n\t"
    "v_lshlrev_b32 v24, 16, v39\n\t"
    "v_and_b32 v25, 0xffff0000, v39\n\t"
    "v_pk_mul_f32 v[24:25], v[24:25], s[68:69]\n\t"
    "v_cvt_pk_bf16_f32 v39, v24, v25\n\t"
    "ds_write_b128 v31, v[36:39] offset:1024\n\t"
    "s_waitcnt lgkmcnt(2)\n\t"
    "v_lshlrev_b32 v22, 16, v40\n\t"
    "v_and_b32 v23, 0xffff0000, v40\n\t"
    "v_pk_mul_f32 v[22:23], v[22:23], s[68:69]\n\t"
    "v_cvt_pk_bf16_f32 v40, v22, v23\n\t"
    "v_lshlrev_b32 v24, 16, v41\n\t"
    "v_and_b32 v25, 0xffff0000, v41\n\t"
    "v_pk_mul_f32 v[24:25], v[24:25], s[68:69]\n\t"
    "v_cvt_pk_bf16_f32 v41, v24, v25\n\t"
    "v_lshlrev_b32 v22, 16, v42\n\t"
    "v_and_b32 v23, 0xffff0000, v42\n\t"
    "v_pk_mul_f32 v[22:23], v[22:23], s[68:69]\n\t"
    "v_cvt_pk_bf16_f32 v42, v22, v23\n\t"
    "v_lshlrev_b32 v24, 16, v43\n\t"
    "v_and_b32 v25, 0xffff0000, v43\n\t"
    "v_pk_mul_f32 v[24:25], v[24:25], s[68:69]\n\t"
    "v_cvt_pk_bf16_f32 v43, v24, v25\n\t"
    "ds_write_b128 v31, v[40:43] offset:2048\n\t"
    "v_lshlrev_b32 v22, 16, v44\n\t"
    "v_and_b32 v23, 0xffff0000, v44\n\t"
    "v_pk_mul_f32 v[22:23], v[22:23], s[68:69]\n\t"
    "v_cvt_pk_bf16_f32 v44, v22, v23\n\t"
    "v_lshlrev_b32 v24, 16, v45\n\t"
    "v_and_b32 v25, 0xffff0000, v45\n\t"
    "v_pk_mul_f32 v[24:25], v[24:25], s[68:69]\n\t"
    "v_cvt_pk_bf16_f32 v45, v24, v25\n\t"
    "v_lshlrev_b32 v22, 16, v46\n\t"
    "v_and_b32 v23, 0xffff0000, v46\n\t"
    "v_pk_mul_f32 v[22:23], v[22:23], s[68:69]\n\t"
    "v_cvt_pk_bf16_f32 v46, v22, v23\n\t"
    "v_lshlrev_b32 v24, 16, v47\n\t"
    "v_and_b32 v25, 0xffff0000, v47\n\t"
    "v_pk_mul_f32 v[24:25], v[24:25], s[68:69]\n\t"
    "v_cvt_pk_bf16_f32 v47, v24, v25\n\t"
    "ds_write_b128 v31, v[44:47] offset:3072\n\t"
    "s_waitcnt vmcnt(8)\n\t"
    "ds_read_b128 v[48:51], v26 offset:4096\n\t"
    "ds_read_b128 v[52:55], v27 offset:4096\n\t"
    "ds_read_b128 v[56:59], v28 offset:4096\n\t"
    "ds_read_b128 v[60:63], v29 offset:4096\n\t"
    "s_waitcnt lgkmcnt(2)\n\t"
    "v_lshlrev_b32 v22, 16, v48\n\t"
    "v_and_b32 v23, 0xffff0000, v48\n\t"
    "v_pk_mul_f32 v[22:23], v[22:23], s[68:69]\n\t"
    "v_cvt_pk_bf16_f32 v48, v22, v23\n\t"
    "v_lshlrev_b32 v24, 16, v49\n\t"
    "v_and_b32 v25, 0xffff0000, v49\n\t"
    "v_pk_mul_f32 v[24:25], v[24:25], s[68:69]\n\t"
    "v_cvt_pk_bf16_f32 v49, v24, v25\n\t"
    "s_add_u32 m0, s53, 16384\n\t"
    "s_add_u32 s63, s54, 0\n\t"
    "buffer_load_dwordx4 v6, s[44:47], s63 offen offset:0 nt lds\n\t"
    "v_lshlrev_b32 v22, 16, v50\n\t"
    "v_and_b32 v23, 0xffff0000, v50\n\t"
    "v_pk_mul_f32 v[22:23], v[22:23], s[68:69]\n\t"
    "v_cvt_pk_bf16_f32 v50, v22, v23\n\t"
    "v_lshlrev_b32 v24, 16, v51\n\t"
    "v_and_b32 v25, 0xffff0000, v51\n\t"
    "v_pk_mul_f32 v[24:25], v[24:25], s[68:69]\n\t"
    "v_cvt_pk_bf16_f32 v51, v24, v25\n\t"
    "buffer_load_dwordx4 v7, s[44:47], s63 offen offset:1024 nt lds\n\t"
    "ds_write_b128 v31, v[48:51] offset:4096\n\t"
    "v_lshlrev_b32 v22, 16, v52\n\t"
    "v_and_b32 v23, 0xffff0000, v52\n\t"
    "v_pk_mul_f32 v[22:23], v[22:23], s[68:69]\n\t"
    "v_cvt_pk_bf16_f32 v52, v22, v23\n\t"
    "v_lshlrev_b32 v24, 16, v53\n\t"
    "v_and_b32 v25, 0xffff0000, v53\n\t"
    "v_pk_mul_f32 v[24:25], v[24:25], s[68:69]\n\t"
    "v_cvt_pk_bf16_f32 v53, v24, v25\n\t"
    "buffer_load_dwordx4 v8, s[44:47], s63 offen offset:2048 nt lds\n\t"
    "v_lshlrev_b32 v22, 16, v54\n\t"
    "v_and_b32 v23, 0xffff0000, v54\n\t"
    "v_pk_mul_f32 v[22:23], v[22:23], s[68:69]\n\t"
    "v_cvt_pk_bf16_f32 v54, v22, v23\n\t"
    "v_lshlrev_b32 v24, 16, v55\n\t"
    "v_and_b32 v25, 0xffff0000, v55\n\t"
    "v_pk_mul_f32 v[24:25], v[24:25], s[68:69]\n\t"
    "v_cvt_pk_bf16_f32 v55, v24, v25\n\t"
    "buffer_load_dwordx4 v9, s[44:47], s63 offen offset:3072 nt lds\n\t"
    "ds_write_b128 v31, v[52:55] offset:5120\n\t"
    "s_waitcnt lgkmcnt(2)\n\t"
    "v_lshlrev_b32 v22, 16, v56\n\t"
    "v_and_b32 v23, 0xffff0000, v56\n\t"
    "v_pk_mul_f32 v[22:23], v[22:23], s[68:69]\n\t"
    "v_cvt_pk_bf16_f32 v56, v22, v23\n\t"
    "v_lshlrev_b32 v24, 16, v57\n\t"
    "v_and_b32 v25, 0xffff0000, v57\n\t"
    "v_pk_mul_f32 v[24:25], v[24:25], s[68:69]\n\t"
    "v_cvt_pk_bf16_f32 v57, v24, v25\n\t"
    "s_add_u32 m0, s52, 16384\n\t"
    "s_add_u32 s61, s54, 16384\n\t"
    "buffer_load_dwordx4 v2, s[40:43], s61 offen offset:0 nt lds\n\t"
    "v_lshlrev_b32 v22, 16, v58\n\t"
    "v_and_b32 v23, 0xffff0000, v58\n\t"
    "v_pk_mul_f32 v[22:23], v[22:23], s[68:69]\n\t"
    "v_cvt_pk_bf16_f32 v58, v22, v23\n\t"
    "v_lshlrev_b32 v24, 16, v59\n\t"
    "v_and_b32 v25, 0xffff0000, v59\n\t"
    "v_pk_mul_f32 v[24:25], v[24:25], s[68:69]\n\t"
    "v_cvt_pk_bf16_f32 v59, v24, v25\n\t"
    "buffer_load_dwordx4 v3, s[40:43], s61 offen offset:1024 nt lds\n\t"
    "ds_write_b128 v31, v[56:59] offset:6144\n\t"
    "v_lshlrev_b32 v22, 16, v60\n\t"
    "v_and_b32 v23, 0xffff0000, v60\n\t"
    "v_pk_mul_f32 v[22:23], v[22:23], s[68:69]\n\t"
    "v_cvt_pk_bf16_f32 v60, v22, v23\n\t"
    "v_lshlrev_b32 v24, 16, v61\n\t"
    "v_and_b32 v25, 0xffff0000, v61\n\t"
    "v_pk_mul_f32 v[24:25], v[24:25], s[68:69]\n\t"
    "v_cvt_pk_bf16_f32 v61, v24, v25\n\t"
    "buffer_load_dwordx4 v4, s[40:43], s61 offen offset:2048 nt lds\n\t"
    "v_lshlrev_b32 v22, 16, v62\n\t"
    "v_and_b32 v23, 0xffff0000, v62\n\t"
    "v_pk_mul_f32 v[22:23], v[22:23], s[68:69]\n\t"
    "v_cvt_pk_bf16_f32 v62, v22, v23\n\t"
    "v_lshlrev_b32 v24, 16, v63\n\t"
    "v_and_b32 v25, 0xffff0000, v63\n\t"
    "v_pk_mul_f32 v[24:25], v[24:25], s[68:69]\n\t"
    "v_cvt_pk_bf16_f32 v63, v24, v25\n\t"
    "buffer_load_dwordx4 v5, s[40:43], s61 offen offset:3072 nt lds\n\t"
    "ds_write_b128 v31, v[60:63] offset:7168\n\t"
    "s_waitcnt vmcnt(8) lgkmcnt(0)\n\t"
    "s_barrier\n\t"
    "s_cmp_lg_u32 s57, 0\n\t"
    "s_cbranch_scc1 L_var1_%=\n\t"
    "s_add_u32 s61, %7, 40960\n\t"
    "v_add_u32 v22, s61, v30\n\t"
    "v_add_u32 v23, 81920, v22\n\t"
    "ds_read_b128 a[120:123], v10 offset:0\n\t"
    "ds_read_b128 a[136:139], v10 offset:4096\n\t"
    "ds_read_b128 v[96:99], v22 offset:0\n\t"
    "ds_read_b128 a[124:127], v11 offset:0\n\t"
    "ds_read_b128 a[140:143], v11 offset:4096\n\t"
    "ds_read_b128 v[100:103], v22 offset:1024\n\t"
    "ds_read_b128 a[128:131], v12 offset:0\n\t"
    "ds_read_b128 a[144:147], v12 offset:4096\n\t"
    "ds_read_b128 v[104:107], v22 offset:2048\n\t"
    "ds_read_b128 a[132:135], v13 offset:0\n\t"
    "ds_read_b128 a[148:151], v13 offset:4096\n\t"
    "ds_read_b128 v[108:111], v22 offset:3072\n\t"
    "ds_read_b128 v[112:115], v22 offset:4096\n\t"
    "ds_read_b128 v[116:119], v22 offset:5120\n\t"
    "ds_read_b128 v[120:123], v22 offset:6144\n\t"
    "ds_read_b128 v[124:127], v22 offset:7168\n\t"
    "ds_read_b128 v[128:131], v23 offset:0\n\t"
    "ds_read_b128 v[132:135], v23 offset:1024\n\t"
    "ds_read_b128 v[136:139], v23 offset:2048\n\t"
    "ds_read_b128 v[140:143], v23 offset:3072\n\t"
    "ds_read_b128 v[144:147], v23 offset:4096\n\t"
    "ds_read_b128 v[148:151], v23 offset:5120\n\t"
    "ds_read_b128 v[152:155], v23 offset:6144\n\t"
    "ds_read_b128 v[156:159], v23 offset:7168\n\t"
    "s_mov_b32 s36, 0\n\t"
    "s_mov_b32 s37, 4096\n\t"
    "s_mov_b32 s38, 8192\n\t"
    "s_mov_b32 s39, 12288\n\t"
    "s_mov_b32 s75, 0x07060302\n\t"
    "s_waitcnt lgkmcnt(15)\n\t"
    "v_mfma_f32_16x16x32_bf16 v[32:35], a[120:123], v[96:99], 0\n\t"
    "s_add_u32 s60, s54, 8192\n\t"
    "ds_read_b128 a[88:91], v10 offset:8192\n\t"
    "ds_read_b128 a[92:95], v11 offset:8192\n\t"
    "v_mfma_f32_16x16x32_bf16 v[36:39], a[136:139], v[96:99], 0\n\t"
    "s_add_u32 m0, s53, 24576\n\t"
    "ds_read_b128 a[96:99], v12 offset:8192\n\t"
    "ds_read_b128 a[100:103], v13 offset:8192\n\t"
    "v_mfma_f32_16x16x32_bf16 v[32:35], a[124:127], v[100:103], v[32:35]\n\t"
    "buffer_load_dwordx4 v6, s[44:47], s60 offen offset:0 nt lds\n\t"
    "v_mfma_f32_16x16x32_bf16 v[36:39], a[140:143], v[100:103], v[36:39]\n\t"
    "s_waitcnt lgkmcnt(15)\n\t"
    "ds_read_b128 a[104:107], v10 offset:12288\n\t"
    "ds_read_b128 a[108:111], v11 offset:12288\n\t"
    "v_mfma_f32_16x16x32_bf16 v[32:35], a[128:131], v[104:107], v[32:35]\n\t"
    "ds_read_b128 a[112:115], v12 offset:12288\n\t"
    "ds_read_b128 a[116:119], v13 offset:12288\n\t"
    "v_mfma_f32_16x16x32_bf16 v[36:39], a[144:147], v[104:107], v[36:39]\n\t"
    "v_mfma_f32_16x16x32_bf16 v[32:35], a[132:135], v[108:111], v[32:35]\n\t"
    "v_mfma_f32_16x16x32_bf16 v[36:39], a[148:151], v[108:111], v[36:39]\n\t"
    "s_waitcnt lgkmcnt(15)\n\t"
    "v_mfma_f32_16x16x32_bf16 v[40:43], a[120:123], v[112:115], 0\n\t"
    "buffer_load_dwordx4 v7, s[44:47], s60 offen offset:1024 nt lds\n\t"
    "v_mfma_f32_16x16x32_bf16 v[44:47], a[136:139], v[112:115], 0\n\t"
    "v_mfma_f32_16x16x32_bf16 v[40:43], a[124:127], v[116:119], v[40:43]\n\t"
    "v_mfma_f32_16x16x32_bf16 v[44:47], a[140:143], v[116:119], v[44:47]\n\t"
    "s_waitcnt lgkmcnt(15)\n\t"
    "v_exp_f32 v32, v32\n\t"
    "v_mfma_f32_16x16x32_bf16 v[40:43], a[128:131], v[120:123], v[40:43]\n\t"
    "v_exp_f32 v33, v33\n\t"
    "v_mfma_f32_16x16x32_bf16 v[44:47], a[144:147], v[120:123], v[44:47]\n\t"
    "v_exp_f32 v34, v34\n\t"
    "v_mfma_f32_16x16x32_bf16 v[40:43], a[132:135], v[124:127], v[40:43]\n\t"
    "buffer_load_dwordx4 v8, s[44:47], s60 offen offset:2048 nt lds\n\t"
    "v_mfma_f32_16x16x32_bf16 v[44:47], a[148:151], v[124:127], v[44:47]\n\t"
    "s_waitcnt lgkmcnt(14)\n\t"
    "v_exp_f32 v35, v35\n\t"
    "v_mfma_f32_16x16x32_bf16 v[48:51], a[120:123], v[128:131], 0\n\t"
    "v_exp_f32 v36, v36\n\t"
    "v_mfma_f32_16x16x32_bf16 v[52:55], a[136:139], v[128:131], 0\n\t"
    "v_exp_f32 v37, v37\n\t"
    "v_mfma_f32_16x16x32_bf16 v[48:51], a[124:127], v[132:135], v[48:51]\n\t"
    "v_cvt_pk_bf16_f32 v32, v32, v33\n\t"
    "v_cvt_pk_bf16_f32 v33, v34, v35\n\t"
    "v_mfma_f32_16x16x32_bf16 v[52:55], a[140:143], v[132:135], v[52:55]\n\t"
    "s_waitcnt lgkmcnt(12)\n\t"
    "v_exp_f32 v38, v38\n\t"
    "v_mfma_f32_16x16x32_bf16 v[48:51], a[128:131], v[136:139], v[48:51]\n\t"
    "buffer_load_dwordx4 v9, s[44:47], s60 offen offset:3072 nt lds\n\t"
    "v_mfma_f32_16x16x32_bf16 v[52:55], a[144:147], v[136:139], v[52:55]\n\t"
    "s_add_u32 s61, s54, 24576\n\t"
    "v_exp_f32 v39, v39\n\t"
    "v_mfma_f32_16x16x32_bf16 v[48:51], a[132:135], v[140:143], v[48:51]\n\t"
    "s_add_u32 m0, s52, 24576\n\t"
    "v_cvt_pk_bf16_f32 v34, v36, v37\n\t"
    "v_cvt_pk_bf16_f32 v35, v38, v39\n\t"
    "v_mfma_f32_16x16x32_bf16 v[52:55], a[148:151], v[140:143], v[52:55]\n\t"
    "s_waitcnt lgkmcnt(10)\n\t"
    "v_exp_f32 v40, v40\n\t"
    "v_mfma_f32_16x16x32_bf16 v[56:59], a[120:123], v[144:147], 0\n\t"
    "v_exp_f32 v41, v41\n\t"
    "v_mfma_f32_16x16x32_bf16 v[60:63], a[136:139], v[144:147], 0\n\t"
    "v_exp_f32 v42, v42\n\t"
    "v_mfma_f32_16x16x32_bf16 v[56:59], a[124:127], v[148:151], v[56:59]\n\t"
    "buffer_load_dwordx4 v2, s[40:43], s61 offen offset:0 nt lds\n\t"
    "v_mfma_f32_16x16x32_bf16 v[60:63], a[140:143], v[148:151], v[60:63]\n\t"
    "s_waitcnt lgkmcnt(8)\n\t"
    "v_exp_f32 v43, v43\n\t"
    "v_mfma_f32_16x16x32_bf16 v[56:59], a[128:131], v[152:155], v[56:59]\n\t"
    "v_exp_f32 v44, v44\n\t"
    "v_mfma_f32_16x16x32_bf16 v[60:63], a[144:147], v[152:155], v[60:63]\n\t"
    "v_exp_f32 v45, v45\n\t"
    "v_mfma_f32_16x16x32_bf16 v[56:59], a[132:135], v[156:159], v[56:59]\n\t"
    "v_cvt_pk_bf16_f32 v40, v40, v41\n\t"
    "v_cvt_pk_bf16_f32 v41, v42, v43\n\t"
    "v_mfma_f32_16x16x32_bf16 v[60:63], a[148:151], v[156:159], v[60:63]\n\t"
    "s_waitcnt vmcnt(9) lgkmcnt(0)\n\t"
    "s_barrier\n\t"
    "v_mfma_f32_16x16x32_bf16 v[64:67], a[88:91], v[96:99], 0\n\t"
    "buffer_load_dwordx4 v3, s[40:43], s61 offen offset:1024 nt lds\n\t"
    "v_mfma_f32_16x16x32_bf16 v[72:75], a[88:91], v[112:115], 0\n\t"
    "ds_read_b64_tr_b16 a[120:121], v14 offset:16384\n\t"
    "ds_read_b64_tr_b16 a[122:123], v14 offset:20480\n\t"
    "v_mfma_f32_16x16x32_bf16 v[80:83], a[88:91], v[128:131], 0\n\t"
    "v_exp_f32 v46, v46\n\t"
    "v_mfma_f32_16x16x32_bf16 v[64:67], a[92:95], v[100:103], v[64:67]\n\t"
    "ds_read_b64_tr_b16 a[124:125], v15 offset:16384\n\t"
    "v_cvt_pk_bf16_f32 v42, v44, v45\n\t"
    "v_mfma_f32_16x16x32_bf16 v[72:75], a[92:95], v[116:119], v[72:75]\n\t"
    "v_exp_f32 v47, v47\n\t"
    "v_mfma_f32_16x16x32_bf16 v[80:83], a[92:95], v[132:135], v[80:83]\n\t"
    "ds_read_b64_tr_b16 a[126:127], v15 offset:20480\n\t"
    "v_cvt_pk_bf16_f32 v43, v46, v47\n\t"
    "v_mfma_f32_16x16x32_bf16 v[64:67], a[96:99], v[104:107], v[64:67]\n\t"
    "buffer_load_dwordx4 v4, s[40:43], s61 offen offset:2048 nt lds\n\t"
    "v_mfma_f32_16x16x32_bf16 v[72:75], a[96:99], v[120:123], v[72:75]\n\t"
    "ds_read_b64_tr_b16 a[128:129], v14 offset:16896\n\t"
    "ds_read_b64_tr_b16 a[130:131], v14 offset:20992\n\t"
    "v_mfma_f32_16x16x32_bf16 v[80:83], a[96:99], v[136:139], v[80:83]\n\t"
    "v_exp_f32 v48, v48\n\t"
    "v_mfma_f32_16x16x32_bf16 v[64:67], a[100:103], v[108:111], v[64:67]\n\t"
    "v_exp_f32 v49, v49\n\t"
    "v_mfma_f32_16x16x32_bf16 v[72:75], a[100:103], v[124:127], v[72:75]\n\t"
    "ds_read_b64_tr_b16 a[132:133], v15 offset:16896\n\t"
    "ds_read_b64_tr_b16 a[134:135], v15 offset:20992\n\t"
    "v_mfma_f32_16x16x32_bf16 v[80:83], a[100:103], v[140:143], v[80:83]\n\t"
    "v_exp_f32 v50, v50\n\t"
    "v_mfma_f32_16x16x32_bf16 v[68:71], a[104:107], v[96:99], 0\n\t"
    "ds_read_b64_tr_b16 a[136:137], v16 offset:17408\n\t"
    "v_cvt_pk_bf16_f32 v48, v48, v49\n\t"
    "v_mfma_f32_16x16x32_bf16 v[76:79], a[104:107], v[112:115], 0\n\t"
    "buffer_load_dwordx4 v5, s[40:43], s61 offen offset:3072 nt lds\n\t"
    "v_mfma_f32_16x16x32_bf16 v[84:87], a[104:107], v[128:131], 0\n\t"
    "s_add_u32 s60, s54, 16384\n\t"
    "v_exp_f32 v51, v51\n\t"
    "v_mfma_f32_16x16x32_bf16 v[68:71], a[108:111], v[100:103], v[68:71]\n\t"
    "s_add_u32 m0, s53, 32768\n\t"
    "ds_read_b64_tr_b16 a[138:139], v16 offset:21504\n\t"
    "ds_read_b64_tr_b16 a[140:141], v17 offset:17408\n\t"
    "v_mfma_f32_16x16x32_bf16 v[76:79], a[108:111], v[116:119], v[76:79]\n\t"
    "ds_read_b64_tr_b16 a[142:143], v17 offset:21504\n\t"
    "v_cvt_pk_bf16_f32 v49, v50, v51\n\t"
    "v_mfma_f32_16x16x32_bf16 v[84:87], a[108:111], v[132:135], v[84:87]\n\t"
    "v_exp_f32 v52, v52\n\t"
    "v_mfma_f32_16x16x32_bf16 v[68:71], a[112:115], v[104:107], v[68:71]\n\t"
    "v_exp_f32 v53, v53\n\t"
    "v_mfma_f32_16x16x32_bf16 v[76:79], a[112:115], v[120:123], v[76:79]\n\t"
    "ds_read_b64_tr_b16 a[144:145], v16 offset:17920\n\t"
    "ds_read_b64_tr_b16 a[146:147], v16 offset:22016\n\t"
    "v_mfma_f32_16x16x32_bf16 v[84:87], a[112:115], v[136:139], v[84:87]\n\t"
    "s_waitcnt vmcnt(8)\n\t"
    "s_barrier\n\t"
    "v_mfma_f32_16x16x32_bf16 v[68:71], a[116:119], v[108:111], v[68:71]\n\t"
    "ds_read_b64_tr_b16 a[148:149], v17 offset:17920\n\t"
    "v_cvt_pk_bf16_f32 v50, v52, v53\n\t"
    "v_mfma_f32_16x16x32_bf16 v[76:79], a[116:119], v[124:127], v[76:79]\n\t"
    "ds_read_b64_tr_b16 a[150:151], v17 offset:22016\n\t"
    "ds_read_b128 a[88:91], v10 offset:16384\n\t"
    "v_mfma_f32_16x16x32_bf16 v[84:87], a[116:119], v[140:143], v[84:87]\n\t"
    "s_waitcnt lgkmcnt(6)\n\t"
    "buffer_load_dwordx4 v6, s[44:47], s60 offen offset:0 nt lds\n\t"
    "v_mfma_f32_16x16x32_bf16 v[160:163], a[120:123], v[32:35], 0\n\t"
    "v_exp_f32 v54, v54\n\t"
    "v_mfma_f32_16x16x32_bf16 v[164:167], a[124:127], v[32:35], 0\n\t"
    "v_exp_f32 v55, v55\n\t"
    "v_mfma_f32_16x16x32_bf16 v[168:171], a[128:131], v[32:35], 0\n\t"
    "ds_read_b128 a[92:95], v11 offset:16384\n\t"
    "v_cvt_pk_bf16_f32 v51, v54, v55\n\t"
    "v_mfma_f32_16x16x32_bf16 v[172:175], a[132:135], v[32:35], 0\n\t"
    "v_exp_f32 v56, v56\n\t"
    "v_mfma_f32_16x16x32_bf16 a[0:3], a[136:139], v[32:35], 0\n\t"
    "s_waitcnt lgkmcnt(4)\n\t"
    "v_exp_f32 v57, v57\n\t"
    "v_mfma_f32_16x16x32_bf16 a[4:7], a[140:143], v[32:35], 0\n\t"
    "buffer_load_dwordx4 v7, s[44:47], s60 offen offset:1024 nt lds\n\t"
    "v_mfma_f32_16x16x32_bf16 a[8:11], a[144:147], v[32:35], 0\n\t"
    "s_waitcnt lgkmcnt(1)\n\t"
    "v_exp_f32 v58, v58\n\t"
    "v_mfma_f32_16x16x32_bf16 a[12:15], a[148:151], v[32:35], 0\n\t"
    "v_exp_f32 v59, v59\n\t"
    "v_mfma_f32_16x16x32_bf16 a[72:75], v[216:219], v[32:35], 0\n\t"
    "v_exp_f32 v60, v60\n\t"
    "v_mfma_f32_16x16x32_bf16 v[176:179], a[120:123], v[40:43], 0\n\t"
    "v_exp_f32 v61, v61\n\t"
    "v_mfma_f32_16x16x32_bf16 v[180:183], a[124:127], v[40:43], 0\n\t"
    "v_cvt_pk_bf16_f32 v56, v56, v57\n\t"
    "v_cvt_pk_bf16_f32 v57, v58, v59\n\t"
    "v_mfma_f32_16x16x32_bf16 v[184:187], a[128:131], v[40:43], 0\n\t"
    "buffer_load_dwordx4 v8, s[44:47], s60 offen offset:2048 nt lds\n\t"
    "v_mfma_f32_16x16x32_bf16 v[188:191], a[132:135], v[40:43], 0\n\t"
    "v_exp_f32 v62, v62\n\t"
    "v_mfma_f32_16x16x32_bf16 a[16:19], a[136:139], v[40:43], 0\n\t"
    "v_exp_f32 v63, v63\n\t"
    "v_mfma_f32_16x16x32_bf16 a[20:23], a[140:143], v[40:43], 0\n\t"
    "v_cvt_pk_bf16_f32 v58, v60, v61\n\t"
    "v_cvt_pk_bf16_f32 v59, v62, v63\n\t"
    "v_mfma_f32_16x16x32_bf16 a[24:27], a[144:147], v[40:43], 0\n\t"
    "ds_read_b128 a[96:99], v12 offset:16384\n\t"
    "ds_read_b128 a[100:103], v13 offset:16384\n\t"
    "v_mfma_f32_16x16x32_bf16 a[28:31], a[148:151], v[40:43], 0\n\t"
    "ds_read_b128 a[104:107], v10 offset:20480\n\t"
    "ds_read_b128 a[108:111], v11 offset:20480\n\t"
    "v_mfma_f32_16x16x32_bf16 a[76:79], v[216:219], v[40:43], 0\n\t"
    "buffer_load_dwordx4 v9, s[44:47], s60 offen offset:3072 nt lds\n\t"
    "v_mfma_f32_16x16x32_bf16 v[192:195], a[120:123], v[48:51], 0\n\t"
    "s_add_u32 s61, s54, 32768\n\t"
    "v_exp_f32 v64, v64\n\t"
    "v_mfma_f32_16x16x32_bf16 v[196:199], a[124:127], v[48:51], 0\n\t"
    "s_add_u32 m0, s52, 32768\n\t"
    "v_exp_f32 v65, v65\n\t"
    "v_mfma_f32_16x16x32_bf16 v[200:203], a[128:131], v[48:51], 0\n\t"
    "ds_read_b128 a[112:115], v12 offset:20480\n\t"
    "ds_read_b128 a[116:119], v13 offset:20480\n\t"
    "v_mfma_f32_16x16x32_bf16 v[204:207], a[132:135], v[48:51], 0\n\t"
    "v_exp_f32 v66, v66\n\t"
    "v_mfma_f32_16x16x32_bf16 a[32:35], a[136:139], v[48:51], 0\n\t"
    "v_exp_f32 v67, v67\n\t"
    "v_mfma_f32_16x16x32_bf16 a[36:39], a[140:143], v[48:51], 0\n\t"
    "buffer_load_dwordx4 v2, s[40:43], s61 offen offset:0 nt lds\n\t"
    "v_mfma_f32_16x16x32_bf16 a[40:43], a[144:147], v[48:51], 0\n\t"
    "v_exp_f32 v68, v68\n\t"
    "v_mfma_f32_16x16x32_bf16 a[44:47], a[148:151], v[48:51], 0\n\t"
    "v_exp_f32 v69, v69\n\t"
    "v_mfma_f32_16x16x32_bf16 a[80:83], v[216:219], v[48:51], 0\n\t"
    "v_cvt_pk_bf16_f32 v64, v64, v65\n\t"
    "v_cvt_pk_bf16_f32 v65, v66, v67\n\t"
    "v_mfma_f32_16x16x32_bf16 v[208:211], a[120:123], v[56:59], 0\n\t"
    "v_exp_f32 v70, v70\n\t"
    "v_mfma_f32_16x16x32_bf16 v[212:215], a[124:127], v[56:59], 0\n\t"
    "v_exp_f32 v71, v71\n\t"
    "v_mfma_f32_16x16x32_bf16 a[48:51], a[128:131], v[56:59], 0\n\t"
    "buffer_load_dwordx4 v3, s[40:43], s61 offen offset:1024 nt lds\n\t"
    "v_mfma_f32_16x16x32_bf16 a[52:55], a[132:135], v[56:59], 0\n\t"
    "v_cvt_pk_bf16_f32 v66, v68, v69\n\t"
    "v_cvt_pk_bf16_f32 v67, v70, v71\n\t"
    "v_mfma_f32_16x16x32_bf16 a[56:59], a[136:139], v[56:59], 0\n\t"
    "v_exp_f32 v72, v72\n\t"
    "v_mfma_f32_16x16x32_bf16 a[60:63], a[140:143], v[56:59], 0\n\t"
    "v_exp_f32 v73, v73\n\t"
    "v_mfma_f32_16x16x32_bf16 a[64:67], a[144:147], v[56:59], 0\n\t"
    "v_exp_f32 v74, v74\n\t"
    "v_mfma_f32_16x16x32_bf16 a[68:71], a[148:151], v[56:59], 0\n\t"
    "s_waitcnt vmcnt(10) lgkmcnt(0)\n\t"
    "s_barrier\n\t"
    "v_mfma_f32_16x16x32_bf16 a[84:87], v[216:219], v[56:59], 0\n\t"
    "buffer_load_dwordx4 v4, s[40:43], s61 offen offset:2048 nt lds\n\t"
    "v_mfma_f32_16x16x32_bf16 v[32:35], a[88:91], v[96:99], 0\n\t"
    "ds_read_b64_tr_b16 a[120:121], v14 offset:24576\n\t"
    "v_cvt_pk_bf16_f32 v72, v72, v73\n\t"
    "v_mfma_f32_16x16x32_bf16 v[40:43], a[88:91], v[112:115], 0\n\t"
    "v_exp_f32 v75, v75\n\t"
    "v_mfma_f32_16x16x32_bf16 v[48:51], a[88:91], v[128:131], 0\n\t"
    "ds_read_b64_tr_b16 a[122:123], v14 offset:28672\n\t"
    "v_cvt_pk_bf16_f32 v73, v74, v75\n\t"
    "v_mfma_f32_16x16x32_bf16 v[56:59], a[88:91], v[144:147], 0\n\t"
    "v_exp_f32 v76, v76\n\t"
    "v_mfma_f32_16x16x32_bf16 v[32:35], a[92:95], v[100:103], v[32:35]\n\t"
    "v_exp_f32 v77, v77\n\t"
    "v_mfma_f32_16x16x32_bf16 v[40:43], a[92:95], v[116:119], v[40:43]\n\t"
    "buffer_load_dwordx4 v5, s[40:43], s61 offen offset:3072 nt lds\n\t"
    "v_mfma_f32_16x16x32_bf16 v[48:51], a[92:95], v[132:135], v[48:51]\n\t"
    "s_add_u32 s60, s54, 24576\n\t"
    "ds_read_b64_tr_b16 a[124:125], v15 offset:24576\n\t"
    "ds_read_b64_tr_b16 a[126:127], v15 offset:28672\n\t"
    "v_mfma_f32_16x16x32_bf16 v[56:59], a[92:95], v[148:151], v[56:59]\n\t"
    "s_add_u32 m0, s53, 0\n\t"
    "ds_read_b64_tr_b16 a[128:129], v14 offset:25088\n\t"
    "v_cvt_pk_bf16_f32 v74, v76, v77\n\t"
    "v_mfma_f32_16x16x32_bf16 v[32:35], a[96:99], v[104:107], v[32:35]\n\t"
    "v_exp_f32 v78, v78\n\t"
    "v_mfma_f32_16x16x32_bf16 v[40:43], a[96:99], v[120:123], v[40:43]\n\t"
    "v_exp_f32 v79, v79\n\t"
    "v_mfma_f32_16x16x32_bf16 v[48:51], a[96:99], v[136:139], v[48:51]\n\t"
    "ds_read_b64_tr_b16 a[130:131], v14 offset:29184\n\t"
    "v_cvt_pk_bf16_f32 v75, v78, v79\n\t"
    "v_mfma_f32_16x16x32_bf16 v[56:59], a[96:99], v[152:155], v[56:59]\n\t"
    "buffer_load_dwordx4 v6, s[44:47], s60 offen offset:0 nt lds\n\t"
    "v_mfma_f32_16x16x32_bf16 v[32:35], a[100:103], v[108:111], v[32:35]\n\t"
    "v_exp_f32 v80, v80\n\t"
    "v_mfma_f32_16x16x32_bf16 v[40:43], a[100:103], v[124:127], v[40:43]\n\t"
    "ds_read_b64_tr_b16 a[132:133], v15 offset:25088\n\t"
    "ds_read_b64_tr_b16 a[134:135], v15 offset:29184\n\t"
    "v_mfma_f32_16x16x32_bf16 v[48:51], a[100:103], v[140:143], v[48:51]\n\t"
    "v_exp_f32 v81, v81\n\t"
    "v_mfma_f32_16x16x32_bf16 v[56:59], a[100:103], v[156:159], v[56:59]\n\t"
    "ds_read_b64_tr_b16 a[136:137], v16 offset:25600\n\t"
    "v_cvt_pk_bf16_f32 v80, v80, v81\n\t"
    "v_mfma_f32_16x16x32_bf16 v[36:39], a[104:107], v[96:99], 0\n\t"
    "v_exp_f32 v82, v82\n\t"
    "v_mfma_f32_16x16x32_bf16 v[44:47], a[104:107], v[112:115], 0\n\t"
    "buffer_load_dwordx4 v7, s[44:47], s60 offen offset:1024 nt lds\n\t"
    "v_mfma_f32_16x16x32_bf16 v[52:55], a[104:107], v[128:131], 0\n\t"
    "v_exp_f32 v83, v83\n\t"
    "v_mfma_f32_16x16x32_bf16 v[60:63], a[104:107], v[144:147], 0\n\t"
    "s_waitcnt vmcnt(10)\n\t"
    "s_barrier\n\t"
    "v_mfma_f32_16x16x32_bf16 v[36:39], a[108:111], v[100:103], v[36:39]\n\t"
    "ds_read_b64_tr_b16 a[138:139], v16 offset:29696\n\t"
    "ds_read_b64_tr_b16 a[140:141], v17 offset:25600\n\t"
    "v_mfma_f32_16x16x32_bf16 v[44:47], a[108:111], v[116:119], v[44:47]\n\t"
    "ds_read_b64_tr_b16 a[142:143], v17 offset:29696\n\t"
    "v_cvt_pk_bf16_f32 v81, v82, v83\n\t"
    "v_mfma_f32_16x16x32_bf16 v[52:55], a[108:111], v[132:135], v[52:55]\n\t"
    "v_exp_f32 v84, v84\n\t"
    "v_mfma_f32_16x16x32_bf16 v[60:63], a[108:111], v[148:151], v[60:63]\n\t"
    "buffer_load_dwordx4 v8, s[44:47], s60 offen offset:2048 nt lds\n\t"
    "v_mfma_f32_16x16x32_bf16 v[36:39], a[112:115], v[104:107], v[36:39]\n\t"
    "v_exp_f32 v85, v85\n\t"
    "v_mfma_f32_16x16x32_bf16 v[44:47], a[112:115], v[120:123], v[44:47]\n\t"
    "ds_read_b64_tr_b16 a[144:145], v16 offset:26112\n\t"
    "ds_read_b64_tr_b16 a[146:147], v16 offset:30208\n\t"
    "v_mfma_f32_16x16x32_bf16 v[52:55], a[112:115], v[136:139], v[52:55]\n\t"
    "v_exp_f32 v86, v86\n\t"
    "v_mfma_f32_16x16x32_bf16 v[60:63], a[112:115], v[152:155], v[60:63]\n\t"
    "ds_read_b64_tr_b16 a[148:149], v17 offset:26112\n\t"
    "v_cvt_pk_bf16_f32 v82, v84, v85\n\t"
    "v_mfma_f32_16x16x32_bf16 v[36:39], a[116:119], v[108:111], v[36:39]\n\t"
    "v_exp_f32 v87, v87\n\t"
    "v_mfma_f32_16x16x32_bf16 v[44:47], a[116:119], v[124:127], v[44:47]\n\t"
    "s_waitcnt lgkmcnt(3)\n\t"
    "buffer_load_dwordx4 v9, s[44:47], s60 offen offset:3072 nt lds\n\t"
    "v_mfma_f32_16x16x32_bf16 v[52:55], a[116:119], v[140:143], v[52:55]\n\t"
    "s_add_u32 s61, s54, 40960\n\t"
    "ds_read_b64_tr_b16 a[150:151], v17 offset:30208\n\t"
    "v_cvt_pk_bf16_f32 v83, v86, v87\n\t"
    "v_mfma_f32_16x16x32_bf16 v[60:63], a[116:119], v[156:159], v[60:63]\n\t"
    "s_add_u32 m0, s52, 0\n\t"
    "ds_read_b128 a[88:91], v10 offset:24576\n\t"
    "ds_read_b128 a[92:95], v11 offset:24576\n\t"
    "v_mfma_f32_16x16x32_bf16 v[160:163], a[120:123], v[64:67], v[160:163]\n\t"
    "v_exp_f32 v32, v32\n\t"
    "v_mfma_f32_16x16x32_bf16 v[164:167], a[124:127], v[64:67], v[164:167]\n\t"
    "v_exp_f32 v33, v33\n\t"
    "v_mfma_f32_16x16x32_bf16 v[168:171], a[128:131], v[64:67], v[168:171]\n\t"
    "ds_read_b128 a[96:99], v12 offset:24576\n\t"
    "v_cvt_pk_bf16_f32 v32, v32, v33\n\t"
    "v_mfma_f32_16x16x32_bf16 v[172:175], a[132:135], v[64:67], v[172:175]\n\t"
    "buffer_load_dwordx4 v2, s[40:43], s61 offen offset:0 nt lds\n\t"
    "v_mfma_f32_16x16x32_bf16 a[0:3], a[136:139], v[64:67], a[0:3]\n\t"
    "v_exp_f32 v34, v34\n\t"
    "v_mfma_f32_16x16x32_bf16 a[4:7], a[140:143], v[64:67], a[4:7]\n\t"
    "s_waitcnt lgkmcnt(4)\n\t"
    "v_exp_f32 v35, v35\n\t"
    "v_mfma_f32_16x16x32_bf16 a[8:11], a[144:147], v[64:67], a[8:11]\n\t"
    "s_waitcnt lgkmcnt(3)\n\t"
    "ds_read_b128 a[100:103], v13 offset:24576\n\t"
    "ds_read_b128 a[104:107], v10 offset:28672\n\t"
    "v_mfma_f32_16x16x32_bf16 a[12:15], a[148:151], v[64:67], a[12:15]\n\t"
    "v_exp_f32 v36, v36\n\t"
    "v_mfma_f32_16x16x32_bf16 a[72:75], v[216:219], v[64:67], a[72:75]\n\t"
    "ds_read_b128 a[108:111], v11 offset:28672\n\t"
    "v_cvt_pk_bf16_f32 v33, v34, v35\n\t"
    "v_mfma_f32_16x16x32_bf16 v[176:179], a[120:123], v[72:75], v[176:179]\n\t"
    "buffer_load_dwordx4 v3, s[40:43], s61 offen offset:1024 nt lds\n\t"
    "v_mfma_f32_16x16x32_bf16 v[180:183], a[124:127], v[72:75], v[180:183]\n\t"
    "v_exp_f32 v37, v37\n\t"
    "v_mfma_f32_16x16x32_bf16 v[184:187], a[128:131], v[72:75], v[184:187]\n\t"
    "ds_read_b128 a[112:115], v12 offset:28672\n\t"
    "v_cvt_pk_bf16_f32 v34, v36, v37\n\t"
    "v_mfma_f32_16x16x32_bf16 v[188:191], a[132:135], v[72:75], v[188:191]\n\t"
    "v_exp_f32 v38, v38\n\t"
    "v_mfma_f32_16x16x32_bf16 a[16:19], a[136:139], v[72:75], a[16:19]\n\t"
    "v_exp_f32 v39, v39\n\t"
    "v_mfma_f32_16x16x32_bf16 a[20:23], a[140:143], v[72:75], a[20:23]\n\t"
    "ds_read_b128 a[116:119], v13 offset:28672\n\t"
    "v_cvt_pk_bf16_f32 v35, v38, v39\n\t"
    "v_mfma_f32_16x16x32_bf16 a[24:27], a[144:147], v[72:75], a[24:27]\n\t"
    "buffer_load_dwordx4 v4, s[40:43], s61 offen offset:2048 nt lds\n\t"
    "v_mfma_f32_16x16x32_bf16 a[28:31], a[148:151], v[72:75], a[28:31]\n\t"
    "v_exp_f32 v40, v40\n\t"
    "v_mfma_f32_16x16x32_bf16 a[76:79], v[216:219], v[72:75], a[76:79]\n\t"
    "v_exp_f32 v41, v41\n\t"
    "v_mfma_f32_16x16x32_bf16 v[192:195], a[120:123], v[80:83], v[192:195]\n\t"
    "v_exp_f32 v42, v42\n\t"
    "v_mfma_f32_16x16x32_bf16 v[196:199], a[124:127], v[80:83], v[196:199]\n\t"
    "v_exp_f32 v43, v43\n\t"
    "v_mfma_f32_16x16x32_bf16 v[200:203], a[128:131], v[80:83], v[200:203]\n\t"
    "v_exp_f32 v44, v44\n\t"
    "v_mfma_f32_16x16x32_bf16 v[204:207], a[132:135], v[80:83], v[204:207]\n\t"
    "buffer_load_dwordx4 v5, s[40:43], s61 offen offset:3072 nt lds\n\t"
    "v_mfma_f32_16x16x32_bf16 a[32:35], a[136:139], v[80:83], a[32:35]\n\t"
    "s_add_u32 s60, s54, 32768\n\t"
    "v_exp_f32 v45, v45\n\t"
    "v_mfma_f32_16x16x32_bf16 a[36:39], a[140:143], v[80:83], a[36:39]\n\t"
    "s_add_u32 m0, s53, 8192\n\t"
    "v_cvt_pk_bf16_f32 v40, v40, v41\n\t"
    "v_cvt_pk_bf16_f32 v41, v42, v43\n\t"
    "v_mfma_f32_16x16x32_bf16 a[40:43], a[144:147], v[80:83], a[40:43]\n\t"
    "v_exp_f32 v46, v46\n\t"
    "v_mfma_f32_16x16x32_bf16 a[44:47], a[148:151], v[80:83], a[44:47]\n\t"
    "v_exp_f32 v47, v47\n\t"
    "v_mfma_f32_16x16x32_bf16 a[80:83], v[216:219], v[80:83], a[80:83]\n\t"
    "s_waitcnt vmcnt(12) lgkmcnt(0)\n\t"
    "s_barrier\n\t"
    "v_mfma_f32_16x16x32_bf16 v[64:67], a[88:91], v[96:99], 0\n\t"
    "buffer_load_dwordx4 v6, s[44:47], s60 offen offset:0 nt lds\n\t"
    "v_mfma_f32_16x16x32_bf16 v[72:75], a[88:91], v[112:115], 0\n\t"
    "ds_read_b64_tr_b16 a[120:121], v14 offset:32768\n\t"
    "ds_read_b64_tr_b16 a[122:123], v14 offset:36864\n\t"
    "v_mfma_f32_16x16x32_bf16 v[80:83], a[88:91], v[128:131], 0\n\t"
    "v_cvt_pk_bf16_f32 v42, v44, v45\n\t"
    "v_cvt_pk_bf16_f32 v43, v46, v47\n\t"
    "v_mfma_f32_16x16x32_bf16 v[64:67], a[92:95], v[100:103], v[64:67]\n\t"
    "v_exp_f32 v48, v48\n\t"
    "v_mfma_f32_16x16x32_bf16 v[72:75], a[92:95], v[116:119], v[72:75]\n\t"
    "ds_read_b64_tr_b16 a[124:125], v15 offset:32768\n\t"
    "ds_read_b64_tr_b16 a[126:127], v15 offset:36864\n\t"
    "v_mfma_f32_16x16x32_bf16 v[80:83], a[92:95], v[132:135], v[80:83]\n\t"
    "v_exp_f32 v49, v49\n\t"
    "v_mfma_f32_16x16x32_bf16 v[64:67], a[96:99], v[104:107], v[64:67]\n\t"
    "buffer_load_dwordx4 v7, s[44:47], s60 offen offset:1024 nt lds\n\t"
    "v_mfma_f32_16x16x32_bf16 v[72:75], a[96:99], v[120:123], v[72:75]\n\t"
    "ds_read_b64_tr_b16 a[128:129], v14 offset:33280\n\t"
    "ds_read_b64_tr_b16 a[130:131], v14 offset:37376\n\t"
    "v_mfma_f32_16x16x32_bf16 v[80:83], a[96:99], v[136:139], v[80:83]\n\t"
    "v_exp_f32 v50, v50\n\t"
    "v_mfma_f32_16x16x32_bf16 v[64:67], a[100:103], v[108:111], v[64:67]\n\t"
    "ds_read_b64_tr_b16 a[132:133], v15 offset:33280\n\t"
    "v_cvt_pk_bf16_f32 v48, v48, v49\n\t"
    "v_mfma_f32_16x16x32_bf16 v[72:75], a[100:103], v[124:127], v[72:75]\n\t"
    "v_exp_f32 v51, v51\n\t"
    "v_mfma_f32_16x16x32_bf16 v[80:83], a[100:103], v[140:143], v[80:83]\n\t"
    "ds_read_b64_tr_b16 a[134:135], v15 offset:37376\n\t"
    "v_cvt_pk_bf16_f32 v49, v50, v51\n\t"
    "v_mfma_f32_16x16x32_bf16 v[68:71], a[104:107], v[96:99], 0\n\t"
    "buffer_load_dwordx4 v8, s[44:47], s60 offen offset:2048 nt lds\n\t"
    "v_mfma_f32_16x16x32_bf16 v[76:79], a[104:107], v[112:115], 0\n\t"
    "ds_read_b64_tr_b16 a[136:137], v16 offset:33792\n\t"
    "ds_read_b64_tr_b16 a[138:139], v16 offset:37888\n\t"
    "v_mfma_f32_16x16x32_bf16 v[84:87], a[104:107], v[128:131], 0\n\t"
    "v_exp_f32 v52, v52\n\t"
    "v_mfma_f32_16x16x32_bf16 v[68:71], a[108:111], v[100:103], v[68:71]\n\t"
    "v_exp_f32 v53, v53\n\t"
    "v_mfma_f32_16x16x32_bf16 v[76:79], a[108:111], v[116:119], v[76:79]\n\t"
    "ds_read_b64_tr_b16 a[140:141], v17 offset:33792\n\t"
    "ds_read_b64_tr_b16 a[142:143], v17 offset:37888\n\t"
    "v_mfma_f32_16x16x32_bf16 v[84:87], a[108:111], v[132:135], v[84:87]\n\t"
    "v_exp_f32 v54, v54\n\t"
    "v_mfma_f32_16x16x32_bf16 v[68:71], a[112:115], v[104:107], v[68:71]\n\t"
    "buffer_load_dwordx4 v9, s[44:47], s60 offen offset:3072 nt lds\n\t"
    "v_mfma_f32_16x16x32_bf16 v[76:79], a[112:115], v[120:123], v[76:79]\n\t"
    "s_add_u32 s61, s54, 49152\n\t"
    "ds_read_b64_tr_b16 a[144:145], v16 offset:34304\n\t"
    "ds_read_b64_tr_b16 a[146:147], v16 offset:38400\n\t"
    "v_mfma_f32_16x16x32_bf16 v[84:87], a[112:115], v[136:139], v[84:87]\n\t"
    "s_waitcnt vmcnt(12)\n\t"
    "s_barrier\n\t"
    "v_mfma_f32_16x16x32_bf16 v[68:71], a[116:119], v[108:111], v[68:71]\n\t"
    "s_add_u32 m0, s52, 8192\n\t"
    "ds_read_b64_tr_b16 a[148:149], v17 offset:34304\n\t"
    "v_cvt_pk_bf16_f32 v50, v52, v53\n\t"
    "v_mfma_f32_16x16x32_bf16 v[76:79], a[116:119], v[124:127], v[76:79]\n\t"
    "ds_read_b64_tr_b16 a[150:151], v17 offset:38400\n\t"
    "ds_read_b128 a[88:91], v10 offset:32768\n\t"
    "v_mfma_f32_16x16x32_bf16 v[84:87], a[116:119], v[140:143], v[84:87]\n\t"
    "s_waitcnt lgkmcnt(7)\n\t"
    "v_exp_f32 v55, v55\n\t"
    "v_mfma_f32_16x16x32_bf16 v[160:163], a[120:123], v[32:35], v[160:163]\n\t"
    "buffer_load_dwordx4 v2, s[40:43], s61 offen offset:0 nt lds\n\t"
    "v_mfma_f32_16x16x32_bf16 v[164:167], a[124:127], v[32:35], v[164:167]\n\t"
    "ds_read_b128 a[92:95], v11 offset:32768\n\t"
    "v_cvt_pk_bf16_f32 v51, v54, v55\n\t"
    "v_mfma_f32_16x16x32_bf16 v[168:171], a[128:131], v[32:35], v[168:171]\n\t"
    "v_exp_f32 v56, v56\n\t"
    "v_mfma_f32_16x16x32_bf16 v[172:175], a[132:135], v[32:35], v[172:175]\n\t"
    "v_exp_f32 v57, v57\n\t"
    "v_mfma_f32_16x16x32_bf16 a[0:3], a[136:139], v[32:35], a[0:3]\n\t"
    "s_waitcnt lgkmcnt(4)\n\t"
    "v_exp_f32 v58, v58\n\t"
    "v_mfma_f32_16x16x32_bf16 a[4:7], a[140:143], v[32:35], a[4:7]\n\t"
    "v_exp_f32 v59, v59\n\t"
    "v_mfma_f32_16x16x32_bf16 a[8:11], a[144:147], v[32:35], a[8:11]\n\t"
    "s_waitcnt lgkmcnt(1)\n\t"
    "buffer_load_dwordx4 v3, s[40:43], s61 offen offset:1024 nt lds\n\t"
    "v_mfma_f32_16x16x32_bf16 a[12:15], a[148:151], v[32:35], a[12:15]\n\t"
    "v_exp_f32 v60, v60\n\t"
    "v_mfma_f32_16x16x32_bf16 a[72:75], v[216:219], v[32:35], a[72:75]\n\t"
    "v_exp_f32 v61, v61\n\t"
    "v_mfma_f32_16x16x32_bf16 v[176:179], a[120:123], v[40:43], v[176:179]\n\t"
    "v_cvt_pk_bf16_f32 v56, v56, v57\n\t"
    "v_cvt_pk_bf16_f32 v57, v58, v59\n\t"
    "v_mfma_f32_16x16x32_bf16 v[180:183], a[124:127], v[40:43], v[180:183]\n\t"
    "v_exp_f32 v62, v62\n\t"
    "v_mfma_f32_16x16x32_bf16 v[184:187], a[128:131], v[40:43], v[184:187]\n\t"
    "v_exp_f32 v63, v63\n\t"
    "v_mfma_f32_16x16x32_bf16 v[188:191], a[132:135], v[40:43], v[188:191]\n\t"
    "buffer_load_dwordx4 v4, s[40:43], s61 offen offset:2048 nt lds\n\t"
    "v_mfma_f32_16x16x32_bf16 a[16:19], a[136:139], v[40:43], a[16:19]\n\t"
    "v_cvt_pk_bf16_f32 v58, v60, v61\n\t"
    "v_cvt_pk_bf16_f32 v59, v62, v63\n\t"
    "v_mfma_f32_16x16x32_bf16 a[20:23], a[140:143], v[40:43], a[20:23]\n\t"
    "ds_read_b128 a[96:99], v12 offset:32768\n\t"
    "ds_read_b128 a[100:103], v13 offset:32768\n\t"
    "v_mfma_f32_16x16x32_bf16 a[24:27], a[144:147], v[40:43], a[24:27]\n\t"
    "ds_read_b128 a[104:107], v10 offset:36864\n\t"
    "ds_read_b128 a[108:111], v11 offset:36864\n\t"
    "v_mfma_f32_16x16x32_bf16 a[28:31], a[148:151], v[40:43], a[28:31]\n\t"
    "v_exp_f32 v64, v64\n\t"
    "v_mfma_f32_16x16x32_bf16 a[76:79], v[216:219], v[40:43], a[76:79]\n\t"
    "v_exp_f32 v65, v65\n\t"
    "v_mfma_f32_16x16x32_bf16 v[192:195], a[120:123], v[48:51], v[192:195]\n\t"
    "buffer_load_dwordx4 v5, s[40:43], s61 offen offset:3072 nt lds\n\t"
    "v_mfma_f32_16x16x32_bf16 v[196:199], a[124:127], v[48:51], v[196:199]\n\t"
    "s_add_u32 s60, s54, 40960\n\t"
    "ds_read_b128 a[112:115], v12 offset:36864\n\t"
    "v_cvt_pk_bf16_f32 v64, v64, v65\n\t"
    "v_mfma_f32_16x16x32_bf16 v[200:203], a[128:131], v[48:51], v[200:203]\n\t"
    "s_add_u32 m0, s53, 16384\n\t"
    "v_exp_f32 v66, v66\n\t"
    "v_mfma_f32_16x16x32_bf16 v[204:207], a[132:135], v[48:51], v[204:207]\n\t"
    "v_exp_f32 v67, v67\n\t"
    "v_mfma_f32_16x16x32_bf16 a[32:35], a[136:139], v[48:51], a[32:35]\n\t"
    "ds_read_b128 a[116:119], v13 offset:36864\n\t"
    "v_cvt_pk_bf16_f32 v65, v66, v67\n\t"
    "v_mfma_f32_16x16x32_bf16 a[36:39], a[140:143], v[48:51], a[36:39]\n\t"
    "v_exp_f32 v68, v68\n\t"
    "v_mfma_f32_16x16x32_bf16 a[40:43], a[144:147], v[48:51], a[40:43]\n\t"
    "buffer_load_dwordx4 v6, s[44:47], s60 offen offset:0 nt lds\n\t"
    "v_mfma_f32_16x16x32_bf16 a[44:47], a[148:151], v[48:51], a[44:47]\n\t"
    "v_exp_f32 v69, v69\n\t"
    "v_mfma_f32_16x16x32_bf16 a[80:83], v[216:219], v[48:51], a[80:83]\n\t"
    "v_exp_f32 v70, v70\n\t"
    "v_mfma_f32_16x16x32_bf16 v[208:211], a[120:123], v[56:59], v[208:211]\n\t"
    "v_exp_f32 v71, v71\n\t"
    "v_mfma_f32_16x16x32_bf16 v[212:215], a[124:127], v[56:59], v[212:215]\n\t"
    "v_cvt_pk_bf16_f32 v66, v68, v69\n\t"
    "v_cvt_pk_bf16_f32 v67, v70, v71\n\t"
    "v_mfma_f32_16x16x32_bf16 a[48:51], a[128:131], v[56:59], a[48:51]\n\t"
    "v_exp_f32 v72, v72\n\t"
    "v_mfma_f32_16x16x32_bf16 a[52:55], a[132:135], v[56:59], a[52:55]\n\t"
    "buffer_load_dwordx4 v7, s[44:47], s60 offen offset:1024 nt lds\n\t"
    "v_mfma_f32_16x16x32_bf16 a[56:59], a[136:139], v[56:59], a[56:59]\n\t"
    "v_exp_f32 v73, v73\n\t"
    "v_mfma_f32_16x16x32_bf16 a[60:63], a[140:143], v[56:59], a[60:63]\n\t"
    "v_exp_f32 v74, v74\n\t"
    "v_mfma_f32_16x16x32_bf16 a[64:67], a[144:147], v[56:59], a[64:67]\n\t"
    "v_exp_f32 v75, v75\n\t"
    "v_mfma_f32_16x16x32_bf16 a[68:71], a[148:151], v[56:59], a[68:71]\n\t"
    "s_waitcnt vmcnt(10) lgkmcnt(0)\n\t"
    "s_barrier\n\t"
    "v_mfma_f32_16x16x32_bf16 a[84:87], v[216:219], v[56:59], a[84:87]\n\t"
    "ds_read_b64_tr_b16 a[120:121], v14 offset:0\n\t"
    "v_cvt_pk_bf16_f32 v72, v72, v73\n\t"
    "v_mfma_f32_16x16x32_bf16 v[32:35], a[88:91], v[96:99], 0\n\t"
    "buffer_load_dwordx4 v8, s[44:47], s60 offen offset:2048 nt lds\n\t"
    "v_mfma_f32_16x16x32_bf16 v[40:43], a[88:91], v[112:115], 0\n\t"
    "ds_read_b64_tr_b16 a[122:123], v14 offset:4096\n\t"
    "v_cvt_pk_bf16_f32 v73, v74, v75\n\t"
    "v_mfma_f32_16x16x32_bf16 v[48:51], a[88:91], v[128:131], 0\n\t"
    "v_exp_f32 v76, v76\n\t"
    "v_mfma_f32_16x16x32_bf16 v[56:59], a[88:91], v[144:147], 0\n\t"
    "v_exp_f32 v77, v77\n\t"
    "v_mfma_f32_16x16x32_bf16 v[32:35], a[92:95], v[100:103], v[32:35]\n\t"
    "ds_read_b64_tr_b16 a[124:125], v15 offset:0\n\t"
    "v_cvt_pk_bf16_f32 v74, v76, v77\n\t"
    "v_mfma_f32_16x16x32_bf16 v[40:43], a[92:95], v[116:119], v[40:43]\n\t"
    "v_exp_f32 v78, v78\n\t"
    "v_mfma_f32_16x16x32_bf16 v[48:51], a[92:95], v[132:135], v[48:51]\n\t"
    "buffer_load_dwordx4 v9, s[44:47], s60 offen offset:3072 nt lds\n\t"
    "v_mfma_f32_16x16x32_bf16 v[56:59], a[92:95], v[148:151], v[56:59]\n\t"
    "s_add_u32 s61, s54, 57344\n\t"
    "ds_read_b64_tr_b16 a[126:127], v15 offset:4096\n\t"
    "ds_read_b64_tr_b16 a[128:129], v14 offset:512\n\t"
    "v_mfma_f32_16x16x32_bf16 v[32:35], a[96:99], v[104:107], v[32:35]\n\t"
    "s_add_u32 m0, s52, 16384\n\t"
    "v_exp_f32 v79, v79\n\t"
    "v_mfma_f32_16x16x32_bf16 v[40:43], a[96:99], v[120:123], v[40:43]\n\t"
    "ds_read_b64_tr_b16 a[130:131], v14 offset:4608\n\t"
    "v_cvt_pk_bf16_f32 v75, v78, v79\n\t"
    "v_mfma_f32_16x16x32_bf16 v[48:51], a[96:99], v[136:139], v[48:51]\n\t"
    "v_exp_f32 v80, v80\n\t"
    "v_mfma_f32_16x16x32_bf16 v[56:59], a[96:99], v[152:155], v[56:59]\n\t"
    "v_exp_f32 v81, v81\n\t"
    "v_mfma_f32_16x16x32_bf16 v[32:35], a[100:103], v[108:111], v[32:35]\n\t"
    "buffer_load_dwordx4 v2, s[40:43], s61 offen offset:0 nt lds\n\t"
    "v_mfma_f32_16x16x32_bf16 v[40:43], a[100:103], v[124:127], v[40:43]\n\t"
    "ds_read_b64_tr_b16 a[132:133], v15 offset:512\n\t"
    "ds_read_b64_tr_b16 a[134:135], v15 offset:4608\n\t"
    "v_mfma_f32_16x16x32_bf16 v[48:51], a[100:103], v[140:143], v[48:51]\n\t"
    "v_exp_f32 v82, v82\n\t"
    "v_mfma_f32_16x16x32_bf16 v[56:59], a[100:103], v[156:159], v[56:59]\n\t"
    "ds_read_b64_tr_b16 a[136:137], v16 offset:1024\n\t"
    "v_cvt_pk_bf16_f32 v80, v80, v81\n\t"
    "v_mfma_f32_16x16x32_bf16 v[36:39], a[104:107], v[96:99], 0\n\t"
    "v_exp_f32 v83, v83\n\t"
    "v_mfma_f32_16x16x32_bf16 v[44:47], a[104:107], v[112:115], 0\n\t"
    "ds_read_b64_tr_b16 a[138:139], v16 offset:5120\n\t"
    "v_cvt_pk_bf16_f32 v81, v82, v83\n\t"
    "v_mfma_f32_16x16x32_bf16 v[52:55], a[104:107], v[128:131], 0\n\t"
    "buffer_load_dwordx4 v3, s[40:43], s61 offen offset:1024 nt lds\n\t"
    "v_mfma_f32_16x16x32_bf16 v[60:63], a[104:107], v[144:147], 0\n\t"
    "v_exp_f32 v84, v84\n\t"
    "v_mfma_f32_16x16x32_bf16 v[36:39], a[108:111], v[100:103], v[36:39]\n\t"
    "v_exp_f32 v85, v85\n\t"
    "v_mfma_f32_16x16x32_bf16 v[44:47], a[108:111], v[116:119], v[44:47]\n\t"
    "ds_read_b64_tr_b16 a[140:141], v17 offset:1024\n\t"
    "ds_read_b64_tr_b16 a[142:143], v17 offset:5120\n\t"
    "v_mfma_f32_16x16x32_bf16 v[52:55], a[108:111], v[132:135], v[52:55]\n\t"
    "v_exp_f32 v86, v86\n\t"
    "v_mfma_f32_16x16x32_bf16 v[60:63], a[108:111], v[148:151], v[60:63]\n\t"
    "ds_read_b64_tr_b16 a[144:145], v16 offset:1536\n\t"
    "v_cvt_pk_bf16_f32 v82, v84, v85\n\t"
    "v_mfma_f32_16x16x32_bf16 v[36:39], a[112:115], v[104:107], v[36:39]\n\t"
    "buffer_load_dwordx4 v4, s[40:43], s61 offen offset:2048 nt lds\n\t"
    "v_mfma_f32_16x16x32_bf16 v[44:47], a[112:115], v[120:123], v[44:47]\n\t"
    "v_exp_f32 v87, v87\n\t"
    "v_mfma_f32_16x16x32_bf16 v[52:55], a[112:115], v[136:139], v[52:55]\n\t"
    "ds_read_b64_tr_b16 a[146:147], v16 offset:5632\n\t"
    "v_cvt_pk_bf16_f32 v83, v86, v87\n\t"
    "v_mfma_f32_16x16x32_bf16 v[60:63], a[112:115], v[152:155], v[60:63]\n\t"
    "v_exp_f32 v32, v32\n\t"
    "v_mfma_f32_16x16x32_bf16 v[36:39], a[116:119], v[108:111], v[36:39]\n\t"
    "ds_read_b64_tr_b16 a[148:149], v17 offset:1536\n\t"
    "ds_read_b128 a[88:91], v10 offset:0\n\t"
    "v_mfma_f32_16x16x32_bf16 v[44:47], a[116:119], v[124:127], v[44:47]\n\t"
    "v_exp_f32 v33, v33\n\t"
    "v_mfma_f32_16x16x32_bf16 v[52:55], a[116:119], v[140:143], v[52:55]\n\t"
    "s_waitcnt lgkmcnt(4)\n\t"
    "buffer_load_dwordx4 v5, s[40:43], s61 offen offset:3072 nt lds\n\t"
    "v_mfma_f32_16x16x32_bf16 v[60:63], a[116:119], v[156:159], v[60:63]\n\t"
    "s_add_u32 s60, s54, 49152\n\t"
    "ds_read_b64_tr_b16 a[150:151], v17 offset:5632\n\t"
    "ds_read_b128 a[92:95], v11 offset:0\n\t"
    "v_mfma_f32_16x16x32_bf16 v[160:163], a[120:123], v[64:67], v[160:163]\n\t"
    "s_add_u32 m0, s53, 24576\n\t"
    "v_exp_f32 v34, v34\n\t"
    "v_mfma_f32_16x16x32_bf16 v[164:167], a[124:127], v[64:67], v[164:167]\n\t"
    "ds_read_b128 a[96:99], v12 offset:0\n\t"
    "v_cvt_pk_bf16_f32 v32, v32, v33\n\t"
    "v_mfma_f32_16x16x32_bf16 v[168:171], a[128:131], v[64:67], v[168:171]\n\t"
    "v_exp_f32 v35, v35\n\t"
    "v_mfma_f32_16x16x32_bf16 v[172:175], a[132:135], v[64:67], v[172:175]\n\t"
    "ds_read_b128 a[100:103], v13 offset:0\n\t"
    "v_cvt_pk_bf16_f32 v33, v34, v35\n\t"
    "v_mfma_f32_16x16x32_bf16 a[0:3], a[136:139], v[64:67], a[0:3]\n\t"
    "v_exp_f32 v36, v36\n\t"
    "v_mfma_f32_16x16x32_bf16 a[4:7], a[140:143], v[64:67], a[4:7]\n\t"
    "s_waitcnt lgkmcnt(4)\n\t"
    "v_exp_f32 v37, v37\n\t"
    "v_mfma_f32_16x16x32_bf16 a[8:11], a[144:147], v[64:67], a[8:11]\n\t"
    "s_waitcnt lgkmcnt(3)\n\t"
    "ds_read_b128 a[104:107], v10 offset:4096\n\t"
    "v_cvt_pk_bf16_f32 v34, v36, v37\n\t"
    "v_mfma_f32_16x16x32_bf16 a[12:15], a[148:151], v[64:67], a[12:15]\n\t"
    "v_exp_f32 v38, v38\n\t"
    "v_mfma_f32_16x16x32_bf16 a[72:75], v[216:219], v[64:67], a[72:75]\n\t"
    "buffer_load_dwordx4 v6, s[44:47], s60 offen offset:0 nt lds\n\t"
    "v_mfma_f32_16x16x32_bf16 v[176:179], a[120:123], v[72:75], v[176:179]\n\t"
    "v_exp_f32 v39, v39\n\t"
    "v_mfma_f32_16x16x32_bf16 v[180:183], a[124:127], v[72:75], v[180:183]\n\t"
    "ds_read_b128 a[108:111], v11 offset:4096\n\t"
    "v_cvt_pk_bf16_f32 v35, v38, v39\n\t"
    "v_mfma_f32_16x16x32_bf16 v[184:187], a[128:131], v[72:75], v[184:187]\n\t"
    "v_exp_f32 v40, v40\n\t"
    "v_mfma_f32_16x16x32_bf16 v[188:191], a[132:135], v[72:75], v[188:191]\n\t"
    "v_exp_f32 v41, v41\n\t"
    "v_mfma_f32_16x16x32_bf16 a[16:19], a[136:139], v[72:75], a[16:19]\n\t"
    "ds_read_b128 a[112:115], v12 offset:4096\n\t"
    "ds_read_b128 a[116:119], v13 offset:4096\n\t"
    "v_mfma_f32_16x16x32_bf16 a[20:23], a[140:143], v[72:75], a[20:23]\n\t"
    "buffer_load_dwordx4 v7, s[44:47], s60 offen offset:1024 nt lds\n\t"
    "v_mfma_f32_16x16x32_bf16 a[24:27], a[144:147], v[72:75], a[24:27]\n\t"
    "v_exp_f32 v42, v42\n\t"
    "v_mfma_f32_16x16x32_bf16 a[28:31], a[148:151], v[72:75], a[28:31]\n\t"
    "v_exp_f32 v43, v43\n\t"
    "v_mfma_f32_16x16x32_bf16 a[76:79], v[216:219], v[72:75], a[76:79]\n\t"
    "v_exp_f32 v44, v44\n\t"
    "v_mfma_f32_16x16x32_bf16 v[192:195], a[120:123], v[80:83], v[192:195]\n\t"
    "v_exp_f32 v45, v45\n\t"
    "v_mfma_f32_16x16x32_bf16 v[196:199], a[124:127], v[80:83], v[196:199]\n\t"
    "v_cvt_pk_bf16_f32 v40, v40, v41\n\t"
    "v_cvt_pk_bf16_f32 v41, v42, v43\n\t"
    "v_mfma_f32_16x16x32_bf16 v[200:203], a[128:131], v[80:83], v[200:203]\n\t"
    "buffer_load_dwordx4 v8, s[44:47], s60 offen offset:2048 nt lds\n\t"
    "v_mfma_f32_16x16x32_bf16 v[204:207], a[132:135], v[80:83], v[204:207]\n\t"
    "v_exp_f32 v46, v46\n\t"
    "v_mfma_f32_16x16x32_bf16 a[32:35], a[136:139], v[80:83], a[32:35]\n\t"
    "v_exp_f32 v47, v47\n\t"
    "v_mfma_f32_16x16x32_bf16 a[36:39], a[140:143], v[80:83], a[36:39]\n\t"
    "v_cvt_pk_bf16_f32 v42, v44, v45\n\t"
    "v_cvt_pk_bf16_f32 v43, v46, v47\n\t"
    "v_mfma_f32_16x16x32_bf16 a[40:43], a[144:147], v[80:83], a[40:43]\n\t"
    "v_exp_f32 v48, v48\n\t"
    "v_mfma_f32_16x16x32_bf16 a[44:47], a[148:151], v[80:83], a[44:47]\n\t"
    "v_exp_f32 v49, v49\n\t"
    "v_mfma_f32_16x16x32_bf16 a[80:83], v[216:219], v[80:83], a[80:83]\n\t"
    "s_waitcnt vmcnt(11) lgkmcnt(0)\n\t"
    "s_barrier\n\t"
    "v_mfma_f32_16x16x32_bf16 v[64:67], a[88:91], v[96:99], 0\n\t"
    "buffer_load_dwordx4 v9, s[44:47], s60 offen offset:3072 nt lds\n\t"
    "v_mfma_f32_16x16x32_bf16 v[72:75], a[88:91], v[112:115], 0\n\t"
    "s_add_u32 s61, s54, 65536\n\t"
    "ds_read_b64_tr_b16 a[120:121], v14 offset:8192\n\t"
    "ds_read_b64_tr_b16 a[122:123], v14 offset:12288\n\t"
    "v_mfma_f32_16x16x32_bf16 v[80:83], a[88:91], v[128:131], 0\n\t"
    "s_add_u32 m0, s52, 24576\n\t"
    "v_exp_f32 v50, v50\n\t"
    "v_mfma_f32_16x16x32_bf16 v[64:67], a[92:95], v[100:103], v[64:67]\n\t"
    "ds_read_b64_tr_b16 a[124:125], v15 offset:8192\n\t"
    "v_cvt_pk_bf16_f32 v48, v48, v49\n\t"
    "v_mfma_f32_16x16x32_bf16 v[72:75], a[92:95], v[116:119], v[72:75]\n\t"
    "v_exp_f32 v51, v51\n\t"
    "v_mfma_f32_16x16x32_bf16 v[80:83], a[92:95], v[132:135], v[80:83]\n\t"
    "ds_read_b64_tr_b16 a[126:127], v15 offset:12288\n\t"
    "v_cvt_pk_bf16_f32 v49, v50, v51\n\t"
    "v_mfma_f32_16x16x32_bf16 v[64:67], a[96:99], v[104:107], v[64:67]\n\t"
    "buffer_load_dwordx4 v2, s[40:43], s61 offen offset:0 nt lds\n\t"
    "v_mfma_f32_16x16x32_bf16 v[72:75], a[96:99], v[120:123], v[72:75]\n\t"
    "ds_read_b64_tr_b16 a[128:129], v14 offset:8704\n\t"
    "ds_read_b64_tr_b16 a[130:131], v14 offset:12800\n\t"
    "v_mfma_f32_16x16x32_bf16 v[80:83], a[96:99], v[136:139], v[80:83]\n\t"
    "v_exp_f32 v52, v52\n\t"
    "v_mfma_f32_16x16x32_bf16 v[64:67], a[100:103], v[108:111], v[64:67]\n\t"
    "v_exp_f32 v53, v53\n\t"
    "v_mfma_f32_16x16x32_bf16 v[72:75], a[100:103], v[124:127], v[72:75]\n\t"
    "ds_read_b64_tr_b16 a[132:133], v15 offset:8704\n\t"
    "ds_read_b64_tr_b16 a[134:135], v15 offset:12800\n\t"
    "v_mfma_f32_16x16x32_bf16 v[80:83], a[100:103], v[140:143], v[80:83]\n\t"
    "v_exp_f32 v54, v54\n\t"
    "v_mfma_f32_16x16x32_bf16 v[68:71], a[104:107], v[96:99], 0\n\t"
    "ds_read_b64_tr_b16 a[136:137], v16 offset:9216\n\t"
    "v_cvt_pk_bf16_f32 v50, v52, v53\n\t"
    "v_mfma_f32_16x16x32_bf16 v[76:79], a[104:107], v[112:115], 0\n\t"
    "buffer_load_dwordx4 v3, s[40:43], s61 offen offset:1024 nt lds\n\t"
    "v_mfma_f32_16x16x32_bf16 v[84:87], a[104:107], v[128:131], 0\n\t"
    "v_exp_f32 v55, v55\n\t"
    "v_mfma_f32_16x16x32_bf16 v[68:71], a[108:111], v[100:103], v[68:71]\n\t"
    "ds_read_b64_tr_b16 a[138:139], v16 offset:13312\n\t"
    "ds_read_b64_tr_b16 a[140:141], v17 offset:9216\n\t"
    "v_mfma_f32_16x16x32_bf16 v[76:79], a[108:111], v[116:119], v[76:79]\n\t"
    "ds_read_b64_tr_b16 a[142:143], v17 offset:13312\n\t"
    "v_cvt_pk_bf16_f32 v51, v54, v55\n\t"
    "v_mfma_f32_16x16x32_bf16 v[84:87], a[108:111], v[132:135], v[84:87]\n\t"
    "v_exp_f32 v56, v56\n\t"
    "v_mfma_f32_16x16x32_bf16 v[68:71], a[112:115], v[104:107], v[68:71]\n\t"
    "v_exp_f32 v57, v57\n\t"
    "v_mfma_f32_16x16x32_bf16 v[76:79], a[112:115], v[120:123], v[76:79]\n\t"
    "ds_read_b64_tr_b16 a[144:145], v16 offset:9728\n\t"
    "ds_read_b64_tr_b16 a[146:147], v16 offset:13824\n\t"
    "v_mfma_f32_16x16x32_bf16 v[84:87], a[112:115], v[136:139], v[84:87]\n\t"
    "buffer_load_dwordx4 v4, s[40:43], s61 offen offset:2048 nt lds\n\t"
    "v_mfma_f32_16x16x32_bf16 v[68:71], a[116:119], v[108:111], v[68:71]\n\t"
    "ds_read_b64_tr_b16 a[148:149], v17 offset:9728\n\t"
    "v_cvt_pk_bf16_f32 v56, v56, v57\n\t"
    "v_mfma_f32_16x16x32_bf16 v[76:79], a[116:119], v[124:127], v[76:79]\n\t"
    "ds_read_b64_tr_b16 a[150:151], v17 offset:13824\n\t"
    "ds_read_b128 a[88:91], v10 offset:8192\n\t"
    "v_mfma_f32_16x16x32_bf16 v[84:87], a[116:119], v[140:143], v[84:87]\n\t"
    "s_waitcnt lgkmcnt(6)\n\t"
    "v_exp_f32 v58, v58\n\t"
    "v_mfma_f32_16x16x32_bf16 v[160:163], a[120:123], v[32:35], v[160:163]\n\t"
    "v_exp_f32 v59, v59\n\t"
    "v_mfma_f32_16x16x32_bf16 v[164:167], a[124:127], v[32:35], v[164:167]\n\t"
    "v_exp_f32 v60, v60\n\t"
    "v_mfma_f32_16x16x32_bf16 v[168:171], a[128:131], v[32:35], v[168:171]\n\t"
    "s_waitcnt lgkmcnt(5)\n\t"
    "buffer_load_dwordx4 v5, s[40:43], s61 offen offset:3072 nt lds\n\t"
    "v_mfma_f32_16x16x32_bf16 v[172:175], a[132:135], v[32:35], v[172:175]\n\t"
    "s_add_u32 s60, s54, 57344\n\t"
    "v_exp_f32 v61, v61\n\t"
    "v_mfma_f32_16x16x32_bf16 a[0:3], a[136:139], v[32:35], a[0:3]\n\t"
    "s_add_u32 m0, s53, 32768\n\t"
    "v_cvt_pk_bf16_f32 v57, v58, v59\n\t"
    "v_cvt_pk_bf16_f32 v58, v60, v61\n\t"
    "v_mfma_f32_16x16x32_bf16 a[4:7], a[140:143], v[32:35], a[4:7]\n\t"
    "s_waitcnt lgkmcnt(2)\n\t"
    "v_exp_f32 v62, v62\n\t"
    "v_mfma_f32_16x16x32_bf16 a[8:11], a[144:147], v[32:35], a[8:11]\n\t"
    "s_waitcnt lgkmcnt(0)\n\t"
    "v_exp_f32 v63, v63\n\t"
    "v_mfma_f32_16x16x32_bf16 a[12:15], a[148:151], v[32:35], a[12:15]\n\t"
    "ds_read_b128 a[92:95], v11 offset:8192\n\t"
    "v_cvt_pk_bf16_f32 v59, v62, v63\n\t"
    "v_mfma_f32_16x16x32_bf16 a[72:75], v[216:219], v[32:35], a[72:75]\n\t"
    "ds_read_b128 a[96:99], v12 offset:8192\n\t"
    "ds_read_b128 a[100:103], v13 offset:8192\n\t"
    "v_mfma_f32_16x16x32_bf16 v[176:179], a[120:123], v[40:43], v[176:179]\n\t"
    "v_exp_f32 v64, v64\n\t"
    "v_mfma_f32_16x16x32_bf16 v[180:183], a[124:127], v[40:43], v[180:183]\n\t"
    "v_exp_f32 v65, v65\n\t"
    "v_mfma_f32_16x16x32_bf16 v[184:187], a[128:131], v[40:43], v[184:187]\n\t"
    "ds_read_b128 a[104:107], v10 offset:12288\n\t"
    "v_cvt_pk_bf16_f32 v64, v64, v65\n\t"
    "v_mfma_f32_16x16x32_bf16 v[188:191], a[132:135], v[40:43], v[188:191]\n\t"
    "v_exp_f32 v66, v66\n\t"
    "v_mfma_f32_16x16x32_bf16 a[16:19], a[136:139], v[40:43], a[16:19]\n\t"
    "v_exp_f32 v67, v67\n\t"
    "v_mfma_f32_16x16x32_bf16 a[20:23], a[140:143], v[40:43], a[20:23]\n\t"
    "ds_read_b128 a[108:111], v11 offset:12288\n\t"
    "v_cvt_pk_bf16_f32 v65, v66, v67\n\t"
    "v_mfma_f32_16x16x32_bf16 a[24:27], a[144:147], v[40:43], a[24:27]\n\t"
    "v_exp_f32 v68, v68\n\t"
    "v_mfma_f32_16x16x32_bf16 a[28:31], a[148:151], v[40:43], a[28:31]\n\t"
    "buffer_load_dwordx4 v6, s[44:47], s60 offen offset:0 nt lds\n\t"
    "v_mfma_f32_16x16x32_bf16 a[76:79], v[216:219], v[40:43], a[76:79]\n\t"
    "v_exp_f32 v69, v69\n\t"
    "v_mfma_f32_16x16x32_bf16 v[192:195], a[120:123], v[48:51], v[192:195]\n\t"
    "ds_read_b128 a[112:115], v12 offset:12288\n\t"
    "v_cvt_pk_bf16_f32 v66, v68, v69\n\t"
    "v_mfma_f32_16x16x32_bf16 v[196:199], a[124:127], v[48:51], v[196:199]\n\t"
    "v_exp_f32 v70, v70\n\t"
    "v_mfma_f32_16x16x32_bf16 v[200:203], a[128:131], v[48:51], v[200:203]\n\t"
    "v_exp_f32 v71, v71\n\t"
    "v_mfma_f32_16x16x32_bf16 v[204:207], a[132:135], v[48:51], v[204:207]\n\t"
    "ds_read_b128 a[116:119], v13 offset:12288\n\t"
    "v_cvt_pk_bf16_f32 v67, v70, v71\n\t"
    "v_mfma_f32_16x16x32_bf16 a[32:35], a[136:139], v[48:51], a[32:35]\n\t"
    "buffer_load_dwordx4 v7, s[44:47], s60 offen offset:1024 nt lds\n\t"
    "v_mfma_f32_16x16x32_bf16 a[36:39], a[140:143], v[48:51], a[36:39]\n\t"
    "v_exp_f32 v72, v72\n\t"
    "v_mfma_f32_16x16x32_bf16 a[40:43], a[144:147], v[48:51], a[40:43]\n\t"
    "v_exp_f32 v73, v73\n\t"
    "v_mfma_f32_16x16x32_bf16 a[44:47], a[148:151], v[48:51], a[44:47]\n\t"
    "v_exp_f32 v74, v74\n\t"
    "v_mfma_f32_16x16x32_bf16 a[80:83], v[216:219], v[48:51], a[80:83]\n\t"
    "v_exp_f32 v75, v75\n\t"
    "v_mfma_f32_16x16x32_bf16 v[208:211], a[120:123], v[56:59], v[208:211]\n\t"
    "v_exp_f32 v76, v76\n\t"
    "v_mfma_f32_16x16x32_bf16 v[212:215], a[124:127], v[56:59], v[212:215]\n\t"
    "buffer_load_dwordx4 v8, s[44:47], s60 offen offset:2048 nt lds\n\t"
    "v_mfma_f32_16x16x32_bf16 a[48:51], a[128:131], v[56:59], a[48:51]\n\t"
    "v_exp_f32 v77, v77\n\t"
    "v_mfma_f32_16x16x32_bf16 a[52:55], a[132:135], v[56:59], a[52:55]\n\t"
    "v_cvt_pk_bf16_f32 v72, v72, v73\n\t"
    "v_cvt_pk_bf16_f32 v73, v74, v75\n\t"
    "v_mfma_f32_16x16x32_bf16 a[56:59], a[136:139], v[56:59], a[56:59]\n\t"
    "v_exp_f32 v78, v78\n\t"
    "v_mfma_f32_16x16x32_bf16 a[60:63], a[140:143], v[56:59], a[60:63]\n\t"
    "v_exp_f32 v79, v79\n\t"
    "v_mfma_f32_16x16x32_bf16 a[64:67], a[144:147], v[56:59], a[64:67]\n\t"
    "v_cvt_pk_bf16_f32 v74, v76, v77\n\t"
    "v_cvt_pk_bf16_f32 v75, v78, v79\n\t"
    "v_mfma_f32_16x16x32_bf16 a[68:71], a[148:151], v[56:59], a[68:71]\n\t"
    "s_waitcnt vmcnt(11) lgkmcnt(0)\n\t"
    "s_barrier\n\t"
    "v_mfma_f32_16x16x32_bf16 a[84:87], v[216:219], v[56:59], a[84:87]\n\t"
    "buffer_load_dwordx4 v9, s[44:47], s60 offen offset:3072 nt lds\n\t"
    "v_mfma_f32_16x16x32_bf16 v[32:35], a[88:91], v[96:99], 0\n\t"
    "s_add_u32 s61, s54, 73728\n\t"
    "v_exp_f32 v80, v80\n\t"
    "v_mfma_f32_16x16x32_bf16 v[40:43], a[88:91], v[112:115], 0\n\t"
    "s_add_u32 m0, s52, 32768\n\t"
    "ds_read_b64_tr_b16 a[120:121], v14 offset:16384\n\t"
    "ds_read_b64_tr_b16 a[122:123], v14 offset:20480\n\t"
    "v_mfma_f32_16x16x32_bf16 v[48:51], a[88:91], v[128:131], 0\n\t"
    "v_exp_f32 v81, v81\n\t"
    "v_mfma_f32_16x16x32_bf16 v[56:59], a[88:91], v[144:147], 0\n\t"
    "ds_read_b64_tr_b16 a[124:125], v15 offset:16384\n\t"
    "v_cvt_pk_bf16_f32 v80, v80, v81\n\t"
    "v_mfma_f32_16x16x32_bf16 v[32:35], a[92:95], v[100:103], v[32:35]\n\t"
    "v_exp_f32 v82, v82\n\t"
    "v_mfma_f32_16x16x32_bf16 v[40:43], a[92:95], v[116:119], v[40:43]\n\t"
    "buffer_load_dwordx4 v2, s[40:43], s61 offen offset:0 nt lds\n\t"
    "v_mfma_f32_16x16x32_bf16 v[48:51], a[92:95], v[132:135], v[48:51]\n\t"
    "v_exp_f32 v83, v83\n\t"
    "v_mfma_f32_16x16x32_bf16 v[56:59], a[92:95], v[148:151], v[56:59]\n\t"
    "ds_read_b64_tr_b16 a[126:127], v15 offset:20480\n\t"
    "ds_read_b64_tr_b16 a[128:129], v14 offset:16896\n\t"
    "v_mfma_f32_16x16x32_bf16 v[32:35], a[96:99], v[104:107], v[32:35]\n\t"
    "v_exp_f32 v84, v84\n\t"
    "v_mfma_f32_16x16x32_bf16 v[40:43], a[96:99], v[120:123], v[40:43]\n\t"
    "ds_read_b64_tr_b16 a[130:131], v14 offset:20992\n\t"
    "v_cvt_pk_bf16_f32 v81, v82, v83\n\t"
    "v_mfma_f32_16x16x32_bf16 v[48:51], a[96:99], v[136:139], v[48:51]\n\t"
    "v_exp_f32 v85, v85\n\t"
    "v_mfma_f32_16x16x32_bf16 v[56:59], a[96:99], v[152:155], v[56:59]\n\t"
    "buffer_load_dwordx4 v3, s[40:43], s61 offen offset:1024 nt lds\n\t"
    "v_mfma_f32_16x16x32_bf16 v[32:35], a[100:103], v[108:111], v[32:35]\n\t"
    "ds_read_b64_tr_b16 a[132:133], v15 offset:16896\n\t"
    "v_cvt_pk_bf16_f32 v82, v84, v85\n\t"
    "v_mfma_f32_16x16x32_bf16 v[40:43], a[100:103], v[124:127], v[40:43]\n\t"
    "v_exp_f32 v86, v86\n\t"
    "v_mfma_f32_16x16x32_bf16 v[48:51], a[100:103], v[140:143], v[48:51]\n\t"
    "v_exp_f32 v87, v87\n\t"
    "v_mfma_f32_16x16x32_bf16 v[56:59], a[100:103], v[156:159], v[56:59]\n\t"
    "ds_read_b64_tr_b16 a[134:135], v15 offset:20992\n\t"
    "ds_read_b64_tr_b16 a[136:137], v16 offset:17408\n\t"
    "v_mfma_f32_16x16x32_bf16 v[36:39], a[104:107], v[96:99], 0\n\t"
    "v_cvt_pk_bf16_f32 v83, v86, v87\n\t"
    "v_mfma_f32_16x16x32_bf16 v[44:47], a[104:107], v[112:115], 0\n\t"
    "buffer_load_dwordx4 v4, s[40:43], s61 offen offset:2048 nt lds\n\t"
    "v_mfma_f32_16x16x32_bf16 v[52:55], a[104:107], v[128:131], 0\n\t"
    "v_exp_f32 v32, v32\n\t"
    "v_mfma_f32_16x16x32_bf16 v[60:63], a[104:107], v[144:147], 0\n\t"
    "ds_read_b64_tr_b16 a[138:139], v16 offset:21504\n\t"
    "ds_read_b64_tr_b16 a[140:141], v17 offset:17408\n\t"
    "v_mfma_f32_16x16x32_bf16 v[36:39], a[108:111], v[100:103], v[36:39]\n\t"
    "v_exp_f32 v33, v33\n\t"
    "v_mfma_f32_16x16x32_bf16 v[44:47], a[108:111], v[116:119], v[44:47]\n\t"
    "ds_read_b64_tr_b16 a[142:143], v17 offset:21504\n\t"
    "v_cvt_pk_bf16_f32 v32, v32, v33\n\t"
    "v_mfma_f32_16x16x32_bf16 v[52:55], a[108:111], v[132:135], v[52:55]\n\t"
    "v_exp_f32 v34, v34\n\t"
    "v_mfma_f32_16x16x32_bf16 v[60:63], a[108:111], v[148:151], v[60:63]\n\t"
    "buffer_load_dwordx4 v5, s[40:43], s61 offen offset:3072 nt lds\n\t"
    "v_mfma_f32_16x16x32_bf16 v[36:39], a[112:115], v[104:107], v[36:39]\n\t"
    "s_add_u32 s60, s54, 65536\n\t"
    "v_exp_f32 v35, v35\n\t"
    "v_mfma_f32_16x16x32_bf16 v[44:47], a[112:115], v[120:123], v[44:47]\n\t"
    "s_add_u32 m0, s53, 0\n\t"
    "ds_read_b64_tr_b16 a[144:145], v16 offset:17920\n\t"
    "ds_read_b64_tr_b16 a[146:147], v16 offset:22016\n\t"
    "v_mfma_f32_16x16x32_bf16 v[52:55], a[112:115], v[136:139], v[52:55]\n\t"
    "v_exp_f32 v40, v40\n\t"
    "v_mfma_f32_16x16x32_bf16 v[60:63], a[112:115], v[152:155], v[60:63]\n\t"
    "ds_read_b64_tr_b16 a[148:149], v17 offset:17920\n\t"
    "v_cvt_pk_bf16_f32 v33, v34, v35\n\t"
    "v_mfma_f32_16x16x32_bf16 v[36:39], a[116:119], v[108:111], v[36:39]\n\t"
    "v_exp_f32 v41, v41\n\t"
    "v_mfma_f32_16x16x32_bf16 v[44:47], a[116:119], v[124:127], v[44:47]\n\t"
    "ds_read_b64_tr_b16 a[150:151], v17 offset:22016\n\t"
    "ds_read_b128 a[88:91], v10 offset:16384\n\t"
    "v_mfma_f32_16x16x32_bf16 v[52:55], a[116:119], v[140:143], v[52:55]\n\t"
    "ds_read_b128 a[92:95], v11 offset:16384\n\t"
    "v_cvt_pk_bf16_f32 v40, v40, v41\n\t"
    "v_mfma_f32_16x16x32_bf16 v[60:63], a[116:119], v[156:159], v[60:63]\n\t"
    "s_waitcnt lgkmcnt(6)\n\t"
    "v_exp_f32 v42, v42\n\t"
    "v_mfma_f32_16x16x32_bf16 v[160:163], a[120:123], v[64:67], v[160:163]\n\t"
    "v_exp_f32 v43, v43\n\t"
    "v_mfma_f32_16x16x32_bf16 v[164:167], a[124:127], v[64:67], v[164:167]\n\t"
    "ds_read_b128 a[96:99], v12 offset:16384\n\t"
    "v_cvt_pk_bf16_f32 v41, v42, v43\n\t"
    "v_mfma_f32_16x16x32_bf16 v[168:171], a[128:131], v[64:67], v[168:171]\n\t"
    "v_exp_f32 v36, v36\n\t"
    "v_mfma_f32_16x16x32_bf16 v[172:175], a[132:135], v[64:67], v[172:175]\n\t"
    "v_exp_f32 v37, v37\n\t"
    "v_mfma_f32_16x16x32_bf16 a[0:3], a[136:139], v[64:67], a[0:3]\n\t"
    "ds_read_b128 a[100:103], v13 offset:16384\n\t"
    "v_cvt_pk_bf16_f32 v34, v36, v37\n\t"
    "v_mfma_f32_16x16x32_bf16 a[4:7], a[140:143], v[64:67], a[4:7]\n\t"
    "s_waitcnt lgkmcnt(3)\n\t"
    "v_exp_f32 v38, v38\n\t"
    "v_mfma_f32_16x16x32_bf16 a[8:11], a[144:147], v[64:67], a[8:11]\n\t"
    "v_exp_f32 v39, v39\n\t"
    "v_mfma_f32_16x16x32_bf16 a[12:15], a[148:151], v[64:67], a[12:15]\n\t"
    "ds_read_b128 a[104:107], v10 offset:20480\n\t"
    "v_cvt_pk_bf16_f32 v35, v38, v39\n\t"
    "v_mfma_f32_16x16x32_bf16 a[72:75], v[216:219], v[64:67], a[72:75]\n\t"
    "buffer_load_dwordx4 v6, s[44:47], s60 offen offset:0 nt lds\n\t"
    "v_mfma_f32_16x16x32_bf16 v[176:179], a[120:123], v[72:75], v[176:179]\n\t"
    "v_exp_f32 v44, v44\n\t"
    "v_mfma_f32_16x16x32_bf16 v[180:183], a[124:127], v[72:75], v[180:183]\n\t"
    "v_exp_f32 v45, v45\n\t"
    "v_mfma_f32_16x16x32_bf16 v[184:187], a[128:131], v[72:75], v[184:187]\n\t"
    "ds_read_b128 a[108:111], v11 offset:20480\n\t"
    "ds_read_b128 a[112:115], v12 offset:20480\n\t"
    "v_mfma_f32_16x16x32_bf16 v[188:191], a[132:135], v[72:75], v[188:191]\n\t"
    "v_exp_f32 v46, v46\n\t"
    "v_mfma_f32_16x16x32_bf16 a[16:19], a[136:139], v[72:75], a[16:19]\n\t"
    "ds_read_b128 a[116:119], v13 offset:20480\n\t"
    "v_cvt_pk_bf16_f32 v42, v44, v45\n\t"
    "v_mfma_f32_16x16x32_bf16 a[20:23], a[140:143], v[72:75], a[20:23]\n\t"
    "buffer_load_dwordx4 v7, s[44:47], s60 offen offset:1024 nt lds\n\t"
    "v_mfma_f32_16x16x32_bf16 a[24:27], a[144:147], v[72:75], a[24:27]\n\t"
    "v_exp_f32 v47, v47\n\t"
    "v_mfma_f32_16x16x32_bf16 a[28:31], a[148:151], v[72:75], a[28:31]\n\t"
    "v_exp_f32 v48, v48\n\t"
    "v_mfma_f32_16x16x32_bf16 a[76:79], v[216:219], v[72:75], a[76:79]\n\t"
    "v_exp_f32 v49, v49\n\t"
    "v_mfma_f32_16x16x32_bf16 v[192:195], a[120:123], v[80:83], v[192:195]\n\t"
    "v_cvt_pk_bf16_f32 v43, v46, v47\n\t"
    "v_cvt_pk_bf16_f32 v48, v48, v49\n\t"
    "v_mfma_f32_16x16x32_bf16 v[196:199], a[124:127], v[80:83], v[196:199]\n\t"
    "v_exp_f32 v50, v50\n\t"
    "v_mfma_f32_16x16x32_bf16 v[200:203], a[128:131], v[80:83], v[200:203]\n\t"
    "buffer_load_dwordx4 v8, s[44:47], s60 offen offset:2048 nt lds\n\t"
    "v_mfma_f32_16x16x32_bf16 v[204:207], a[132:135], v[80:83], v[204:207]\n\t"
    "v_exp_f32 v51, v51\n\t"
    "v_mfma_f32_16x16x32_bf16 a[32:35], a[136:139], v[80:83], a[32:35]\n\t"
    "v_exp_f32 v52, v52\n\t"
    "v_mfma_f32_16x16x32_bf16 a[36:39], a[140:143], v[80:83], a[36:39]\n\t"
    "v_exp_f32 v53, v53\n\t"
    "v_mfma_f32_16x16x32_bf16 a[40:43], a[144:147], v[80:83], a[40:43]\n\t"
    "v_cvt_pk_bf16_f32 v49, v50, v51\n\t"
    "v_cvt_pk_bf16_f32 v50, v52, v53\n\t"
    "v_mfma_f32_16x16x32_bf16 a[44:47], a[148:151], v[80:83], a[44:47]\n\t"
    "v_exp_f32 v54, v54\n\t"
    "v_mfma_f32_16x16x32_bf16 a[80:83], v[216:219], v[80:83], a[80:83]\n\t"
    "s_waitcnt vmcnt(11) lgkmcnt(0)\n\t"
    "s_barrier\n\t"
    "v_mfma_f32_16x16x32_bf16 v[64:67], a[88:91], v[96:99], 0\n\t"
    "buffer_load_dwordx4 v9, s[44:47], s60 offen offset:3072 nt lds\n\t"
    "v_mfma_f32_16x16x32_bf16 v[72:75], a[88:91], v[112:115], 0\n\t"
    "s_add_u32 s61, s54, 81920\n\t"
    "ds_read_b64_tr_b16 a[120:121], v14 offset:24576\n\t"
    "ds_read_b64_tr_b16 a[122:123], v14 offset:28672\n\t"
    "v_mfma_f32_16x16x32_bf16 v[80:83], a[88:91], v[128:131], 0\n\t"
    "s_add_u32 m0, s52, 0\n\t"
    "v_exp_f32 v55, v55\n\t"
    "v_mfma_f32_16x16x32_bf16 v[64:67], a[92:95], v[100:103], v[64:67]\n\t"
    "ds_read_b64_tr_b16 a[124:125], v15 offset:24576\n\t"
    "v_cvt_pk_bf16_f32 v51, v54, v55\n\t"
    "v_mfma_f32_16x16x32_bf16 v[72:75], a[92:95], v[116:119], v[72:75]\n\t"
    "v_exp_f32 v56, v56\n\t"
    "v_mfma_f32_16x16x32_bf16 v[80:83], a[92:95], v[132:135], v[80:83]\n\t"
    "v_exp_f32 v57, v57\n\t"
    "v_mfma_f32_16x16x32_bf16 v[64:67], a[96:99], v[104:107], v[64:67]\n\t"
    "ds_read_b64_tr_b16 a[126:127], v15 offset:28672\n\t"
    "ds_read_b64_tr_b16 a[128:129], v14 offset:25088\n\t"
    "v_mfma_f32_16x16x32_bf16 v[72:75], a[96:99], v[120:123], v[72:75]\n\t"
    "buffer_load_dwordx4 v2, s[40:43], s61 offen offset:0 nt lds\n\t"
    "v_mfma_f32_16x16x32_bf16 v[80:83], a[96:99], v[136:139], v[80:83]\n\t"
    "ds_read_b64_tr_b16 a[130:131], v14 offset:29184\n\t"
    "v_cvt_pk_bf16_f32 v56, v56, v57\n\t"
    "v_mfma_f32_16x16x32_bf16 v[64:67], a[100:103], v[108:111], v[64:67]\n\t"
    "v_exp_f32 v58, v58\n\t"
    "v_mfma_f32_16x16x32_bf16 v[72:75], a[100:103], v[124:127], v[72:75]\n\t"
    "ds_read_b64_tr_b16 a[132:133], v15 offset:25088\n\t"
    "ds_read_b64_tr_b16 a[134:135], v15 offset:29184\n\t"
    "v_mfma_f32_16x16x32_bf16 v[80:83], a[100:103], v[140:143], v[80:83]\n\t"
    "v_exp_f32 v59, v59\n\t"
    "v_mfma_f32_16x16x32_bf16 v[68:71], a[104:107], v[96:99], 0\n\t"
    "ds_read_b64_tr_b16 a[136:137], v16 offset:25600\n\t"
    "v_cvt_pk_bf16_f32 v57, v58, v59\n\t"
    "v_mfma_f32_16x16x32_bf16 v[76:79], a[104:107], v[112:115], 0\n\t"
    "buffer_load_dwordx4 v3, s[40:43], s61 offen offset:1024 nt lds\n\t"
    "v_mfma_f32_16x16x32_bf16 v[84:87], a[104:107], v[128:131], 0\n\t"
    "v_exp_f32 v60, v60\n\t"
    "v_mfma_f32_16x16x32_bf16 v[68:71], a[108:111], v[100:103], v[68:71]\n\t"
    "ds_read_b64_tr_b16 a[138:139], v16 offset:29696\n\t"
    "ds_read_b64_tr_b16 a[140:141], v17 offset:25600\n\t"
    "v_mfma_f32_16x16x32_bf16 v[76:79], a[108:111], v[116:119], v[76:79]\n\t"
    "v_exp_f32 v61, v61\n\t"
    "v_mfma_f32_16x16x32_bf16 v[84:87], a[108:111], v[132:135], v[84:87]\n\t"
    "ds_read_b64_tr_b16 a[142:143], v17 offset:29696\n\t"
    "v_cvt_pk_bf16_f32 v58, v60, v61\n\t"
    "v_mfma_f32_16x16x32_bf16 v[68:71], a[112:115], v[104:107], v[68:71]\n\t"
    "v_exp_f32 v62, v62\n\t"
    "v_mfma_f32_16x16x32_bf16 v[76:79], a[112:115], v[120:123], v[76:79]\n\t"
    "ds_read_b64_tr_b16 a[144:145], v16 offset:26112\n\t"
    "ds_read_b64_tr_b16 a[146:147], v16 offset:30208\n\t"
    "v_mfma_f32_16x16x32_bf16 v[84:87], a[112:115], v[136:139], v[84:87]\n\t"
    "buffer_load_dwordx4 v4, s[40:43], s61 offen offset:2048 nt lds\n\t"
    "v_mfma_f32_16x16x32_bf16 v[68:71], a[116:119], v[108:111], v[68:71]\n\t"
    "ds_read_b64_tr_b16 a[148:149], v17 offset:26112\n\t"
    "ds_read_b128 a[88:91], v10 offset:24576\n\t"
    "v_mfma_f32_16x16x32_bf16 v[76:79], a[116:119], v[124:127], v[76:79]\n\t"
    "ds_read_b64_tr_b16 a[150:151], v17 offset:30208\n\t"
    "v_mfma_f32_16x16x32_bf16 v[84:87], a[116:119], v[140:143], v[84:87]\n\t"
    "s_waitcnt lgkmcnt(6)\n\t"
    "v_exp_f32 v63, v63\n\t"
    "v_mfma_f32_16x16x32_bf16 v[160:163], a[120:123], v[32:35], v[160:163]\n\t"
    "ds_read_b128 a[92:95], v11 offset:24576\n\t"
    "v_cvt_pk_bf16_f32 v59, v62, v63\n\t"
    "v_mfma_f32_16x16x32_bf16 v[164:167], a[124:127], v[32:35], v[164:167]\n\t"
    "v_exp_f32 v64, v64\n\t"
    "v_mfma_f32_16x16x32_bf16 v[168:171], a[128:131], v[32:35], v[168:171]\n\t"
    "s_waitcnt lgkmcnt(6)\n\t"
    "buffer_load_dwordx4 v5, s[40:43], s61 offen offset:3072 nt lds\n\t"
    "v_mfma_f32_16x16x32_bf16 v[172:175], a[132:135], v[32:35], v[172:175]\n\t"
    "s_add_u32 s60, s54, 73728\n\t"
    "v_exp_f32 v65, v65\n\t"
    "v_mfma_f32_16x16x32_bf16 a[0:3], a[136:139], v[32:35], a[0:3]\n\t"
    "s_add_u32 m0, s53, 8192\n\t"
    "ds_read_b128 a[96:99], v12 offset:24576\n\t"
    "v_cvt_pk_bf16_f32 v64, v64, v65\n\t"
    "v_mfma_f32_16x16x32_bf16 a[4:7], a[140:143], v[32:35], a[4:7]\n\t"
    "s_waitcnt lgkmcnt(3)\n\t"
    "v_exp_f32 v66, v66\n\t"
    "v_mfma_f32_16x16x32_bf16 a[8:11], a[144:147], v[32:35], a[8:11]\n\t"
    "s_waitcnt lgkmcnt(2)\n\t"
    "v_exp_f32 v67, v67\n\t"
    "v_mfma_f32_16x16x32_bf16 a[12:15], a[148:151], v[32:35], a[12:15]\n\t"
    "ds_read_b128 a[100:103], v13 offset:24576\n\t"
    "v_cvt_pk_bf16_f32 v65, v66, v67\n\t"
    "v_mfma_f32_16x16x32_bf16 a[72:75], v[216:219], v[32:35], a[72:75]\n\t"
    "v_exp_f32 v68, v68\n\t"
    "v_mfma_f32_16x16x32_bf16 v[176:179], a[120:123], v[40:43], v[176:179]\n\t"
    "v_exp_f32 v69, v69\n\t"
    "v_mfma_f32_16x16x32_bf16 v[180:183], a[124:127], v[40:43], v[180:183]\n\t"
    "ds_read_b128 a[104:107], v10 offset:28672\n\t"
    "v_cvt_pk_bf16_f32 v66, v68, v69\n\t"
    "v_mfma_f32_16x16x32_bf16 v[184:187], a[128:131], v[40:43], v[184:187]\n\t"
    "v_exp_f32 v70, v70\n\t"
    "v_mfma_f32_16x16x32_bf16 v[188:191], a[132:135], v[40:43], v[188:191]\n\t"
    "v_exp_f32 v71, v71\n\t"
    "v_mfma_f32_16x16x32_bf16 a[16:19], a[136:139], v[40:43], a[16:19]\n\t"
    "ds_read_b128 a[108:111], v11 offset:28672\n\t"
    "v_cvt_pk_bf16_f32 v67, v70, v71\n\t"
    "v_mfma_f32_16x16x32_bf16 a[20:23], a[140:143], v[40:43], a[20:23]\n\t"
    "v_exp_f32 v72, v72\n\t"
    "v_mfma_f32_16x16x32_bf16 a[24:27], a[144:147], v[40:43], a[24:27]\n\t"
    "v_exp_f32 v73, v73\n\t"
    "v_mfma_f32_16x16x32_bf16 a[28:31], a[148:151], v[40:43], a[28:31]\n\t"
    "buffer_load_dwordx4 v6, s[44:47], s60 offen offset:0 nt lds\n\t"
    "v_mfma_f32_16x16x32_bf16 a[76:79], v[216:219], v[40:43], a[76:79]\n\t"
    "ds_read_b128 a[112:115], v12 offset:28672\n\t"
    "v_cvt_pk_bf16_f32 v72, v72, v73\n\t"
    "v_mfma_f32_16x16x32_bf16 v[192:195], a[120:123], v[48:51], v[192:195]\n\t"
    "v_exp_f32 v74, v74\n\t"
    "v_mfma_f32_16x16x32_bf16 v[196:199], a[124:127], v[48:51], v[196:199]\n\t"
    "v_exp_f32 v75, v75\n\t"
    "v_mfma_f32_16x16x32_bf16 v[200:203], a[128:131], v[48:51], v[200:203]\n\t"
    "ds_read_b128 a[116:119], v13 offset:28672\n\t"
    "v_cvt_pk_bf16_f32 v73, v74, v75\n\t"
    "v_mfma_f32_16x16x32_bf16 v[204:207], a[132:135], v[48:51], v[204:207]\n\t"
    "v_exp_f32 v76, v76\n\t"
    "v_mfma_f32_16x16x32_bf16 a[32:35], a[136:139], v[48:51], a[32:35]\n\t"
    "buffer_load_dwordx4 v7, s[44:47], s60 offen offset:1024 nt lds\n\t"
    "v_mfma_f32_16x16x32_bf16 a[36:39], a[140:143], v[48:51], a[36:39]\n\t"
    "v_exp_f32 v77, v77\n\t"
    "v_mfma_f32_16x16x32_bf16 a[40:43], a[144:147], v[48:51], a[40:43]\n\t"
    "v_exp_f32 v78, v78\n\t"
    "v_mfma_f32_16x16x32_bf16 a[44:47], a[148:151], v[48:51], a[44:47]\n\t"
    "v_exp_f32 v79, v79\n\t"
    "v_mfma_f32_16x16x32_bf16 a[80:83], v[216:219], v[48:51], a[80:83]\n\t"
    "v_cvt_pk_bf16_f32 v74, v76, v77\n\t"
    "v_cvt_pk_bf16_f32 v75, v78, v79\n\t"
    "v_mfma_f32_16x16x32_bf16 v[208:211], a[120:123], v[56:59], v[208:211]\n\t"
    "v_exp_f32 v80, v80\n\t"
    "v_mfma_f32_16x16x32_bf16 v[212:215], a[124:127], v[56:59], v[212:215]\n\t"
    "buffer_load_dwordx4 v8, s[44:47], s60 offen offset:2048 nt lds\n\t"
    "v_mfma_f32_16x16x32_bf16 a[48:51], a[128:131], v[56:59], a[48:51]\n\t"
    "v_exp_f32 v81, v81\n\t"
    "v_mfma_f32_16x16x32_bf16 a[52:55], a[132:135], v[56:59], a[52:55]\n\t"
    "v_exp_f32 v82, v82\n\t"
    "v_mfma_f32_16x16x32_bf16 a[56:59], a[136:139], v[56:59], a[56:59]\n\t"
    "v_exp_f32 v83, v83\n\t"
    "v_mfma_f32_16x16x32_bf16 a[60:63], a[140:143], v[56:59], a[60:63]\n\t"
    "v_exp_f32 v84, v84\n\t"
    "v_mfma_f32_16x16x32_bf16 a[64:67], a[144:147], v[56:59], a[64:67]\n\t"
    "v_exp_f32 v85, v85\n\t"
    "v_mfma_f32_16x16x32_bf16 a[68:71], a[148:151], v[56:59], a[68:71]\n\t"
    "s_waitcnt vmcnt(11) lgkmcnt(0)\n\t"
    "s_barrier\n\t"
    "v_mfma_f32_16x16x32_bf16 a[84:87], v[216:219], v[56:59], a[84:87]\n\t"
    "buffer_load_dwordx4 v9, s[44:47], s60 offen offset:3072 nt lds\n\t"
    "v_mfma_f32_16x16x32_bf16 v[32:35], a[88:91], v[96:99], 0\n\t"
    "s_add_u32 s61, s54, 90112\n\t"
    "ds_read_b64_tr_b16 a[120:121], v14 offset:32768\n\t"
    "v_cvt_pk_bf16_f32 v80, v80, v81\n\t"
    "v_mfma_f32_16x16x32_bf16 v[40:43], a[88:91], v[112:115], 0\n\t"
    "s_add_u32 m0, s52, 8192\n\t"
    "ds_read_b64_tr_b16 a[122:123], v14 offset:36864\n\t"
    "v_cvt_pk_bf16_f32 v81, v82, v83\n\t"
    "v_mfma_f32_16x16x32_bf16 v[48:51], a[88:91], v[128:131], 0\n\t"
    "v_exp_f32 v86, v86\n\t"
    "v_mfma_f32_16x16x32_bf16 v[56:59], a[88:91], v[144:147], 0\n\t"
    "ds_read_b64_tr_b16 a[124:125], v15 offset:32768\n\t"
    "v_cvt_pk_bf16_f32 v82, v84, v85\n\t"
    "v_mfma_f32_16x16x32_bf16 v[32:35], a[92:95], v[100:103], v[32:35]\n\t"
    "v_exp_f32 v87, v87\n\t"
    "v_mfma_f32_16x16x32_bf16 v[40:43], a[92:95], v[116:119], v[40:43]\n\t"
    "buffer_load_dwordx4 v2, s[40:43], s61 offen offset:0 nt lds\n\t"
    "v_mfma_f32_16x16x32_bf16 v[48:51], a[92:95], v[132:135], v[48:51]\n\t"
    "ds_read_b64_tr_b16 a[126:127], v15 offset:36864\n\t"
    "v_cvt_pk_bf16_f32 v83, v86, v87\n\t"
    "v_mfma_f32_16x16x32_bf16 v[56:59], a[92:95], v[148:151], v[56:59]\n\t"
    "ds_read_b64_tr_b16 a[128:129], v14 offset:33280\n\t"
    "v_mfma_f32_16x16x32_bf16 v[32:35], a[96:99], v[104:107], v[32:35]\n\t"
    "v_mfma_f32_16x16x32_bf16 v[40:43], a[96:99], v[120:123], v[40:43]\n\t"
    "ds_read_b64_tr_b16 a[130:131], v14 offset:37376\n\t"
    "v_mfma_f32_16x16x32_bf16 v[48:51], a[96:99], v[136:139], v[48:51]\n\t"
    "v_mfma_f32_16x16x32_bf16 v[56:59], a[96:99], v[152:155], v[56:59]\n\t"
    "buffer_load_dwordx4 v3, s[40:43], s61 offen offset:1024 nt lds\n\t"
    "v_mfma_f32_16x16x32_bf16 v[32:35], a[100:103], v[108:111], v[32:35]\n\t"
    "ds_read_b64_tr_b16 a[132:133], v15 offset:33280\n\t"
    "v_mfma_f32_16x16x32_bf16 v[40:43], a[100:103], v[124:127], v[40:43]\n\t"
    "ds_read_b64_tr_b16 a[134:135], v15 offset:37376\n\t"
    "v_mfma_f32_16x16x32_bf16 v[48:51], a[100:103], v[140:143], v[48:51]\n\t"
    "v_mfma_f32_16x16x32_bf16 v[56:59], a[100:103], v[156:159], v[56:59]\n\t"
    "ds_read_b64_tr_b16 a[136:137], v16 offset:33792\n\t"
    "v_mfma_f32_16x16x32_bf16 v[36:39], a[104:107], v[96:99], 0\n\t"
    "v_mfma_f32_16x16x32_bf16 v[44:47], a[104:107], v[112:115], 0\n\t"
    "buffer_load_dwordx4 v4, s[40:43], s61 offen offset:2048 nt lds\n\t"
    "v_mfma_f32_16x16x32_bf16 v[52:55], a[104:107], v[128:131], 0\n\t"
    "v_exp_f32 v32, v32\n\t"
    "v_mfma_f32_16x16x32_bf16 v[60:63], a[104:107], v[144:147], 0\n\t"
    "ds_read_b64_tr_b16 a[138:139], v16 offset:37888\n\t"
    "ds_read_b64_tr_b16 a[140:141], v17 offset:33792\n\t"
    "v_mfma_f32_16x16x32_bf16 v[36:39], a[108:111], v[100:103], v[36:39]\n\t"
    "v_exp_f32 v33, v33\n\t"
    "v_mfma_f32_16x16x32_bf16 v[44:47], a[108:111], v[116:119], v[44:47]\n\t"
    "ds_read_b64_tr_b16 a[142:143], v17 offset:37888\n\t"
    "v_cvt_pk_bf16_f32 v32, v32, v33\n\t"
    "v_mfma_f32_16x16x32_bf16 v[52:55], a[108:111], v[132:135], v[52:55]\n\t"
    "v_exp_f32 v34, v34\n\t"
    "v_mfma_f32_16x16x32_bf16 v[60:63], a[108:111], v[148:151], v[60:63]\n\t"
    "buffer_load_dwordx4 v5, s[40:43], s61 offen offset:3072 nt lds\n\t"
    "v_mfma_f32_16x16x32_bf16 v[36:39], a[112:115], v[104:107], v[36:39]\n\t"
    "s_add_u32 s60, s54, 81920\n\t"
    "v_exp_f32 v35, v35\n\t"
    "v_mfma_f32_16x16x32_bf16 v[44:47], a[112:115], v[120:123], v[44:47]\n\t"
    "s_add_u32 m0, s53, 16384\n\t"
    "ds_read_b64_tr_b16 a[144:145], v16 offset:34304\n\t"
    "ds_read_b64_tr_b16 a[146:147], v16 offset:38400\n\t"
    "v_mfma_f32_16x16x32_bf16 v[52:55], a[112:115], v[136:139], v[52:55]\n\t"
    "v_exp_f32 v40, v40\n\t"
    "v_mfma_f32_16x16x32_bf16 v[60:63], a[112:115], v[152:155], v[60:63]\n\t"
    "ds_read_b64_tr_b16 a[148:149], v17 offset:34304\n\t"
    "v_cvt_pk_bf16_f32 v33, v34, v35\n\t"
    "v_mfma_f32_16x16x32_bf16 v[36:39], a[116:119], v[108:111], v[36:39]\n\t"
    "v_exp_f32 v41, v41\n\t"
    "v_mfma_f32_16x16x32_bf16 v[44:47], a[116:119], v[124:127], v[44:47]\n\t"
    "ds_read_b64_tr_b16 a[150:151], v17 offset:38400\n\t"
    "ds_read_b128 a[88:91], v10 offset:32768\n\t"
    "v_mfma_f32_16x16x32_bf16 v[52:55], a[116:119], v[140:143], v[52:55]\n\t"
    "ds_read_b128 a[92:95], v11 offset:32768\n\t"
    "v_cvt_pk_bf16_f32 v40, v40, v41\n\t"
    "v_mfma_f32_16x16x32_bf16 v[60:63], a[116:119], v[156:159], v[60:63]\n\t"
    "s_waitcnt lgkmcnt(6)\n\t"
    "v_exp_f32 v42, v42\n\t"
    "v_mfma_f32_16x16x32_bf16 v[160:163], a[120:123], v[64:67], v[160:163]\n\t"
    "v_exp_f32 v43, v43\n\t"
    "v_mfma_f32_16x16x32_bf16 v[164:167], a[124:127], v[64:67], v[164:167]\n\t"
    "ds_read_b128 a[96:99], v12 offset:32768\n\t"
    "v_cvt_pk_bf16_f32 v41, v42, v43\n\t"
    "v_mfma_f32_16x16x32_bf16 v[168:171], a[128:131], v[64:67], v[168:171]\n\t"
    "v_exp_f32 v36, v36\n\t"
    "v_mfma_f32_16x16x32_bf16 v[172:175], a[132:135], v[64:67], v[172:175]\n\t"
    "v_exp_f32 v37, v37\n\t"
    "v_mfma_f32_16x16x32_bf16 a[0:3], a[136:139], v[64:67], a[0:3]\n\t"
    "ds_read_b128 a[100:103], v13 offset:32768\n\t"
    "v_cvt_pk_bf16_f32 v34, v36, v37\n\t"
    "v_mfma_f32_16x16x32_bf16 a[4:7], a[140:143], v[64:67], a[4:7]\n\t"
    "s_waitcnt lgkmcnt(3)\n\t"
    "v_exp_f32 v38, v38\n\t"
    "v_mfma_f32_16x16x32_bf16 a[8:11], a[144:147], v[64:67], a[8:11]\n\t"
    "v_exp_f32 v39, v39\n\t"
    "v_mfma_f32_16x16x32_bf16 a[12:15], a[148:151], v[64:67], a[12:15]\n\t"
    "ds_read_b128 a[104:107], v10 offset:36864\n\t"
    "v_cvt_pk_bf16_f32 v35, v38, v39\n\t"
    "v_mfma_f32_16x16x32_bf16 a[72:75], v[216:219], v[64:67], a[72:75]\n\t"
    "buffer_load_dwordx4 v6, s[44:47], s60 offen offset:0 nt lds\n\t"
    "v_mfma_f32_16x16x32_bf16 v[176:179], a[120:123], v[72:75], v[176:179]\n\t"
    "v_exp_f32 v44, v44\n\t"
    "v_mfma_f32_16x16x32_bf16 v[180:183], a[124:127], v[72:75], v[180:183]\n\t"
    "v_exp_f32 v45, v45\n\t"
    "v_mfma_f32_16x16x32_bf16 v[184:187], a[128:131], v[72:75], v[184:187]\n\t"
    "ds_read_b128 a[108:111], v11 offset:36864\n\t"
    "ds_read_b128 a[112:115], v12 offset:36864\n\t"
    "v_mfma_f32_16x16x32_bf16 v[188:191], a[132:135], v[72:75], v[188:191]\n\t"
    "v_exp_f32 v46, v46\n\t"
    "v_mfma_f32_16x16x32_bf16 a[16:19], a[136:139], v[72:75], a[16:19]\n\t"
    "ds_read_b128 a[116:119], v13 offset:36864\n\t"
    "v_cvt_pk_bf16_f32 v42, v44, v45\n\t"
    "v_mfma_f32_16x16x32_bf16 a[20:23], a[140:143], v[72:75], a[20:23]\n\t"
    "buffer_load_dwordx4 v7, s[44:47], s60 offen offset:1024 nt lds\n\t"
    "v_mfma_f32_16x16x32_bf16 a[24:27], a[144:147], v[72:75], a[24:27]\n\t"
    "v_exp_f32 v47, v47\n\t"
    "v_mfma_f32_16x16x32_bf16 a[28:31], a[148:151], v[72:75], a[28:31]\n\t"
    "v_exp_f32 v48, v48\n\t"
    "v_mfma_f32_16x16x32_bf16 a[76:79], v[216:219], v[72:75], a[76:79]\n\t"
    "v_exp_f32 v49, v49\n\t"
    "v_mfma_f32_16x16x32_bf16 v[192:195], a[120:123], v[80:83], v[192:195]\n\t"
    "v_cvt_pk_bf16_f32 v43, v46, v47\n\t"
    "v_cvt_pk_bf16_f32 v48, v48, v49\n\t"
    "v_mfma_f32_16x16x32_bf16 v[196:199], a[124:127], v[80:83], v[196:199]\n\t"
    "v_exp_f32 v50, v50\n\t"
    "v_mfma_f32_16x16x32_bf16 v[200:203], a[128:131], v[80:83], v[200:203]\n\t"
    "buffer_load_dwordx4 v8, s[44:47], s60 offen offset:2048 nt lds\n\t"
    "v_mfma_f32_16x16x32_bf16 v[204:207], a[132:135], v[80:83], v[204:207]\n\t"
    "v_exp_f32 v51, v51\n\t"
    "v_mfma_f32_16x16x32_bf16 a[32:35], a[136:139], v[80:83], a[32:35]\n\t"
    "v_exp_f32 v52, v52\n\t"
    "v_mfma_f32_16x16x32_bf16 a[36:39], a[140:143], v[80:83], a[36:39]\n\t"
    "v_exp_f32 v53, v53\n\t"
    "v_mfma_f32_16x16x32_bf16 a[40:43], a[144:147], v[80:83], a[40:43]\n\t"
    "v_cvt_pk_bf16_f32 v49, v50, v51\n\t"
    "v_cvt_pk_bf16_f32 v50, v52, v53\n\t"
    "v_mfma_f32_16x16x32_bf16 a[44:47], a[148:151], v[80:83], a[44:47]\n\t"
    "v_exp_f32 v54, v54\n\t"
    "v_mfma_f32_16x16x32_bf16 a[80:83], v[216:219], v[80:83], a[80:83]\n\t"
    "s_waitcnt vmcnt(11) lgkmcnt(0)\n\t"
    "s_barrier\n\t"
    "v_mfma_f32_16x16x32_bf16 v[64:67], a[88:91], v[96:99], 0\n\t"
    "buffer_load_dwordx4 v9, s[44:47], s60 offen offset:3072 nt lds\n\t"
    "v_mfma_f32_16x16x32_bf16 v[72:75], a[88:91], v[112:115], 0\n\t"
    "s_add_u32 s61, s54, 98304\n\t"
    "ds_read_b64_tr_b16 a[120:121], v14 offset:0\n\t"
    "ds_read_b64_tr_b16 a[122:123], v14 offset:4096\n\t"
    "v_mfma_f32_16x16x32_bf16 v[80:83], a[88:91], v[128:131], 0\n\t"
    "s_add_u32 m0, s52, 16384\n\t"
    "v_exp_f32 v55, v55\n\t"
    "v_mfma_f32_16x16x32_bf16 v[64:67], a[92:95], v[100:103], v[64:67]\n\t"
    "ds_read_b64_tr_b16 a[124:125], v15 offset:0\n\t"
    "v_cvt_pk_bf16_f32 v51, v54, v55\n\t"
    "v_mfma_f32_16x16x32_bf16 v[72:75], a[92:95], v[116:119], v[72:75]\n\t"
    "v_exp_f32 v56, v56\n\t"
    "v_mfma_f32_16x16x32_bf16 v[80:83], a[92:95], v[132:135], v[80:83]\n\t"
    "v_exp_f32 v57, v57\n\t"
    "v_mfma_f32_16x16x32_bf16 v[64:67], a[96:99], v[104:107], v[64:67]\n\t"
    "ds_read_b64_tr_b16 a[126:127], v15 offset:4096\n\t"
    "ds_read_b64_tr_b16 a[128:129], v14 offset:512\n\t"
    "v_mfma_f32_16x16x32_bf16 v[72:75], a[96:99], v[120:123], v[72:75]\n\t"
    "buffer_load_dwordx4 v2, s[40:43], s61 offen offset:0 nt lds\n\t"
    "v_mfma_f32_16x16x32_bf16 v[80:83], a[96:99], v[136:139], v[80:83]\n\t"
    "ds_read_b64_tr_b16 a[130:131], v14 offset:4608\n\t"
    "v_cvt_pk_bf16_f32 v56, v56, v57\n\t"
    "v_mfma_f32_16x16x32_bf16 v[64:67], a[100:103], v[108:111], v[64:67]\n\t"
    "v_exp_f32 v58, v58\n\t"
    "v_mfma_f32_16x16x32_bf16 v[72:75], a[100:103], v[124:127], v[72:75]\n\t"
    "ds_read_b64_tr_b16 a[132:133], v15 offset:512\n\t"
    "ds_read_b64_tr_b16 a[134:135], v15 offset:4608\n\t"
    "v_mfma_f32_16x16x32_bf16 v[80:83], a[100:103], v[140:143], v[80:83]\n\t"
    "v_exp_f32 v59, v59\n\t"
    "v_mfma_f32_16x16x32_bf16 v[68:71], a[104:107], v[96:99], 0\n\t"
    "ds_read_b64_tr_b16 a[136:137], v16 offset:1024\n\t"
    "v_cvt_pk_bf16_f32 v57, v58, v59\n\t"
    "v_mfma_f32_16x16x32_bf16 v[76:79], a[104:107], v[112:115], 0\n\t"
    "buffer_load_dwordx4 v3, s[40:43], s61 offen offset:1024 nt lds\n\t"
    "v_mfma_f32_16x16x32_bf16 v[84:87], a[104:107], v[128:131], 0\n\t"
    "v_exp_f32 v60, v60\n\t"
    "v_mfma_f32_16x16x32_bf16 v[68:71], a[108:111], v[100:103], v[68:71]\n\t"
    "ds_read_b64_tr_b16 a[138:139], v16 offset:5120\n\t"
    "ds_read_b64_tr_b16 a[140:141], v17 offset:1024\n\t"
    "v_mfma_f32_16x16x32_bf16 v[76:79], a[108:111], v[116:119], v[76:79]\n\t"
    "v_exp_f32 v61, v61\n\t"
    "v_mfma_f32_16x16x32_bf16 v[84:87], a[108:111], v[132:135], v[84:87]\n\t"
    "ds_read_b64_tr_b16 a[142:143], v17 offset:5120\n\t"
    "v_cvt_pk_bf16_f32 v58, v60, v61\n\t"
    "v_mfma_f32_16x16x32_bf16 v[68:71], a[112:115], v[104:107], v[68:71]\n\t"
    "v_exp_f32 v62, v62\n\t"
    "v_mfma_f32_16x16x32_bf16 v[76:79], a[112:115], v[120:123], v[76:79]\n\t"
    "ds_read_b64_tr_b16 a[144:145], v16 offset:1536\n\t"
    "ds_read_b64_tr_b16 a[146:147], v16 offset:5632\n\t"
    "v_mfma_f32_16x16x32_bf16 v[84:87], a[112:115], v[136:139], v[84:87]\n\t"
    "buffer_load_dwordx4 v4, s[40:43], s61 offen offset:2048 nt lds\n\t"
    "v_mfma_f32_16x16x32_bf16 v[68:71], a[116:119], v[108:111], v[68:71]\n\t"
    "ds_read_b64_tr_b16 a[148:149], v17 offset:1536\n\t"
    "ds_read_b128 a[88:91], v10 offset:0\n\t"
    "v_mfma_f32_16x16x32_bf16 v[76:79], a[116:119], v[124:127], v[76:79]\n\t"
    "ds_read_b64_tr_b16 a[150:151], v17 offset:5632\n\t"
    "v_mfma_f32_16x16x32_bf16 v[84:87], a[116:119], v[140:143], v[84:87]\n\t"
    "s_waitcnt lgkmcnt(6)\n\t"
    "v_exp_f32 v63, v63\n\t"
    "v_mfma_f32_16x16x32_bf16 v[160:163], a[120:123], v[32:35], v[160:163]\n\t"
    "ds_read_b128 a[92:95], v11 offset:0\n\t"
    "v_cvt_pk_bf16_f32 v59, v62, v63\n\t"
    "v_mfma_f32_16x16x32_bf16 v[164:167], a[124:127], v[32:35], v[164:167]\n\t"
    "v_exp_f32 v64, v64\n\t"
    "v_mfma_f32_16x16x32_bf16 v[168:171], a[128:131], v[32:35], v[168:171]\n\t"
    "s_waitcnt lgkmcnt(6)\n\t"
    "buffer_load_dwordx4 v5, s[40:43], s61 offen offset:3072 nt lds\n\t"
    "v_mfma_f32_16x16x32_bf16 v[172:175], a[132:135], v[32:35], v[172:175]\n\t"
    "s_add_u32 s60, s54, 90112\n\t"
    "v_exp_f32 v65, v65\n\t"
    "v_mfma_f32_16x16x32_bf16 a[0:3], a[136:139], v[32:35], a[0:3]\n\t"
    "s_add_u32 m0, s53, 24576\n\t"
    "ds_read_b128 a[96:99], v12 offset:0\n\t"
    "v_cvt_pk_bf16_f32 v64, v64, v65\n\t"
    "v_mfma_f32_16x16x32_bf16 a[4:7], a[140:143], v[32:35], a[4:7]\n\t"
    "s_waitcnt lgkmcnt(3)\n\t"
    "v_exp_f32 v66, v66\n\t"
    "v_mfma_f32_16x16x32_bf16 a[8:11], a[144:147], v[32:35], a[8:11]\n\t"
    "s_waitcnt lgkmcnt(2)\n\t"
    "v_exp_f32 v67, v67\n\t"
    "v_mfma_f32_16x16x32_bf16 a[12:15], a[148:151], v[32:35], a[12:15]\n\t"
    "ds_read_b128 a[100:103], v13 offset:0\n\t"
    "v_cvt_pk_bf16_f32 v65, v66, v67\n\t"
    "v_mfma_f32_16x16x32_bf16 a[72:75], v[216:219], v[32:35], a[72:75]\n\t"
    "v_exp_f32 v68, v68\n\t"
    "v_mfma_f32_16x16x32_bf16 v[176:179], a[120:123], v[40:43], v[176:179]\n\t"
    "v_exp_f32 v69, v69\n\t"
    "v_mfma_f32_16x16x32_bf16 v[180:183], a[124:127], v[40:43], v[180:183]\n\t"
    "ds_read_b128 a[104:107], v10 offset:4096\n\t"
    "v_cvt_pk_bf16_f32 v66, v68, v69\n\t"
    "v_mfma_f32_16x16x32_bf16 v[184:187], a[128:131], v[40:43], v[184:187]\n\t"
    "v_exp_f32 v70, v70\n\t"
    "v_mfma_f32_16x16x32_bf16 v[188:191], a[132:135], v[40:43], v[188:191]\n\t"
    "v_exp_f32 v71, v71\n\t"
    "v_mfma_f32_16x16x32_bf16 a[16:19], a[136:139], v[40:43], a[16:19]\n\t"
    "ds_read_b128 a[108:111], v11 offset:4096\n\t"
    "v_cvt_pk_bf16_f32 v67, v70, v71\n\t"
    "v_mfma_f32_16x16x32_bf16 a[20:23], a[140:143], v[40:43], a[20:23]\n\t"
    "v_exp_f32 v72, v72\n\t"
    "v_mfma_f32_16x16x32_bf16 a[24:27], a[144:147], v[40:43], a[24:27]\n\t"
    "v_exp_f32 v73, v73\n\t"
    "v_mfma_f32_16x16x32_bf16 a[28:31], a[148:151], v[40:43], a[28:31]\n\t"
    "buffer_load_dwordx4 v6, s[44:47], s60 offen offset:0 nt lds\n\t"
    "v_mfma_f32_16x16x32_bf16 a[76:79], v[216:219], v[40:43], a[76:79]\n\t"
    "ds_read_b128 a[112:115], v12 offset:4096\n\t"
    "v_cvt_pk_bf16_f32 v72, v72, v73\n\t"
    "v_mfma_f32_16x16x32_bf16 v[192:195], a[120:123], v[48:51], v[192:195]\n\t"
    "v_exp_f32 v74, v74\n\t"
    "v_mfma_f32_16x16x32_bf16 v[196:199], a[124:127], v[48:51], v[196:199]\n\t"
    "v_exp_f32 v75, v75\n\t"
    "v_mfma_f32_16x16x32_bf16 v[200:203], a[128:131], v[48:51], v[200:203]\n\t"
    "ds_read_b128 a[116:119], v13 offset:4096\n\t"
    "v_cvt_pk_bf16_f32 v73, v74, v75\n\t"
    "v_mfma_f32_16x16x32_bf16 v[204:207], a[132:135], v[48:51], v[204:207]\n\t"
    "v_exp_f32 v76, v76\n\t"
    "v_mfma_f32_16x16x32_bf16 a[32:35], a[136:139], v[48:51], a[32:35]\n\t"
    "buffer_load_dwordx4 v7, s[44:47], s60 offen offset:1024 nt lds\n\t"
    "v_mfma_f32_16x16x32_bf16 a[36:39], a[140:143], v[48:51], a[36:39]\n\t"
    "v_exp_f32 v77, v77\n\t"
    "v_mfma_f32_16x16x32_bf16 a[40:43], a[144:147], v[48:51], a[40:43]\n\t"
    "v_exp_f32 v78, v78\n\t"
    "v_mfma_f32_16x16x32_bf16 a[44:47], a[148:151], v[48:51], a[44:47]\n\t"
    "v_exp_f32 v79, v79\n\t"
    "v_mfma_f32_16x16x32_bf16 a[80:83], v[216:219], v[48:51], a[80:83]\n\t"
    "v_cvt_pk_bf16_f32 v74, v76, v77\n\t"
    "v_cvt_pk_bf16_f32 v75, v78, v79\n\t"
    "v_mfma_f32_16x16x32_bf16 v[208:211], a[120:123], v[56:59], v[208:211]\n\t"
    "v_exp_f32 v80, v80\n\t"
    "v_mfma_f32_16x16x32_bf16 v[212:215], a[124:127], v[56:59], v[212:215]\n\t"
    "buffer_load_dwordx4 v8, s[44:47], s60 offen offset:2048 nt lds\n\t"
    "v_mfma_f32_16x16x32_bf16 a[48:51], a[128:131], v[56:59], a[48:51]\n\t"
    "v_exp_f32 v81, v81\n\t"
    "v_mfma_f32_16x16x32_bf16 a[52:55], a[132:135], v[56:59], a[52:55]\n\t"
    "v_exp_f32 v82, v82\n\t"
    "v_mfma_f32_16x16x32_bf16 a[56:59], a[136:139], v[56:59], a[56:59]\n\t"
    "v_exp_f32 v83, v83\n\t"
    "v_mfma_f32_16x16x32_bf16 a[60:63], a[140:143], v[56:59], a[60:63]\n\t"
    "v_exp_f32 v84, v84\n\t"
    "v_mfma_f32_16x16x32_bf16 a[64:67], a[144:147], v[56:59], a[64:67]\n\t"
    "v_exp_f32 v85, v85\n\t"
    "v_mfma_f32_16x16x32_bf16 a[68:71], a[148:151], v[56:59], a[68:71]\n\t"
    "s_waitcnt vmcnt(11) lgkmcnt(0)\n\t"
    "s_barrier\n\t"
    "v_mfma_f32_16x16x32_bf16 a[84:87], v[216:219], v[56:59], a[84:87]\n\t"
    "buffer_load_dwordx4 v9, s[44:47], s60 offen offset:3072 nt lds\n\t"
    "v_mfma_f32_16x16x32_bf16 v[32:35], a[88:91], v[96:99], 0\n\t"
    "s_add_u32 s61, s54, 106496\n\t"
    "ds_read_b64_tr_b16 a[120:121], v14 offset:8192\n\t"
    "v_cvt_pk_bf16_f32 v80, v80, v81\n\t"
    "v_mfma_f32_16x16x32_bf16 v[40:43], a[88:91], v[112:115], 0\n\t"
    "s_add_u32 m0, s52, 24576\n\t"
    "ds_read_b64_tr_b16 a[122:123], v14 offset:12288\n\t"
    "v_cvt_pk_bf16_f32 v81, v82, v83\n\t"
    "v_mfma_f32_16x16x32_bf16 v[48:51], a[88:91], v[128:131], 0\n\t"
    "v_exp_f32 v86, v86\n\t"
    "v_mfma_f32_16x16x32_bf16 v[56:59], a[88:91], v[144:147], 0\n\t"
    "ds_read_b64_tr_b16 a[124:125], v15 offset:8192\n\t"
    "v_cvt_pk_bf16_f32 v82, v84, v85\n\t"
    "v_mfma_f32_16x16x32_bf16 v[32:35], a[92:95], v[100:103], v[32:35]\n\t"
    "v_exp_f32 v87, v87\n\t"
    "v_mfma_f32_16x16x32_bf16 v[40:43], a[92:95], v[116:119], v[40:43]\n\t"
    "buffer_load_dwordx4 v2, s[40:43], s61 offen offset:0 nt lds\n\t"
    "v_mfma_f32_16x16x32_bf16 v[48:51], a[92:95], v[132:135], v[48:51]\n\t"
    "ds_read_b64_tr_b16 a[126:127], v15 offset:12288\n\t"
    "v_cvt_pk_bf16_f32 v83, v86, v87\n\t"
    "v_mfma_f32_16x16x32_bf16 v[56:59], a[92:95], v[148:151], v[56:59]\n\t"
    "ds_read_b64_tr_b16 a[128:129], v14 offset:8704\n\t"
    "v_mfma_f32_16x16x32_bf16 v[32:35], a[96:99], v[104:107], v[32:35]\n\t"
    "v_mfma_f32_16x16x32_bf16 v[40:43], a[96:99], v[120:123], v[40:43]\n\t"
    "ds_read_b64_tr_b16 a[130:131], v14 offset:12800\n\t"
    "v_mfma_f32_16x16x32_bf16 v[48:51], a[96:99], v[136:139], v[48:51]\n\t"
    "v_mfma_f32_16x16x32_bf16 v[56:59], a[96:99], v[152:155], v[56:59]\n\t"
    "buffer_load_dwordx4 v3, s[40:43], s61 offen offset:1024 nt lds\n\t"
    "v_mfma_f32_16x16x32_bf16 v[32:35], a[100:103], v[108:111], v[32:35]\n\t"
    "ds_read_b64_tr_b16 a[132:133], v15 offset:8704\n\t"
    "v_mfma_f32_16x16x32_bf16 v[40:43], a[100:103], v[124:127], v[40:43]\n\t"
    "ds_read_b64_tr_b16 a[134:135], v15 offset:12800\n\t"
    "v_mfma_f32_16x16x32_bf16 v[48:51], a[100:103], v[140:143], v[48:51]\n\t"
    "v_mfma_f32_16x16x32_bf16 v[56:59], a[100:103], v[156:159], v[56:59]\n\t"
    "ds_read_b64_tr_b16 a[136:137], v16 offset:9216\n\t"
    "v_mfma_f32_16x16x32_bf16 v[36:39], a[104:107], v[96:99], 0\n\t"
    "v_mfma_f32_16x16x32_bf16 v[44:47], a[104:107], v[112:115], 0\n\t"
    "buffer_load_dwordx4 v4, s[40:43], s61 offen offset:2048 nt lds\n\t"
    "v_mfma_f32_16x16x32_bf16 v[52:55], a[104:107], v[128:131], 0\n\t"
    "v_exp_f32 v32, v32\n\t"
    "v_mfma_f32_16x16x32_bf16 v[60:63], a[104:107], v[144:147], 0\n\t"
    "ds_read_b64_tr_b16 a[138:139], v16 offset:13312\n\t"
    "ds_read_b64_tr_b16 a[140:141], v17 offset:9216\n\t"
    "v_mfma_f32_16x16x32_bf16 v[36:39], a[108:111], v[100:103], v[36:39]\n\t"
    "v_exp_f32 v33, v33\n\t"
    "v_mfma_f32_16x16x32_bf16 v[44:47], a[108:111], v[116:119], v[44:47]\n\t"
    "ds_read_b64_tr_b16 a[142:143], v17 offset:13312\n\t"
    "v_cvt_pk_bf16_f32 v32, v32, v33\n\t"
    "v_mfma_f32_16x16x32_bf16 v[52:55], a[108:111], v[132:135], v[52:55]\n\t"
    "v_exp_f32 v34, v34\n\t"
    "v_mfma_f32_16x16x32_bf16 v[60:63], a[108:111], v[148:151], v[60:63]\n\t"
    "buffer_load_dwordx4 v5, s[40:43], s61 offen offset:3072 nt lds\n\t"
    "v_mfma_f32_16x16x32_bf16 v[36:39], a[112:115], v[104:107], v[36:39]\n\t"
    "s_add_u32 s60, s54, 98304\n\t"
    "v_exp_f32 v35, v35\n\t"
    "v_mfma_f32_16x16x32_bf16 v[44:47], a[112:115], v[120:123], v[44:47]\n\t"
    "s_add_u32 m0, s53, 32768\n\t"
    "ds_read_b64_tr_b16 a[144:145], v16 offset:9728\n\t"
    "ds_read_b64_tr_b16 a[146:147], v16 offset:13824\n\t"
    "v_mfma_f32_16x16x32_bf16 v[52:55], a[112:115], v[136:139], v[52:55]\n\t"
    "v_exp_f32 v40, v40\n\t"
    "v_mfma_f32_16x16x32_bf16 v[60:63], a[112:115], v[152:155], v[60:63]\n\t"
    "ds_read_b64_tr_b16 a[148:149], v17 offset:9728\n\t"
    "v_cvt_pk_bf16_f32 v33, v34, v35\n\t"
    "v_mfma_f32_16x16x32_bf16 v[36:39], a[116:119], v[108:111], v[36:39]\n\t"
    "v_exp_f32 v41, v41\n\t"
    "v_mfma_f32_16x16x32_bf16 v[44:47], a[116:119], v[124:127], v[44:47]\n\t"
    "ds_read_b64_tr_b16 a[150:151], v17 offset:13824\n\t"
    "ds_read_b128 a[88:91], v10 offset:8192\n\t"
    "v_mfma_f32_16x16x32_bf16 v[52:55], a[116:119], v[140:143], v[52:55]\n\t"
    "ds_read_b128 a[92:95], v11 offset:8192\n\t"
    "v_cvt_pk_bf16_f32 v40, v40, v41\n\t"
    "v_mfma_f32_16x16x32_bf16 v[60:63], a[116:119], v[156:159], v[60:63]\n\t"
    "s_waitcnt lgkmcnt(6)\n\t"
    "v_exp_f32 v42, v42\n\t"
    "v_mfma_f32_16x16x32_bf16 v[160:163], a[120:123], v[64:67], v[160:163]\n\t"
    "v_exp_f32 v43, v43\n\t"
    "v_mfma_f32_16x16x32_bf16 v[164:167], a[124:127], v[64:67], v[164:167]\n\t"
    "ds_read_b128 a[96:99], v12 offset:8192\n\t"
    "v_cvt_pk_bf16_f32 v41, v42, v43\n\t"
    "v_mfma_f32_16x16x32_bf16 v[168:171], a[128:131], v[64:67], v[168:171]\n\t"
    "v_exp_f32 v36, v36\n\t"
    "v_mfma_f32_16x16x32_bf16 v[172:175], a[132:135], v[64:67], v[172:175]\n\t"
    "v_exp_f32 v37, v37\n\t"
    "v_mfma_f32_16x16x32_bf16 a[0:3], a[136:139], v[64:67], a[0:3]\n\t"
    "ds_read_b128 a[100:103], v13 offset:8192\n\t"
    "v_cvt_pk_bf16_f32 v34, v36, v37\n\t"
    "v_mfma_f32_16x16x32_bf16 a[4:7], a[140:143], v[64:67], a[4:7]\n\t"
    "s_waitcnt lgkmcnt(3)\n\t"
    "v_exp_f32 v38, v38\n\t"
    "v_mfma_f32_16x16x32_bf16 a[8:11], a[144:147], v[64:67], a[8:11]\n\t"
    "v_exp_f32 v39, v39\n\t"
    "v_mfma_f32_16x16x32_bf16 a[12:15], a[148:151], v[64:67], a[12:15]\n\t"
    "ds_read_b128 a[104:107], v10 offset:12288\n\t"
    "v_cvt_pk_bf16_f32 v35, v38, v39\n\t"
    "v_mfma_f32_16x16x32_bf16 a[72:75], v[216:219], v[64:67], a[72:75]\n\t"
    "buffer_load_dwordx4 v6, s[44:47], s60 offen offset:0 nt lds\n\t"
    "v_mfma_f32_16x16x32_bf16 v[176:179], a[120:123], v[72:75], v[176:179]\n\t"
    "v_exp_f32 v44, v44\n\t"
    "v_mfma_f32_16x16x32_bf16 v[180:183], a[124:127], v[72:75], v[180:183]\n\t"
    "v_exp_f32 v45, v45\n\t"
    "v_mfma_f32_16x16x32_bf16 v[184:187], a[128:131], v[72:75], v[184:187]\n\t"
    "ds_read_b128 a[108:111], v11 offset:12288\n\t"
    "ds_read_b128 a[112:115], v12 offset:12288\n\t"
    "v_mfma_f32_16x16x32_bf16 v[188:191], a[132:135], v[72:75], v[188:191]\n\t"
    "v_exp_f32 v46, v46\n\t"
    "v_mfma_f32_16x16x32_bf16 a[16:19], a[136:139], v[72:75], a[16:19]\n\t"
    "ds_read_b128 a[116:119], v13 offset:12288\n\t"
    "v_cvt_pk_bf16_f32 v42, v44, v45\n\t"
    "v_mfma_f32_16x16x32_bf16 a[20:23], a[140:143], v[72:75], a[20:23]\n\t"
    "buffer_load_dwordx4 v7, s[44:47], s60 offen offset:1024 nt lds\n\t"
    "v_mfma_f32_16x16x32_bf16 a[24:27], a[144:147], v[72:75], a[24:27]\n\t"
    "v_exp_f32 v47, v47\n\t"
    "v_mfma_f32_16x16x32_bf16 a[28:31], a[148:151], v[72:75], a[28:31]\n\t"
    "v_exp_f32 v48, v48\n\t"
    "v_mfma_f32_16x16x32_bf16 a[76:79], v[216:219], v[72:75], a[76:79]\n\t"
    "v_exp_f32 v49, v49\n\t"
    "v_mfma_f32_16x16x32_bf16 v[192:195], a[120:123], v[80:83], v[192:195]\n\t"
    "v_cvt_pk_bf16_f32 v43, v46, v47\n\t"
    "v_cvt_pk_bf16_f32 v48, v48, v49\n\t"
    "v_mfma_f32_16x16x32_bf16 v[196:199], a[124:127], v[80:83], v[196:199]\n\t"
    "v_exp_f32 v50, v50\n\t"
    "v_mfma_f32_16x16x32_bf16 v[200:203], a[128:131], v[80:83], v[200:203]\n\t"
    "buffer_load_dwordx4 v8, s[44:47], s60 offen offset:2048 nt lds\n\t"
    "v_mfma_f32_16x16x32_bf16 v[204:207], a[132:135], v[80:83], v[204:207]\n\t"
    "v_exp_f32 v51, v51\n\t"
    "v_mfma_f32_16x16x32_bf16 a[32:35], a[136:139], v[80:83], a[32:35]\n\t"
    "v_exp_f32 v52, v52\n\t"
    "v_mfma_f32_16x16x32_bf16 a[36:39], a[140:143], v[80:83], a[36:39]\n\t"
    "v_exp_f32 v53, v53\n\t"
    "v_mfma_f32_16x16x32_bf16 a[40:43], a[144:147], v[80:83], a[40:43]\n\t"
    "v_cvt_pk_bf16_f32 v49, v50, v51\n\t"
    "v_cvt_pk_bf16_f32 v50, v52, v53\n\t"
    "v_mfma_f32_16x16x32_bf16 a[44:47], a[148:151], v[80:83], a[44:47]\n\t"
    "v_exp_f32 v54, v54\n\t"
    "v_mfma_f32_16x16x32_bf16 a[80:83], v[216:219], v[80:83], a[80:83]\n\t"
    "s_waitcnt vmcnt(11) lgkmcnt(0)\n\t"
    "s_barrier\n\t"
    "v_mfma_f32_16x16x32_bf16 v[64:67], a[88:91], v[96:99], 0\n\t"
    "buffer_load_dwordx4 v9, s[44:47], s60 offen offset:3072 nt lds\n\t"
    "v_mfma_f32_16x16x32_bf16 v[72:75], a[88:91], v[112:115], 0\n\t"
    "s_add_u32 s61, s54, 114688\n\t"
    "ds_read_b64_tr_b16 a[120:121], v14 offset:16384\n\t"
    "ds_read_b64_tr_b16 a[122:123], v14 offset:20480\n\t"
    "v_mfma_f32_16x16x32_bf16 v[80:83], a[88:91], v[128:131], 0\n\t"
    "s_add_u32 m0, s52, 32768\n\t"
    "v_exp_f32 v55, v55\n\t"
    "v_mfma_f32_16x16x32_bf16 v[64:67], a[92:95], v[100:103], v[64:67]\n\t"
    "ds_read_b64_tr_b16 a[124:125], v15 offset:16384\n\t"
    "v_cvt_pk_bf16_f32 v51, v54, v55\n\t"
    "v_mfma_f32_16x16x32_bf16 v[72:75], a[92:95], v[116:119], v[72:75]\n\t"
    "v_exp_f32 v56, v56\n\t"
    "v_mfma_f32_16x16x32_bf16 v[80:83], a[92:95], v[132:135], v[80:83]\n\t"
    "v_exp_f32 v57, v57\n\t"
    "v_mfma_f32_16x16x32_bf16 v[64:67], a[96:99], v[104:107], v[64:67]\n\t"
    "ds_read_b64_tr_b16 a[126:127], v15 offset:20480\n\t"
    "ds_read_b64_tr_b16 a[128:129], v14 offset:16896\n\t"
    "v_mfma_f32_16x16x32_bf16 v[72:75], a[96:99], v[120:123], v[72:75]\n\t"
    "buffer_load_dwordx4 v2, s[40:43], s61 offen offset:0 nt lds\n\t"
    "v_mfma_f32_16x16x32_bf16 v[80:83], a[96:99], v[136:139], v[80:83]\n\t"
    "ds_read_b64_tr_b16 a[130:131], v14 offset:20992\n\t"
    "v_cvt_pk_bf16_f32 v56, v56, v57\n\t"
    "v_mfma_f32_16x16x32_bf16 v[64:67], a[100:103], v[108:111], v[64:67]\n\t"
    "v_exp_f32 v58, v58\n\t"
    "v_mfma_f32_16x16x32_bf16 v[72:75], a[100:103], v[124:127], v[72:75]\n\t"
    "ds_read_b64_tr_b16 a[132:133], v15 offset:16896\n\t"
    "ds_read_b64_tr_b16 a[134:135], v15 offset:20992\n\t"
    "v_mfma_f32_16x16x32_bf16 v[80:83], a[100:103], v[140:143], v[80:83]\n\t"
    "v_exp_f32 v59, v59\n\t"
    "v_mfma_f32_16x16x32_bf16 v[68:71], a[104:107], v[96:99], 0\n\t"
    "ds_read_b64_tr_b16 a[136:137], v16 offset:17408\n\t"
    "v_cvt_pk_bf16_f32 v57, v58, v59\n\t"
    "v_mfma_f32_16x16x32_bf16 v[76:79], a[104:107], v[112:115], 0\n\t"
    "buffer_load_dwordx4 v3, s[40:43], s61 offen offset:1024 nt lds\n\t"
    "v_mfma_f32_16x16x32_bf16 v[84:87], a[104:107], v[128:131], 0\n\t"
    "v_exp_f32 v60, v60\n\t"
    "v_mfma_f32_16x16x32_bf16 v[68:71], a[108:111], v[100:103], v[68:71]\n\t"
    "ds_read_b64_tr_b16 a[138:139], v16 offset:21504\n\t"
    "ds_read_b64_tr_b16 a[140:141], v17 offset:17408\n\t"
    "v_mfma_f32_16x16x32_bf16 v[76:79], a[108:111], v[116:119], v[76:79]\n\t"
    "v_exp_f32 v61, v61\n\t"
    "v_mfma_f32_16x16x32_bf16 v[84:87], a[108:111], v[132:135], v[84:87]\n\t"
    "ds_read_b64_tr_b16 a[142:143], v17 offset:21504\n\t"
    "v_cvt_pk_bf16_f32 v58, v60, v61\n\t"
    "v_mfma_f32_16x16x32_bf16 v[68:71], a[112:115], v[104:107], v[68:71]\n\t"
    "v_exp_f32 v62, v62\n\t"
    "v_mfma_f32_16x16x32_bf16 v[76:79], a[112:115], v[120:123], v[76:79]\n\t"
    "ds_read_b64_tr_b16 a[144:145], v16 offset:17920\n\t"
    "ds_read_b64_tr_b16 a[146:147], v16 offset:22016\n\t"
    "v_mfma_f32_16x16x32_bf16 v[84:87], a[112:115], v[136:139], v[84:87]\n\t"
    "buffer_load_dwordx4 v4, s[40:43], s61 offen offset:2048 nt lds\n\t"
    "v_mfma_f32_16x16x32_bf16 v[68:71], a[116:119], v[108:111], v[68:71]\n\t"
    "ds_read_b64_tr_b16 a[148:149], v17 offset:17920\n\t"
    "ds_read_b128 a[88:91], v10 offset:16384\n\t"
    "v_mfma_f32_16x16x32_bf16 v[76:79], a[116:119], v[124:127], v[76:79]\n\t"
    "ds_read_b64_tr_b16 a[150:151], v17 offset:22016\n\t"
    "v_mfma_f32_16x16x32_bf16 v[84:87], a[116:119], v[140:143], v[84:87]\n\t"
    "s_waitcnt lgkmcnt(6)\n\t"
    "v_exp_f32 v63, v63\n\t"
    "v_mfma_f32_16x16x32_bf16 v[160:163], a[120:123], v[32:35], v[160:163]\n\t"
    "ds_read_b128 a[92:95], v11 offset:16384\n\t"
    "v_cvt_pk_bf16_f32 v59, v62, v63\n\t"
    "v_mfma_f32_16x16x32_bf16 v[164:167], a[124:127], v[32:35], v[164:167]\n\t"
    "v_exp_f32 v64, v64\n\t"
    "v_mfma_f32_16x16x32_bf16 v[168:171], a[128:131], v[32:35], v[168:171]\n\t"
    "s_waitcnt lgkmcnt(6)\n\t"
    "buffer_load_dwordx4 v5, s[40:43], s61 offen offset:3072 nt lds\n\t"
    "v_mfma_f32_16x16x32_bf16 v[172:175], a[132:135], v[32:35], v[172:175]\n\t"
    "s_add_u32 s60, s54, 106496\n\t"
    "v_exp_f32 v65, v65\n\t"
    "v_mfma_f32_16x16x32_bf16 a[0:3], a[136:139], v[32:35], a[0:3]\n\t"
    "s_add_u32 m0, s53, 0\n\t"
    "ds_read_b128 a[96:99], v12 offset:16384\n\t"
    "v_cvt_pk_bf16_f32 v64, v64, v65\n\t"
    "v_mfma_f32_16x16x32_bf16 a[4:7], a[140:143], v[32:35], a[4:7]\n\t"
    "s_waitcnt lgkmcnt(3)\n\t"
    "v_exp_f32 v66, v66\n\t"
    "v_mfma_f32_16x16x32_bf16 a[8:11], a[144:147], v[32:35], a[8:11]\n\t"
    "s_waitcnt lgkmcnt(2)\n\t"
    "v_exp_f32 v67, v67\n\t"
    "v_mfma_f32_16x16x32_bf16 a[12:15], a[148:151], v[32:35], a[12:15]\n\t"
    "ds_read_b128 a[100:103], v13 offset:16384\n\t"
    "v_cvt_pk_bf16_f32 v65, v66, v67\n\t"
    "v_mfma_f32_16x16x32_bf16 a[72:75], v[216:219], v[32:35], a[72:75]\n\t"
    "v_exp_f32 v68, v68\n\t"
    "v_mfma_f32_16x16x32_bf16 v[176:179], a[120:123], v[40:43], v[176:179]\n\t"
    "v_exp_f32 v69, v69\n\t"
    "v_mfma_f32_16x16x32_bf16 v[180:183], a[124:127], v[40:43], v[180:183]\n\t"
    "ds_read_b128 a[104:107], v10 offset:20480\n\t"
    "v_cvt_pk_bf16_f32 v66, v68, v69\n\t"
    "v_mfma_f32_16x16x32_bf16 v[184:187], a[128:131], v[40:43], v[184:187]\n\t"
    "v_exp_f32 v70, v70\n\t"
    "v_mfma_f32_16x16x32_bf16 v[188:191], a[132:135], v[40:43], v[188:191]\n\t"
    "v_exp_f32 v71, v71\n\t"
    "v_mfma_f32_16x16x32_bf16 a[16:19], a[136:139], v[40:43], a[16:19]\n\t"
    "ds_read_b128 a[108:111], v11 offset:20480\n\t"
    "v_cvt_pk_bf16_f32 v67, v70, v71\n\t"
    "v_mfma_f32_16x16x32_bf16 a[20:23], a[140:143], v[40:43], a[20:23]\n\t"
    "v_exp_f32 v72, v72\n\t"
    "v_mfma_f32_16x16x32_bf16 a[24:27], a[144:147], v[40:43], a[24:27]\n\t"
    "v_exp_f32 v73, v73\n\t"
    "v_mfma_f32_16x16x32_bf16 a[28:31], a[148:151], v[40:43], a[28:31]\n\t"
    "buffer_load_dwordx4 v6, s[44:47], s60 offen offset:0 nt lds\n\t"
    "v_mfma_f32_16x16x32_bf16 a[76:79], v[216:219], v[40:43], a[76:79]\n\t"
    "ds_read_b128 a[112:115], v12 offset:20480\n\t"
    "v_cvt_pk_bf16_f32 v72, v72, v73\n\t"
    "v_mfma_f32_16x16x32_bf16 v[192:195], a[120:123], v[48:51], v[192:195]\n\t"
    "v_exp_f32 v74, v74\n\t"
    "v_mfma_f32_16x16x32_bf16 v[196:199], a[124:127], v[48:51], v[196:199]\n\t"
    "v_exp_f32 v75, v75\n\t"
    "v_mfma_f32_16x16x32_bf16 v[200:203], a[128:131], v[48:51], v[200:203]\n\t"
    "ds_read_b128 a[116:119], v13 offset:20480\n\t"
    "v_cvt_pk_bf16_f32 v73, v74, v75\n\t"
    "v_mfma_f32_16x16x32_bf16 v[204:207], a[132:135], v[48:51], v[204:207]\n\t"
    "v_exp_f32 v76, v76\n\t"
    "v_mfma_f32_16x16x32_bf16 a[32:35], a[136:139], v[48:51], a[32:35]\n\t"
    "buffer_load_dwordx4 v7, s[44:47], s60 offen offset:1024 nt lds\n\t"
    "v_mfma_f32_16x16x32_bf16 a[36:39], a[140:143], v[48:51], a[36:39]\n\t"
    "v_exp_f32 v77, v77\n\t"
    "v_mfma_f32_16x16x32_bf16 a[40:43], a[144:147], v[48:51], a[40:43]\n\t"
    "v_exp_f32 v78, v78\n\t"
    "v_mfma_f32_16x16x32_bf16 a[44:47], a[148:151], v[48:51], a[44:47]\n\t"
    "v_exp_f32 v79, v79\n\t"
    "v_mfma_f32_16x16x32_bf16 a[80:83], v[216:219], v[48:51], a[80:83]\n\t"
    "v_cvt_pk_bf16_f32 v74, v76, v77\n\t"
    "v_cvt_pk_bf16_f32 v75, v78, v79\n\t"
    "v_mfma_f32_16x16x32_bf16 v[208:211], a[120:123], v[56:59], v[208:211]\n\t"
    "v_exp_f32 v80, v80\n\t"
    "v_mfma_f32_16x16x32_bf16 v[212:215], a[124:127], v[56:59], v[212:215]\n\t"
    "buffer_load_dwordx4 v8, s[44:47], s60 offen offset:2048 nt lds\n\t"
    "v_mfma_f32_16x16x32_bf16 a[48:51], a[128:131], v[56:59], a[48:51]\n\t"
    "v_exp_f32 v81, v81\n\t"
    "v_mfma_f32_16x16x32_bf16 a[52:55], a[132:135], v[56:59], a[52:55]\n\t"
    "v_exp_f32 v82, v82\n\t"
    "v_mfma_f32_16x16x32_bf16 a[56:59], a[136:139], v[56:59], a[56:59]\n\t"
    "v_exp_f32 v83, v83\n\t"
    "v_mfma_f32_16x16x32_bf16 a[60:63], a[140:143], v[56:59], a[60:63]\n\t"
    "v_exp_f32 v84, v84\n\t"
    "v_mfma_f32_16x16x32_bf16 a[64:67], a[144:147], v[56:59], a[64:67]\n\t"
    "v_exp_f32 v85, v85\n\t"
    "v_mfma_f32_16x16x32_bf16 a[68:71], a[148:151], v[56:59], a[68:71]\n\t"
    "s_waitcnt vmcnt(11) lgkmcnt(0)\n\t"
    "s_barrier\n\t"
    "v_mfma_f32_16x16x32_bf16 a[84:87], v[216:219], v[56:59], a[84:87]\n\t"
    "buffer_load_dwordx4 v9, s[44:47], s60 offen offset:3072 nt lds\n\t"
    "v_mfma_f32_16x16x32_bf16 v[32:35], a[88:91], v[96:99], 0\n\t"
    "s_add_u32 s61, s54, 122880\n\t"
    "ds_read_b64_tr_b16 a[120:121], v14 offset:24576\n\t"
    "v_cvt_pk_bf16_f32 v80, v80, v81\n\t"
    "v_mfma_f32_16x16x32_bf16 v[40:43], a[88:91], v[112:115], 0\n\t"
    "s_add_u32 m0, s52, 0\n\t"
    "ds_read_b64_tr_b16 a[122:123], v14 offset:28672\n\t"
    "v_cvt_pk_bf16_f32 v81, v82, v83\n\t"
    "v_mfma_f32_16x16x32_bf16 v[48:51], a[88:91], v[128:131], 0\n\t"
    "v_exp_f32 v86, v86\n\t"
    "v_mfma_f32_16x16x32_bf16 v[56:59], a[88:91], v[144:147], 0\n\t"
    "ds_read_b64_tr_b16 a[124:125], v15 offset:24576\n\t"
    "v_cvt_pk_bf16_f32 v82, v84, v85\n\t"
    "v_mfma_f32_16x16x32_bf16 v[32:35], a[92:95], v[100:103], v[32:35]\n\t"
    "v_exp_f32 v87, v87\n\t"
    "v_mfma_f32_16x16x32_bf16 v[40:43], a[92:95], v[116:119], v[40:43]\n\t"
    "buffer_load_dwordx4 v2, s[40:43], s61 offen offset:0 nt lds\n\t"
    "v_mfma_f32_16x16x32_bf16 v[48:51], a[92:95], v[132:135], v[48:51]\n\t"
    "ds_read_b64_tr_b16 a[126:127], v15 offset:28672\n\t"
    "v_cvt_pk_bf16_f32 v83, v86, v87\n\t"
    "v_mfma_f32_16x16x32_bf16 v[56:59], a[92:95], v[148:151], v[56:59]\n\t"
    "ds_read_b64_tr_b16 a[128:129], v14 offset:25088\n\t"
    "v_mfma_f32_16x16x32_bf16 v[32:35], a[96:99], v[104:107], v[32:35]\n\t"
    "v_mfma_f32_16x16x32_bf16 v[40:43], a[96:99], v[120:123], v[40:43]\n\t"
    "ds_read_b64_tr_b16 a[130:131], v14 offset:29184\n\t"
    "v_mfma_f32_16x16x32_bf16 v[48:51], a[96:99], v[136:139], v[48:51]\n\t"
    "v_mfma_f32_16x16x32_bf16 v[56:59], a[96:99], v[152:155], v[56:59]\n\t"
    "buffer_load_dwordx4 v3, s[40:43], s61 offen offset:1024 nt lds\n\t"
    "v_mfma_f32_16x16x32_bf16 v[32:35], a[100:103], v[108:111], v[32:35]\n\t"
    "ds_read_b64_tr_b16 a[132:133], v15 offset:25088\n\t"
    "v_mfma_f32_16x16x32_bf16 v[40:43], a[100:103], v[124:127], v[40:43]\n\t"
    "ds_read_b64_tr_b16 a[134:135], v15 offset:29184\n\t"
    "v_mfma_f32_16x16x32_bf16 v[48:51], a[100:103], v[140:143], v[48:51]\n\t"
    "v_mfma_f32_16x16x32_bf16 v[56:59], a[100:103], v[156:159], v[56:59]\n\t"
    "ds_read_b64_tr_b16 a[136:137], v16 offset:25600\n\t"
    "v_mfma_f32_16x16x32_bf16 v[36:39], a[104:107], v[96:99], 0\n\t"
    "v_mfma_f32_16x16x32_bf16 v[44:47], a[104:107], v[112:115], 0\n\t"
    "buffer_load_dwordx4 v4, s[40:43], s61 offen offset:2048 nt lds\n\t"
    "v_mfma_f32_16x16x32_bf16 v[52:55], a[104:107], v[128:131], 0\n\t"
    "v_exp_f32 v32, v32\n\t"
    "v_mfma_f32_16x16x32_bf16 v[60:63], a[104:107], v[144:147], 0\n\t"
    "ds_read_b64_tr_b16 a[138:139], v16 offset:29696\n\t"
    "ds_read_b64_tr_b16 a[140:141], v17 offset:25600\n\t"
    "v_mfma_f32_16x16x32_bf16 v[36:39], a[108:111], v[100:103], v[36:39]\n\t"
    "v_exp_f32 v33, v33\n\t"
    "v_mfma_f32_16x16x32_bf16 v[44:47], a[108:111], v[116:119], v[44:47]\n\t"
    "ds_read_b64_tr_b16 a[142:143], v17 offset:29696\n\t"
    "v_cvt_pk_bf16_f32 v32, v32, v33\n\t"
    "v_mfma_f32_16x16x32_bf16 v[52:55], a[108:111], v[132:135], v[52:55]\n\t"
    "v_exp_f32 v34, v34\n\t"
    "v_mfma_f32_16x16x32_bf16 v[60:63], a[108:111], v[148:151], v[60:63]\n\t"
    "buffer_load_dwordx4 v5, s[40:43], s61 offen offset:3072 nt lds\n\t"
    "v_mfma_f32_16x16x32_bf16 v[36:39], a[112:115], v[104:107], v[36:39]\n\t"
    "s_add_u32 s60, s54, 114688\n\t"
    "v_exp_f32 v35, v35\n\t"
    "v_mfma_f32_16x16x32_bf16 v[44:47], a[112:115], v[120:123], v[44:47]\n\t"
    "s_add_u32 m0, s53, 8192\n\t"
    "ds_read_b64_tr_b16 a[144:145], v16 offset:26112\n\t"
    "ds_read_b64_tr_b16 a[146:147], v16 offset:30208\n\t"
    "v_mfma_f32_16x16x32_bf16 v[52:55], a[112:115], v[136:139], v[52:55]\n\t"
    "v_exp_f32 v40, v40\n\t"
    "v_mfma_f32_16x16x32_bf16 v[60:63], a[112:115], v[152:155], v[60:63]\n\t"
    "ds_read_b64_tr_b16 a[148:149], v17 offset:26112\n\t"
    "v_cvt_pk_bf16_f32 v33, v34, v35\n\t"
    "v_mfma_f32_16x16x32_bf16 v[36:39], a[116:119], v[108:111], v[36:39]\n\t"
    "v_exp_f32 v41, v41\n\t"
    "v_mfma_f32_16x16x32_bf16 v[44:47], a[116:119], v[124:127], v[44:47]\n\t"
    "ds_read_b64_tr_b16 a[150:151], v17 offset:30208\n\t"
    "ds_read_b128 a[88:91], v10 offset:24576\n\t"
    "v_mfma_f32_16x16x32_bf16 v[52:55], a[116:119], v[140:143], v[52:55]\n\t"
    "ds_read_b128 a[92:95], v11 offset:24576\n\t"
    "v_cvt_pk_bf16_f32 v40, v40, v41\n\t"
    "v_mfma_f32_16x16x32_bf16 v[60:63], a[116:119], v[156:159], v[60:63]\n\t"
    "s_waitcnt lgkmcnt(6)\n\t"
    "v_exp_f32 v42, v42\n\t"
    "v_mfma_f32_16x16x32_bf16 v[160:163], a[120:123], v[64:67], v[160:163]\n\t"
    "v_exp_f32 v43, v43\n\t"
    "v_mfma_f32_16x16x32_bf16 v[164:167], a[124:127], v[64:67], v[164:167]\n\t"
    "ds_read_b128 a[96:99], v12 offset:24576\n\t"
    "v_cvt_pk_bf16_f32 v41, v42, v43\n\t"
    "v_mfma_f32_16x16x32_bf16 v[168:171], a[128:131], v[64:67], v[168:171]\n\t"
    "v_exp_f32 v36, v36\n\t"
    "v_mfma_f32_16x16x32_bf16 v[172:175], a[132:135], v[64:67], v[172:175]\n\t"
    "v_exp_f32 v37, v37\n\t"
    "v_mfma_f32_16x16x32_bf16 a[0:3], a[136:139], v[64:67], a[0:3]\n\t"
    "ds_read_b128 a[100:103], v13 offset:24576\n\t"
    "v_cvt_pk_bf16_f32 v34, v36, v37\n\t"
    "v_mfma_f32_16x16x32_bf16 a[4:7], a[140:143], v[64:67], a[4:7]\n\t"
    "s_waitcnt lgkmcnt(3)\n\t"
    "v_exp_f32 v38, v38\n\t"
    "v_mfma_f32_16x16x32_bf16 a[8:11], a[144:147], v[64:67], a[8:11]\n\t"
    "v_exp_f32 v39, v39\n\t"
    "v_mfma_f32_16x16x32_bf16 a[12:15], a[148:151], v[64:67], a[12:15]\n\t"
    "ds_read_b128 a[104:107], v10 offset:28672\n\t"
    "v_cvt_pk_bf16_f32 v35, v38, v39\n\t"
    "v_mfma_f32_16x16x32_bf16 a[72:75], v[216:219], v[64:67], a[72:75]\n\t"
    "buffer_load_dwordx4 v6, s[44:47], s60 offen offset:0 nt lds\n\t"
    "v_mfma_f32_16x16x32_bf16 v[176:179], a[120:123], v[72:75], v[176:179]\n\t"
    "v_exp_f32 v44, v44\n\t"
    "v_mfma_f32_16x16x32_bf16 v[180:183], a[124:127], v[72:75], v[180:183]\n\t"
    "v_exp_f32 v45, v45\n\t"
    "v_mfma_f32_16x16x32_bf16 v[184:187], a[128:131], v[72:75], v[184:187]\n\t"
    "ds_read_b128 a[108:111], v11 offset:28672\n\t"
    "ds_read_b128 a[112:115], v12 offset:28672\n\t"
    "v_mfma_f32_16x16x32_bf16 v[188:191], a[132:135], v[72:75], v[188:191]\n\t"
    "v_exp_f32 v46, v46\n\t"
    "v_mfma_f32_16x16x32_bf16 a[16:19], a[136:139], v[72:75], a[16:19]\n\t"
    "ds_read_b128 a[116:119], v13 offset:28672\n\t"
    "v_cvt_pk_bf16_f32 v42, v44, v45\n\t"
    "v_mfma_f32_16x16x32_bf16 a[20:23], a[140:143], v[72:75], a[20:23]\n\t"
    "buffer_load_dwordx4 v7, s[44:47], s60 offen offset:1024 nt lds\n\t"
    "v_mfma_f32_16x16x32_bf16 a[24:27], a[144:147], v[72:75], a[24:27]\n\t"
    "v_exp_f32 v47, v47\n\t"
    "v_mfma_f32_16x16x32_bf16 a[28:31], a[148:151], v[72:75], a[28:31]\n\t"
    "v_exp_f32 v48, v48\n\t"
    "v_mfma_f32_16x16x32_bf16 a[76:79], v[216:219], v[72:75], a[76:79]\n\t"
    "v_exp_f32 v49, v49\n\t"
    "v_mfma_f32_16x16x32_bf16 v[192:195], a[120:123], v[80:83], v[192:195]\n\t"
    "v_cvt_pk_bf16_f32 v43, v46, v47\n\t"
    "v_cvt_pk_bf16_f32 v48, v48, v49\n\t"
    "v_mfma_f32_16x16x32_bf16 v[196:199], a[124:127], v[80:83], v[196:199]\n\t"
    "v_exp_f32 v50, v50\n\t"
    "v_mfma_f32_16x16x32_bf16 v[200:203], a[128:131], v[80:83], v[200:203]\n\t"
    "buffer_load_dwordx4 v8, s[44:47], s60 offen offset:2048 nt lds\n\t"
    "v_mfma_f32_16x16x32_bf16 v[204:207], a[132:135], v[80:83], v[204:207]\n\t"
    "v_exp_f32 v51, v51\n\t"
    "v_mfma_f32_16x16x32_bf16 a[32:35], a[136:139], v[80:83], a[32:35]\n\t"
    "v_exp_f32 v52, v52\n\t"
    "v_mfma_f32_16x16x32_bf16 a[36:39], a[140:143], v[80:83], a[36:39]\n\t"
    "v_exp_f32 v53, v53\n\t"
    "v_mfma_f32_16x16x32_bf16 a[40:43], a[144:147], v[80:83], a[40:43]\n\t"
    "v_cvt_pk_bf16_f32 v49, v50, v51\n\t"
    "v_cvt_pk_bf16_f32 v50, v52, v53\n\t"
    "v_mfma_f32_16x16x32_bf16 a[44:47], a[148:151], v[80:83], a[44:47]\n\t"
    "v_exp_f32 v54, v54\n\t"
    "v_mfma_f32_16x16x32_bf16 a[80:83], v[216:219], v[80:83], a[80:83]\n\t"
    "s_waitcnt vmcnt(11) lgkmcnt(0)\n\t"
    "s_barrier\n\t"
    "v_mfma_f32_16x16x32_bf16 v[64:67], a[88:91], v[96:99], 0\n\t"
    "buffer_load_dwordx4 v9, s[44:47], s60 offen offset:3072 nt lds\n\t"
    "v_mfma_f32_16x16x32_bf16 v[72:75], a[88:91], v[112:115], 0\n\t"
    "s_add_u32 s61, s54, 122880\n\t"
    "ds_read_b64_tr_b16 a[120:121], v14 offset:32768\n\t"
    "ds_read_b64_tr_b16 a[122:123], v14 offset:36864\n\t"
    "v_mfma_f32_16x16x32_bf16 v[80:83], a[88:91], v[128:131], 0\n\t"
    "s_add_u32 m0, s53, 16384\n\t"
    "v_exp_f32 v55, v55\n\t"
    "v_mfma_f32_16x16x32_bf16 v[64:67], a[92:95], v[100:103], v[64:67]\n\t"
    "ds_read_b64_tr_b16 a[124:125], v15 offset:32768\n\t"
    "v_cvt_pk_bf16_f32 v51, v54, v55\n\t"
    "v_mfma_f32_16x16x32_bf16 v[72:75], a[92:95], v[116:119], v[72:75]\n\t"
    "v_exp_f32 v56, v56\n\t"
    "v_mfma_f32_16x16x32_bf16 v[80:83], a[92:95], v[132:135], v[80:83]\n\t"
    "v_exp_f32 v57, v57\n\t"
    "v_mfma_f32_16x16x32_bf16 v[64:67], a[96:99], v[104:107], v[64:67]\n\t"
    "ds_read_b64_tr_b16 a[126:127], v15 offset:36864\n\t"
    "ds_read_b64_tr_b16 a[128:129], v14 offset:33280\n\t"
    "v_mfma_f32_16x16x32_bf16 v[72:75], a[96:99], v[120:123], v[72:75]\n\t"
    "ds_read_b64_tr_b16 a[130:131], v14 offset:37376\n\t"
    "v_cvt_pk_bf16_f32 v56, v56, v57\n\t"
    "v_mfma_f32_16x16x32_bf16 v[80:83], a[96:99], v[136:139], v[80:83]\n\t"
    "v_exp_f32 v58, v58\n\t"
    "v_mfma_f32_16x16x32_bf16 v[64:67], a[100:103], v[108:111], v[64:67]\n\t"
    "v_exp_f32 v59, v59\n\t"
    "v_mfma_f32_16x16x32_bf16 v[72:75], a[100:103], v[124:127], v[72:75]\n\t"
    "ds_read_b64_tr_b16 a[132:133], v15 offset:33280\n\t"
    "ds_read_b64_tr_b16 a[134:135], v15 offset:37376\n\t"
    "v_mfma_f32_16x16x32_bf16 v[80:83], a[100:103], v[140:143], v[80:83]\n\t"
    "v_exp_f32 v60, v60\n\t"
    "v_mfma_f32_16x16x32_bf16 v[68:71], a[104:107], v[96:99], 0\n\t"
    "ds_read_b64_tr_b16 a[136:137], v16 offset:33792\n\t"
    "v_cvt_pk_bf16_f32 v57, v58, v59\n\t"
    "v_mfma_f32_16x16x32_bf16 v[76:79], a[104:107], v[112:115], 0\n\t"
    "v_exp_f32 v61, v61\n\t"
    "v_mfma_f32_16x16x32_bf16 v[84:87], a[104:107], v[128:131], 0\n\t"
    "ds_read_b64_tr_b16 a[138:139], v16 offset:37888\n\t"
    "v_cvt_pk_bf16_f32 v58, v60, v61\n\t"
    "v_mfma_f32_16x16x32_bf16 v[68:71], a[108:111], v[100:103], v[68:71]\n\t"
    "v_exp_f32 v62, v62\n\t"
    "v_mfma_f32_16x16x32_bf16 v[76:79], a[108:111], v[116:119], v[76:79]\n\t"
    "ds_read_b64_tr_b16 a[140:141], v17 offset:33792\n\t"
    "ds_read_b64_tr_b16 a[142:143], v17 offset:37888\n\t"
    "v_mfma_f32_16x16x32_bf16 v[84:87], a[108:111], v[132:135], v[84:87]\n\t"
    "v_exp_f32 v63, v63\n\t"
    "v_mfma_f32_16x16x32_bf16 v[68:71], a[112:115], v[104:107], v[68:71]\n\t"
    "ds_read_b64_tr_b16 a[144:145], v16 offset:34304\n\t"
    "v_cvt_pk_bf16_f32 v59, v62, v63\n\t"
    "v_mfma_f32_16x16x32_bf16 v[76:79], a[112:115], v[120:123], v[76:79]\n\t"
    "v_exp_f32 v64, v64\n\t"
    "v_mfma_f32_16x16x32_bf16 v[84:87], a[112:115], v[136:139], v[84:87]\n\t"
    "ds_read_b64_tr_b16 a[146:147], v16 offset:38400\n\t"
    "ds_read_b128 a[88:91], v10 offset:32768\n\t"
    "v_mfma_f32_16x16x32_bf16 v[68:71], a[116:119], v[108:111], v[68:71]\n\t"
    "v_exp_f32 v65, v65\n\t"
    "v_mfma_f32_16x16x32_bf16 v[76:79], a[116:119], v[124:127], v[76:79]\n\t"
    "ds_read_b64_tr_b16 a[148:149], v17 offset:34304\n\t"
    "ds_read_b64_tr_b16 a[150:151], v17 offset:38400\n\t"
    "v_mfma_f32_16x16x32_bf16 v[84:87], a[116:119], v[140:143], v[84:87]\n\t"
    "s_waitcnt lgkmcnt(7)\n\t"
    "ds_read_b128 a[92:95], v11 offset:32768\n\t"
    "v_cvt_pk_bf16_f32 v64, v64, v65\n\t"
    "v_mfma_f32_16x16x32_bf16 v[160:163], a[120:123], v[32:35], v[160:163]\n\t"
    "v_exp_f32 v66, v66\n\t"
    "v_mfma_f32_16x16x32_bf16 v[164:167], a[124:127], v[32:35], v[164:167]\n\t"
    "v_exp_f32 v67, v67\n\t"
    "v_mfma_f32_16x16x32_bf16 v[168:171], a[128:131], v[32:35], v[168:171]\n\t"
    "v_exp_f32 v68, v68\n\t"
    "v_mfma_f32_16x16x32_bf16 v[172:175], a[132:135], v[32:35], v[172:175]\n\t"
    "ds_read_b128 a[96:99], v12 offset:32768\n\t"
    "v_cvt_pk_bf16_f32 v65, v66, v67\n\t"
    "v_mfma_f32_16x16x32_bf16 a[0:3], a[136:139], v[32:35], a[0:3]\n\t"
    "s_waitcnt lgkmcnt(4)\n\t"
    "v_exp_f32 v69, v69\n\t"
    "v_mfma_f32_16x16x32_bf16 a[4:7], a[140:143], v[32:35], a[4:7]\n\t"
    "v_exp_f32 v70, v70\n\t"
    "v_mfma_f32_16x16x32_bf16 a[8:11], a[144:147], v[32:35], a[8:11]\n\t"
    "s_waitcnt lgkmcnt(2)\n\t"
    "ds_read_b128 a[100:103], v13 offset:32768\n\t"
    "v_cvt_pk_bf16_f32 v66, v68, v69\n\t"
    "v_mfma_f32_16x16x32_bf16 a[12:15], a[148:151], v[32:35], a[12:15]\n\t"
    "v_exp_f32 v71, v71\n\t"
    "v_mfma_f32_16x16x32_bf16 a[72:75], v[216:219], v[32:35], a[72:75]\n\t"
    "v_exp_f32 v72, v72\n\t"
    "v_mfma_f32_16x16x32_bf16 v[176:179], a[120:123], v[40:43], v[176:179]\n\t"
    "v_exp_f32 v73, v73\n\t"
    "v_mfma_f32_16x16x32_bf16 v[180:183], a[124:127], v[40:43], v[180:183]\n\t"
    "ds_read_b128 a[104:107], v10 offset:36864\n\t"
    "v_cvt_pk_bf16_f32 v67, v70, v71\n\t"
    "v_mfma_f32_16x16x32_bf16 v[184:187], a[128:131], v[40:43], v[184:187]\n\t"
    "v_exp_f32 v74, v74\n\t"
    "v_mfma_f32_16x16x32_bf16 v[188:191], a[132:135], v[40:43], v[188:191]\n\t"
    "v_exp_f32 v75, v75\n\t"
    "v_mfma_f32_16x16x32_bf16 a[16:19], a[136:139], v[40:43], a[16:19]\n\t"
    "ds_read_b128 a[108:111], v11 offset:36864\n\t"
    "v_cvt_pk_bf16_f32 v72, v72, v73\n\t"
    "v_mfma_f32_16x16x32_bf16 a[20:23], a[140:143], v[40:43], a[20:23]\n\t"
    "v_exp_f32 v76, v76\n\t"
    "v_mfma_f32_16x16x32_bf16 a[24:27], a[144:147], v[40:43], a[24:27]\n\t"
    "v_exp_f32 v77, v77\n\t"
    "v_mfma_f32_16x16x32_bf16 a[28:31], a[148:151], v[40:43], a[28:31]\n\t"
    "buffer_load_dwordx4 v6, s[44:47], s61 offen offset:0 nt lds\n\t"
    "v_mfma_f32_16x16x32_bf16 a[76:79], v[216:219], v[40:43], a[76:79]\n\t"
    "ds_read_b128 a[112:115], v12 offset:36864\n\t"
    "v_cvt_pk_bf16_f32 v73, v74, v75\n\t"
    "v_mfma_f32_16x16x32_bf16 v[192:195], a[120:123], v[48:51], v[192:195]\n\t"
    "v_exp_f32 v78, v78\n\t"
    "v_mfma_f32_16x16x32_bf16 v[196:199], a[124:127], v[48:51], v[196:199]\n\t"
    "v_exp_f32 v79, v79\n\t"
    "v_mfma_f32_16x16x32_bf16 v[200:203], a[128:131], v[48:51], v[200:203]\n\t"
    "ds_read_b128 a[116:119], v13 offset:36864\n\t"
    "v_cvt_pk_bf16_f32 v74, v76, v77\n\t"
    "v_mfma_f32_16x16x32_bf16 v[204:207], a[132:135], v[48:51], v[204:207]\n\t"
    "v_exp_f32 v80, v80\n\t"
    "v_mfma_f32_16x16x32_bf16 a[32:35], a[136:139], v[48:51], a[32:35]\n\t"
    "buffer_load_dwordx4 v7, s[44:47], s61 offen offset:1024 nt lds\n\t"
    "v_mfma_f32_16x16x32_bf16 a[36:39], a[140:143], v[48:51], a[36:39]\n\t"
    "v_exp_f32 v81, v81\n\t"
    "v_mfma_f32_16x16x32_bf16 a[40:43], a[144:147], v[48:51], a[40:43]\n\t"
    "v_cvt_pk_bf16_f32 v75, v78, v79\n\t"
    "v_cvt_pk_bf16_f32 v80, v80, v81\n\t"
    "v_mfma_f32_16x16x32_bf16 a[44:47], a[148:151], v[48:51], a[44:47]\n\t"
    "v_exp_f32 v82, v82\n\t"
    "v_mfma_f32_16x16x32_bf16 a[80:83], v[216:219], v[48:51], a[80:83]\n\t"
    "v_exp_f32 v83, v83\n\t"
    "v_mfma_f32_16x16x32_bf16 v[208:211], a[120:123], v[56:59], v[208:211]\n\t"
    "v_exp_f32 v84, v84\n\t"
    "v_mfma_f32_16x16x32_bf16 v[212:215], a[124:127], v[56:59], v[212:215]\n\t"
    "buffer_load_dwordx4 v8, s[44:47], s61 offen offset:2048 nt lds\n\t"
    "v_mfma_f32_16x16x32_bf16 a[48:51], a[128:131], v[56:59], a[48:51]\n\t"
    "v_exp_f32 v85, v85\n\t"
    "v_mfma_f32_16x16x32_bf16 a[52:55], a[132:135], v[56:59], a[52:55]\n\t"
    "v_cvt_pk_bf16_f32 v81, v82, v83\n\t"
    "v_cvt_pk_bf16_f32 v82, v84, v85\n\t"
    "v_mfma_f32_16x16x32_bf16 a[56:59], a[136:139], v[56:59], a[56:59]\n\t"
    "v_exp_f32 v86, v86\n\t"
    "v_mfma_f32_16x16x32_bf16 a[60:63], a[140:143], v[56:59], a[60:63]\n\t"
    "v_exp_f32 v87, v87\n\t"
    "v_mfma_f32_16x16x32_bf16 a[64:67], a[144:147], v[56:59], a[64:67]\n\t"
    "v_cvt_pk_bf16_f32 v83, v86, v87\n\t"
    "v_mfma_f32_16x16x32_bf16 a[68:71], a[148:151], v[56:59], a[68:71]\n\t"
    "s_waitcnt vmcnt(7) lgkmcnt(0)\n\t"
    "s_barrier\n\t"
    "v_mfma_f32_16x16x32_bf16 a[84:87], v[216:219], v[56:59], a[84:87]\n\t"
    "buffer_load_dwordx4 v9, s[44:47], s61 offen offset:3072 nt lds\n\t"
    "v_mfma_f32_16x16x32_bf16 v[32:35], a[88:91], v[96:99], 0\n\t"
    "ds_read_b64_tr_b16 a[120:121], v14 offset:0\n\t"
    "v_mfma_f32_16x16x32_bf16 v[40:43], a[88:91], v[112:115], 0\n\t"
    "ds_read_b64_tr_b16 a[122:123], v14 offset:4096\n\t"
    "v_mfma_f32_16x16x32_bf16 v[48:51], a[88:91], v[128:131], 0\n\t"
    "v_mfma_f32_16x16x32_bf16 v[56:59], a[88:91], v[144:147], 0\n\t"
    "ds_read_b64_tr_b16 a[124:125], v15 offset:0\n\t"
    "v_mfma_f32_16x16x32_bf16 v[32:35], a[92:95], v[100:103], v[32:35]\n\t"
    "v_mfma_f32_16x16x32_bf16 v[40:43], a[92:95], v[116:119], v[40:43]\n\t"
    "ds_read_b64_tr_b16 a[126:127], v15 offset:4096\n\t"
    "v_mfma_f32_16x16x32_bf16 v[48:51], a[92:95], v[132:135], v[48:51]\n\t"
    "v_mfma_f32_16x16x32_bf16 v[56:59], a[92:95], v[148:151], v[56:59]\n\t"
    "ds_read_b64_tr_b16 a[128:129], v14 offset:512\n\t"
    "v_mfma_f32_16x16x32_bf16 v[32:35], a[96:99], v[104:107], v[32:35]\n\t"
    "v_mfma_f32_16x16x32_bf16 v[40:43], a[96:99], v[120:123], v[40:43]\n\t"
    "ds_read_b64_tr_b16 a[130:131], v14 offset:4608\n\t"
    "v_mfma_f32_16x16x32_bf16 v[48:51], a[96:99], v[136:139], v[48:51]\n\t"
    "v_mfma_f32_16x16x32_bf16 v[56:59], a[96:99], v[152:155], v[56:59]\n\t"
    "ds_read_b64_tr_b16 a[132:133], v15 offset:512\n\t"
    "v_mfma_f32_16x16x32_bf16 v[32:35], a[100:103], v[108:111], v[32:35]\n\t"
    "v_mfma_f32_16x16x32_bf16 v[40:43], a[100:103], v[124:127], v[40:43]\n\t"
    "ds_read_b64_tr_b16 a[134:135], v15 offset:4608\n\t"
    "v_mfma_f32_16x16x32_bf16 v[48:51], a[100:103], v[140:143], v[48:51]\n\t"
    "v_mfma_f32_16x16x32_bf16 v[56:59], a[100:103], v[156:159], v[56:59]\n\t"
    "ds_read_b64_tr_b16 a[136:137], v16 offset:1024\n\t"
    "v_mfma_f32_16x16x32_bf16 v[36:39], a[104:107], v[96:99], 0\n\t"
    "v_mfma_f32_16x16x32_bf16 v[44:47], a[104:107], v[112:115], 0\n\t"
    "v_exp_f32 v32, v32\n\t"
    "v_mfma_f32_16x16x32_bf16 v[52:55], a[104:107], v[128:131], 0\n\t"
    "v_exp_f32 v33, v33\n\t"
    "v_mfma_f32_16x16x32_bf16 v[60:63], a[104:107], v[144:147], 0\n\t"
    "ds_read_b64_tr_b16 a[138:139], v16 offset:5120\n\t"
    "ds_read_b64_tr_b16 a[140:141], v17 offset:1024\n\t"
    "v_mfma_f32_16x16x32_bf16 v[36:39], a[108:111], v[100:103], v[36:39]\n\t"
    "v_exp_f32 v34, v34\n\t"
    "v_mfma_f32_16x16x32_bf16 v[44:47], a[108:111], v[116:119], v[44:47]\n\t"
    "ds_read_b64_tr_b16 a[142:143], v17 offset:5120\n\t"
    "v_cvt_pk_bf16_f32 v32, v32, v33\n\t"
    "v_mfma_f32_16x16x32_bf16 v[52:55], a[108:111], v[132:135], v[52:55]\n\t"
    "v_exp_f32 v35, v35\n\t"
    "v_mfma_f32_16x16x32_bf16 v[60:63], a[108:111], v[148:151], v[60:63]\n\t"
    "ds_read_b64_tr_b16 a[144:145], v16 offset:1536\n\t"
    "v_cvt_pk_bf16_f32 v33, v34, v35\n\t"
    "v_mfma_f32_16x16x32_bf16 v[36:39], a[112:115], v[104:107], v[36:39]\n\t"
    "v_exp_f32 v40, v40\n\t"
    "v_mfma_f32_16x16x32_bf16 v[44:47], a[112:115], v[120:123], v[44:47]\n\t"
    "v_exp_f32 v41, v41\n\t"
    "v_mfma_f32_16x16x32_bf16 v[52:55], a[112:115], v[136:139], v[52:55]\n\t"
    "ds_read_b64_tr_b16 a[146:147], v16 offset:5632\n\t"
    "v_cvt_pk_bf16_f32 v40, v40, v41\n\t"
    "v_mfma_f32_16x16x32_bf16 v[60:63], a[112:115], v[152:155], v[60:63]\n\t"
    "v_exp_f32 v42, v42\n\t"
    "v_mfma_f32_16x16x32_bf16 v[36:39], a[116:119], v[108:111], v[36:39]\n\t"
    "ds_read_b64_tr_b16 a[148:149], v17 offset:1536\n\t"
    "ds_read_b128 a[88:91], v10 offset:0\n\t"
    "v_mfma_f32_16x16x32_bf16 v[44:47], a[116:119], v[124:127], v[44:47]\n\t"
    "v_exp_f32 v43, v43\n\t"
    "v_mfma_f32_16x16x32_bf16 v[52:55], a[116:119], v[140:143], v[52:55]\n\t"
    "ds_read_b64_tr_b16 a[150:151], v17 offset:5632\n\t"
    "ds_read_b128 a[92:95], v11 offset:0\n\t"
    "v_mfma_f32_16x16x32_bf16 v[60:63], a[116:119], v[156:159], v[60:63]\n\t"
    "s_waitcnt lgkmcnt(5)\n\t"
    "v_exp_f32 v48, v48\n\t"
    "v_mfma_f32_16x16x32_bf16 v[160:163], a[120:123], v[64:67], v[160:163]\n\t"
    "v_exp_f32 v49, v49\n\t"
    "v_mfma_f32_16x16x32_bf16 v[164:167], a[124:127], v[64:67], v[164:167]\n\t"
    "ds_read_b128 a[96:99], v12 offset:0\n\t"
    "v_cvt_pk_bf16_f32 v41, v42, v43\n\t"
    "v_mfma_f32_16x16x32_bf16 v[168:171], a[128:131], v[64:67], v[168:171]\n\t"
    "v_exp_f32 v36, v36\n\t"
    "v_mfma_f32_16x16x32_bf16 v[172:175], a[132:135], v[64:67], v[172:175]\n\t"
    "ds_read_b128 a[100:103], v13 offset:0\n\t"
    "v_cvt_pk_bf16_f32 v48, v48, v49\n\t"
    "v_mfma_f32_16x16x32_bf16 a[0:3], a[136:139], v[64:67], a[0:3]\n\t"
    "v_exp_f32 v37, v37\n\t"
    "v_mfma_f32_16x16x32_bf16 a[4:7], a[140:143], v[64:67], a[4:7]\n\t"
    "s_waitcnt lgkmcnt(4)\n\t"
    "v_exp_f32 v38, v38\n\t"
    "v_mfma_f32_16x16x32_bf16 a[8:11], a[144:147], v[64:67], a[8:11]\n\t"
    "s_waitcnt lgkmcnt(2)\n\t"
    "ds_read_b128 a[104:107], v10 offset:4096\n\t"
    "v_cvt_pk_bf16_f32 v34, v36, v37\n\t"
    "v_mfma_f32_16x16x32_bf16 a[12:15], a[148:151], v[64:67], a[12:15]\n\t"
    "v_exp_f32 v39, v39\n\t"
    "v_mfma_f32_16x16x32_bf16 a[72:75], v[216:219], v[64:67], a[72:75]\n\t"
    "ds_read_b128 a[108:111], v11 offset:4096\n\t"
    "v_cvt_pk_bf16_f32 v35, v38, v39\n\t"
    "v_mfma_f32_16x16x32_bf16 v[176:179], a[120:123], v[72:75], v[176:179]\n\t"
    "v_exp_f32 v44, v44\n\t"
    "v_mfma_f32_16x16x32_bf16 v[180:183], a[124:127], v[72:75], v[180:183]\n\t"
    "v_exp_f32 v45, v45\n\t"
    "v_mfma_f32_16x16x32_bf16 v[184:187], a[128:131], v[72:75], v[184:187]\n\t"
    "ds_read_b128 a[112:115], v12 offset:4096\n\t"
    "v_cvt_pk_bf16_f32 v42, v44, v45\n\t"
    "v_mfma_f32_16x16x32_bf16 v[188:191], a[132:135], v[72:75], v[188:191]\n\t"
    "v_exp_f32 v46, v46\n\t"
    "v_mfma_f32_16x16x32_bf16 a[16:19], a[136:139], v[72:75], a[16:19]\n\t"
    "v_exp_f32 v47, v47\n\t"
    "v_mfma_f32_16x16x32_bf16 a[20:23], a[140:143], v[72:75], a[20:23]\n\t"
    "ds_read_b128 a[116:119], v13 offset:4096\n\t"
    "v_cvt_pk_bf16_f32 v43, v46, v47\n\t"
    "v_mfma_f32_16x16x32_bf16 a[24:27], a[144:147], v[72:75], a[24:27]\n\t"
    "v_exp_f32 v50, v50\n\t"
    "v_mfma_f32_16x16x32_bf16 a[28:31], a[148:151], v[72:75], a[28:31]\n\t"
    "v_exp_f32 v51, v51\n\t"
    "v_mfma_f32_16x16x32_bf16 a[76:79], v[216:219], v[72:75], a[76:79]\n\t"
    "v_exp_f32 v52, v52\n\t"
    "v_mfma_f32_16x16x32_bf16 v[192:195], a[120:123], v[80:83], v[192:195]\n\t"
    "v_exp_f32 v53, v53\n\t"
    "v_mfma_f32_16x16x32_bf16 v[196:199], a[124:127], v[80:83], v[196:199]\n\t"
    "v_cvt_pk_bf16_f32 v49, v50, v51\n\t"
    "v_cvt_pk_bf16_f32 v50, v52, v53\n\t"
    "v_mfma_f32_16x16x32_bf16 v[200:203], a[128:131], v[80:83], v[200:203]\n\t"
    "v_exp_f32 v54, v54\n\t"
    "v_mfma_f32_16x16x32_bf16 v[204:207], a[132:135], v[80:83], v[204:207]\n\t"
    "v_exp_f32 v55, v55\n\t"
    "v_mfma_f32_16x16x32_bf16 a[32:35], a[136:139], v[80:83], a[32:35]\n\t"
    "v_exp_f32 v56, v56\n\t"
    "v_mfma_f32_16x16x32_bf16 a[36:39], a[140:143], v[80:83], a[36:39]\n\t"
    "v_exp_f32 v57, v57\n\t"
    "v_mfma_f32_16x16x32_bf16 a[40:43], a[144:147], v[80:83], a[40:43]\n\t"
    "v_cvt_pk_bf16_f32 v51, v54, v55\n\t"
    "v_cvt_pk_bf16_f32 v56, v56, v57\n\t"
    "v_mfma_f32_16x16x32_bf16 a[44:47], a[148:151], v[80:83], a[44:47]\n\t"
    "v_exp_f32 v58, v58\n\t"
    "v_mfma_f32_16x16x32_bf16 a[80:83], v[216:219], v[80:83], a[80:83]\n\t"
    "s_waitcnt vmcnt(0) lgkmcnt(0)\n\t"
    "s_barrier\n\t"
    "v_mfma_f32_16x16x32_bf16 v[64:67], a[88:91], v[96:99], 0\n\t"
    "v_exp_f32 v59, v59\n\t"
    "v_mfma_f32_16x16x32_bf16 v[68:71], a[104:107], v[96:99], 0\n\t"
    "ds_read_b64_tr_b16 a[120:121], v14 offset:8192\n\t"
    "ds_read_b64_tr_b16 a[122:123], v14 offset:12288\n\t"
    "v_mfma_f32_16x16x32_bf16 v[64:67], a[92:95], v[100:103], v[64:67]\n\t"
    "v_exp_f32 v60, v60\n\t"
    "v_mfma_f32_16x16x32_bf16 v[68:71], a[108:111], v[100:103], v[68:71]\n\t"
    "ds_read_b64_tr_b16 a[124:125], v15 offset:8192\n\t"
    "v_cvt_pk_bf16_f32 v57, v58, v59\n\t"
    "v_mfma_f32_16x16x32_bf16 v[64:67], a[96:99], v[104:107], v[64:67]\n\t"
    "v_exp_f32 v61, v61\n\t"
    "v_mfma_f32_16x16x32_bf16 v[68:71], a[112:115], v[104:107], v[68:71]\n\t"
    "ds_read_b64_tr_b16 a[126:127], v15 offset:12288\n\t"
    "v_cvt_pk_bf16_f32 v58, v60, v61\n\t"
    "v_mfma_f32_16x16x32_bf16 v[64:67], a[100:103], v[108:111], v[64:67]\n\t"
    "v_exp_f32 v62, v62\n\t"
    "v_mfma_f32_16x16x32_bf16 v[68:71], a[116:119], v[108:111], v[68:71]\n\t"
    "ds_read_b64_tr_b16 a[128:129], v14 offset:8704\n\t"
    "ds_read_b64_tr_b16 a[130:131], v14 offset:12800\n\t"
    "v_mfma_f32_16x16x32_bf16 v[72:75], a[88:91], v[112:115], 0\n\t"
    "v_exp_f32 v63, v63\n\t"
    "v_mfma_f32_16x16x32_bf16 v[76:79], a[104:107], v[112:115], 0\n\t"
    "ds_read_b64_tr_b16 a[132:133], v15 offset:8704\n\t"
    "v_cvt_pk_bf16_f32 v59, v62, v63\n\t"
    "v_mfma_f32_16x16x32_bf16 v[72:75], a[92:95], v[116:119], v[72:75]\n\t"
    "ds_read_b64_tr_b16 a[134:135], v15 offset:12800\n\t"
    "v_mfma_f32_16x16x32_bf16 v[76:79], a[108:111], v[116:119], v[76:79]\n\t"
    "v_exp_f32 v64, v64\n\t"
    "v_mfma_f32_16x16x32_bf16 v[72:75], a[96:99], v[120:123], v[72:75]\n\t"
    "v_exp_f32 v65, v65\n\t"
    "v_mfma_f32_16x16x32_bf16 v[76:79], a[112:115], v[120:123], v[76:79]\n\t"
    "ds_read_b64_tr_b16 a[136:137], v16 offset:9216\n\t"
    "ds_read_b64_tr_b16 a[138:139], v16 offset:13312\n\t"
    "v_mfma_f32_16x16x32_bf16 v[72:75], a[100:103], v[124:127], v[72:75]\n\t"
    "v_exp_f32 v66, v66\n\t"
    "v_mfma_f32_16x16x32_bf16 v[76:79], a[116:119], v[124:127], v[76:79]\n\t"
    "ds_read_b64_tr_b16 a[140:141], v17 offset:9216\n\t"
    "v_cvt_pk_bf16_f32 v64, v64, v65\n\t"
    "v_mfma_f32_16x16x32_bf16 v[80:83], a[88:91], v[128:131], 0\n\t"
    "ds_read_b64_tr_b16 a[142:143], v17 offset:13312\n\t"
    "ds_read_b64_tr_b16 a[152:153], v14 offset:16384\n\t"
    "v_mfma_f32_16x16x32_bf16 v[84:87], a[104:107], v[128:131], 0\n\t"
    "v_exp_f32 v67, v67\n\t"
    "v_mfma_f32_16x16x32_bf16 v[80:83], a[92:95], v[132:135], v[80:83]\n\t"
    "ds_read_b64_tr_b16 a[144:145], v16 offset:9728\n\t"
    "v_cvt_pk_bf16_f32 v65, v66, v67\n\t"
    "v_mfma_f32_16x16x32_bf16 v[84:87], a[108:111], v[132:135], v[84:87]\n\t"
    "ds_read_b64_tr_b16 a[146:147], v16 offset:13824\n\t"
    "ds_read_b64_tr_b16 a[154:155], v14 offset:20480\n\t"
    "v_mfma_f32_16x16x32_bf16 v[80:83], a[96:99], v[136:139], v[80:83]\n\t"
    "v_exp_f32 v68, v68\n\t"
    "v_mfma_f32_16x16x32_bf16 v[84:87], a[112:115], v[136:139], v[84:87]\n\t"
    "ds_read_b64_tr_b16 a[148:149], v17 offset:9728\n\t"
    "ds_read_b64_tr_b16 a[156:157], v15 offset:16384\n\t"
    "v_mfma_f32_16x16x32_bf16 v[80:83], a[100:103], v[140:143], v[80:83]\n\t"
    "ds_read_b64_tr_b16 a[150:151], v17 offset:13824\n\t"
    "ds_read_b64_tr_b16 a[158:159], v15 offset:20480\n\t"
    "v_mfma_f32_16x16x32_bf16 v[84:87], a[116:119], v[140:143], v[84:87]\n\t"
    "s_waitcnt lgkmcnt(9)\n\t"
    "v_exp_f32 v69, v69\n\t"
    "v_mfma_f32_16x16x32_bf16 v[160:163], a[120:123], v[32:35], v[160:163]\n\t"
    "v_exp_f32 v70, v70\n\t"
    "v_mfma_f32_16x16x32_bf16 v[164:167], a[124:127], v[32:35], v[164:167]\n\t"
    "v_exp_f32 v71, v71\n\t"
    "v_mfma_f32_16x16x32_bf16 v[168:171], a[128:131], v[32:35], v[168:171]\n\t"
    "v_cvt_pk_bf16_f32 v66, v68, v69\n\t"
    "v_cvt_pk_bf16_f32 v67, v70, v71\n\t"
    "v_mfma_f32_16x16x32_bf16 v[172:175], a[132:135], v[32:35], v[172:175]\n\t"
    "v_exp_f32 v72, v72\n\t"
    "v_mfma_f32_16x16x32_bf16 a[0:3], a[136:139], v[32:35], a[0:3]\n\t"
    "s_waitcnt lgkmcnt(4)\n\t"
    "v_exp_f32 v73, v73\n\t"
    "v_mfma_f32_16x16x32_bf16 a[4:7], a[140:143], v[32:35], a[4:7]\n\t"
    "v_exp_f32 v74, v74\n\t"
    "v_mfma_f32_16x16x32_bf16 a[8:11], a[144:147], v[32:35], a[8:11]\n\t"
    "s_waitcnt lgkmcnt(0)\n\t"
    "v_exp_f32 v75, v75\n\t"
    "v_mfma_f32_16x16x32_bf16 a[12:15], a[148:151], v[32:35], a[12:15]\n\t"
    "v_exp_f32 v80, v80\n\t"
    "v_mfma_f32_16x16x32_bf16 a[72:75], v[216:219], v[32:35], a[72:75]\n\t"
    "v_exp_f32 v81, v81\n\t"
    "v_mfma_f32_16x16x32_bf16 v[176:179], a[120:123], v[40:43], v[176:179]\n\t"
    "v_exp_f32 v76, v76\n\t"
    "v_mfma_f32_16x16x32_bf16 v[180:183], a[124:127], v[40:43], v[180:183]\n\t"
    "ds_read_b64_tr_b16 a[168:169], v16 offset:17408\n\t"
    "v_cvt_pk_bf16_f32 v72, v72, v73\n\t"
    "v_mfma_f32_16x16x32_bf16 v[184:187], a[128:131], v[40:43], v[184:187]\n\t"
    "v_exp_f32 v77, v77\n\t"
    "v_mfma_f32_16x16x32_bf16 v[188:191], a[132:135], v[40:43], v[188:191]\n\t"
    "ds_read_b64_tr_b16 a[170:171], v16 offset:21504\n\t"
    "v_cvt_pk_bf16_f32 v73, v74, v75\n\t"
    "v_mfma_f32_16x16x32_bf16 a[16:19], a[136:139], v[40:43], a[16:19]\n\t"
    "v_exp_f32 v82, v82\n\t"
    "v_mfma_f32_16x16x32_bf16 a[20:23], a[140:143], v[40:43], a[20:23]\n\t"
    "v_exp_f32 v83, v83\n\t"
    "v_mfma_f32_16x16x32_bf16 a[24:27], a[144:147], v[40:43], a[24:27]\n\t"
    "ds_read_b64_tr_b16 a[172:173], v17 offset:17408\n\t"
    "v_cvt_pk_bf16_f32 v74, v76, v77\n\t"
    "v_mfma_f32_16x16x32_bf16 a[28:31], a[148:151], v[40:43], a[28:31]\n\t"
    "v_exp_f32 v78, v78\n\t"
    "v_mfma_f32_16x16x32_bf16 a[76:79], v[216:219], v[40:43], a[76:79]\n\t"
    "ds_read_b64_tr_b16 a[174:175], v17 offset:21504\n\t"
    "v_cvt_pk_bf16_f32 v80, v80, v81\n\t"
    "v_mfma_f32_16x16x32_bf16 v[192:195], a[120:123], v[48:51], v[192:195]\n\t"
    "v_exp_f32 v79, v79\n\t"
    "v_mfma_f32_16x16x32_bf16 v[196:199], a[124:127], v[48:51], v[196:199]\n\t"
    "ds_read_b64_tr_b16 a[176:177], v16 offset:17920\n\t"
    "v_cvt_pk_bf16_f32 v75, v78, v79\n\t"
    "v_mfma_f32_16x16x32_bf16 v[200:203], a[128:131], v[48:51], v[200:203]\n\t"
    "v_exp_f32 v84, v84\n\t"
    "v_mfma_f32_16x16x32_bf16 v[204:207], a[132:135], v[48:51], v[204:207]\n\t"
    "v_exp_f32 v85, v85\n\t"
    "v_mfma_f32_16x16x32_bf16 a[32:35], a[136:139], v[48:51], a[32:35]\n\t"
    "ds_read_b64_tr_b16 a[178:179], v16 offset:22016\n\t"
    "v_cvt_pk_bf16_f32 v81, v82, v83\n\t"
    "v_mfma_f32_16x16x32_bf16 a[36:39], a[140:143], v[48:51], a[36:39]\n\t"
    "v_exp_f32 v86, v86\n\t"
    "v_mfma_f32_16x16x32_bf16 a[40:43], a[144:147], v[48:51], a[40:43]\n\t"
    "ds_read_b64_tr_b16 a[180:181], v17 offset:17920\n\t"
    "v_cvt_pk_bf16_f32 v82, v84, v85\n\t"
    "v_mfma_f32_16x16x32_bf16 a[44:47], a[148:151], v[48:51], a[44:47]\n\t"
    "v_exp_f32 v87, v87\n\t"
    "v_mfma_f32_16x16x32_bf16 a[80:83], v[216:219], v[48:51], a[80:83]\n\t"
    "ds_read_b64_tr_b16 a[182:183], v17 offset:22016\n\t"
    "v_cvt_pk_bf16_f32 v83, v86, v87\n\t"
    "v_mfma_f32_16x16x32_bf16 v[208:211], a[120:123], v[56:59], v[208:211]\n\t"
    "ds_read_b64_tr_b16 a[160:161], v14 offset:16896\n\t"
    "ds_read_b64_tr_b16 a[162:163], v14 offset:20992\n\t"
    "v_mfma_f32_16x16x32_bf16 v[212:215], a[124:127], v[56:59], v[212:215]\n\t"
    "ds_read_b64_tr_b16 a[164:165], v15 offset:16896\n\t"
    "ds_read_b64_tr_b16 a[166:167], v15 offset:20992\n\t"
    "v_mfma_f32_16x16x32_bf16 a[48:51], a[128:131], v[56:59], a[48:51]\n\t"
    "v_mfma_f32_16x16x32_bf16 a[52:55], a[132:135], v[56:59], a[52:55]\n\t"
    "v_mfma_f32_16x16x32_bf16 a[56:59], a[136:139], v[56:59], a[56:59]\n\t"
    "v_mfma_f32_16x16x32_bf16 a[60:63], a[140:143], v[56:59], a[60:63]\n\t"
    "ds_write_b128 v220, a[48:51] offset:12288\n\t"
    "v_mfma_f32_16x16x32_bf16 a[64:67], a[144:147], v[56:59], a[64:67]\n\t"
    "v_mfma_f32_16x16x32_bf16 a[68:71], a[148:151], v[56:59], a[68:71]\n\t"
    "ds_write_b128 v220, a[52:55] offset:13312\n\t"
    "v_mfma_f32_16x16x32_bf16 a[84:87], v[216:219], v[56:59], a[84:87]\n\t"
    "v_mfma_f32_16x16x32_bf16 a[72:75], v[216:219], v[64:67], a[72:75]\n\t"
    "s_waitcnt lgkmcnt(2)\n\t"
    "ds_write_b128 v220, a[56:59] offset:14336\n\t"
    "v_mfma_f32_16x16x32_bf16 a[0:3], a[168:171], v[64:67], a[0:3]\n\t"
    "v_mfma_f32_16x16x32_bf16 a[4:7], a[172:175], v[64:67], a[4:7]\n\t"
    "ds_write_b128 v220, a[60:63] offset:15360\n\t"
    "v_mfma_f32_16x16x32_bf16 a[8:11], a[176:179], v[64:67], a[8:11]\n\t"
    "ds_write_b32 v221, a84 offset:768\n\t"
    "ds_write_b32 v221, a72 offset:0\n\t"
    "v_mfma_f32_16x16x32_bf16 a[12:15], a[180:183], v[64:67], a[12:15]\n\t"
    "ds_write_b128 v220, a[64:67] offset:16384\n\t"
    "v_mfma_f32_16x16x32_bf16 a[76:79], v[216:219], v[72:75], a[76:79]\n\t"
    "v_mfma_f32_16x16x32_bf16 a[80:83], v[216:219], v[80:83], a[80:83]\n\t"
    "ds_write_b128 v220, a[68:71] offset:17408\n\t"
    "v_mfma_f32_16x16x32_bf16 a[16:19], a[168:171], v[72:75], a[16:19]\n\t"
    "v_mfma_f32_16x16x32_bf16 a[20:23], a[172:175], v[72:75], a[20:23]\n\t"
    "ds_write_b128 v220, a[0:3] offset:0\n\t"
    "v_mfma_f32_16x16x32_bf16 a[24:27], a[176:179], v[72:75], a[24:27]\n\t"
    "ds_write_b32 v221, a76 offset:256\n\t"
    "ds_write_b32 v221, a80 offset:512\n\t"
    "v_mfma_f32_16x16x32_bf16 a[28:31], a[180:183], v[72:75], a[28:31]\n\t"
    "ds_write_b128 v220, a[4:7] offset:1024\n\t"
    "v_mfma_f32_16x16x32_bf16 a[32:35], a[168:171], v[80:83], a[32:35]\n\t"
    "v_accvgpr_read_b32 v232, a72\n\t"
    "v_accvgpr_read_b32 v233, a76\n\t"
    "v_mfma_f32_16x16x32_bf16 a[36:39], a[172:175], v[80:83], a[36:39]\n\t"
    "ds_write_b128 v220, a[8:11] offset:2048\n\t"
    "v_mfma_f32_16x16x32_bf16 a[40:43], a[176:179], v[80:83], a[40:43]\n\t"
    "v_accvgpr_read_b32 v234, a80\n\t"
    "v_accvgpr_read_b32 v235, a84\n\t"
    "v_mfma_f32_16x16x32_bf16 a[44:47], a[180:183], v[80:83], a[44:47]\n\t"
    "s_waitcnt lgkmcnt(2)\n\t"
    "s_barrier\n\t"
    "v_mfma_f32_16x16x32_bf16 v[160:163], a[152:155], v[64:67], v[160:163]\n\t"
    "ds_write_b128 v220, a[12:15] offset:3072\n\t"
    "v_mfma_f32_16x16x32_bf16 v[176:179], a[152:155], v[72:75], v[176:179]\n\t"
    "ds_read_b32 v236, v226 offset:0\n\t"
    "ds_read_b32 v237, v226 offset:256\n\t"
    "ds_read_b32 v238, v226 offset:512\n\t"
    "v_mfma_f32_16x16x32_bf16 v[192:195], a[152:155], v[80:83], v[192:195]\n\t"
    "ds_write_b128 v220, a[16:19] offset:4096\n\t"
    "ds_read_b32 v239, v225 offset:768\n\t"
    "ds_read_b32 v240, v226 offset:768\n\t"
    "ds_read_b32 v241, v227 offset:768\n\t"
    "v_mfma_f32_16x16x32_bf16 v[164:167], a[156:159], v[64:67], v[164:167]\n\t"
    "v_mfma_f32_16x16x32_bf16 v[180:183], a[156:159], v[72:75], v[180:183]\n\t"
    "ds_write_b128 v220, a[20:23] offset:5120\n\t"
    "v_mfma_f32_16x16x32_bf16 v[196:199], a[156:159], v[80:83], v[196:199]\n\t"
    "v_mfma_f32_16x16x32_bf16 v[168:171], a[160:163], v[64:67], v[168:171]\n\t"
    "s_waitcnt lgkmcnt(1)\n\t"
    "ds_write_b128 v220, a[24:27] offset:6144\n\t"
    "v_mfma_f32_16x16x32_bf16 v[184:187], a[160:163], v[72:75], v[184:187]\n\t"
    "v_add_f32 v232, v232, v236\n\t"
    "v_add_f32 v233, v233, v237\n\t"
    "v_mfma_f32_16x16x32_bf16 v[200:203], a[160:163], v[80:83], v[200:203]\n\t"
    "ds_write_b128 v220, a[28:31] offset:7168\n\t"
    "v_mfma_f32_16x16x32_bf16 v[172:175], a[164:167], v[64:67], v[172:175]\n\t"
    "v_add_f32 v234, v234, v238\n\t"
    "v_add_f32 v235, v235, v239\n\t"
    "v_mfma_f32_16x16x32_bf16 v[188:191], a[164:167], v[72:75], v[188:191]\n\t"
    "ds_write_b128 v220, a[32:35] offset:8192\n\t"
    "v_mfma_f32_16x16x32_bf16 v[204:207], a[164:167], v[80:83], v[204:207]\n\t"
    "v_add_f32 v240, v240, v241\n\t"
    "s_mov_b32 s60, 0\n\t"
    "s_mov_b32 s61, 4096\n\t"
    "s_mov_b32 s62, 8192\n\t"
    "s_mov_b32 s63, 12288\n\t"
    "ds_write_b128 v220, a[36:39] offset:9216\n\t"
    "ds_write_b128 v220, a[40:43] offset:10240\n\t"
    "ds_write_b128 v220, a[44:47] offset:11264\n\t"
    "s_nop 3\n\t"
    "v_add_f32 v235, v235, v240\n\t"
    "v_rcp_f32 v244, v232\n\t"
    "v_rcp_f32 v246, v233\n\t"
    "v_rcp_f32 v248, v234\n\t"
    "v_rcp_f32 v250, v235\n\t"
    "s_nop 0\n\t"
    "v_mov_b32 v245, v244\n\t"
    "v_mov_b32 v247, v246\n\t"
    "v_mov_b32 v249, v248\n\t"
    "v_mov_b32 v251, v250\n\t"
    "v_pk_mul_f32 v[208:209], v[208:209], v[250:251]\n\t"
    "v_pk_mul_f32 v[210:211], v[210:211], v[250:251]\n\t"
    "v_pk_mul_f32 v[212:213], v[212:213], v[250:251]\n\t"
    "v_pk_mul_f32 v[214:215], v[214:215], v[250:251]\n\t"
    "v_pk_mul_f32 v[160:161], v[160:161], v[244:245]\n\t"
    "v_pk_mul_f32 v[162:163], v[162:163], v[244:245]\n\t"
    "v_pk_mul_f32 v[176:177], v[176:177], v[246:247]\n\t"
    "v_pk_mul_f32 v[178:179], v[178:179], v[246:247]\n\t"
    "v_pk_mul_f32 v[192:193], v[192:193], v[248:249]\n\t"
    "v_pk_mul_f32 v[194:195], v[194:195], v[248:249]\n\t"
    "v_pk_mul_f32 v[164:165], v[164:165], v[244:245]\n\t"
    "v_pk_mul_f32 v[166:167], v[166:167], v[244:245]\n\t"
    "v_pk_mul_f32 v[180:181], v[180:181], v[246:247]\n\t"
    "v_pk_mul_f32 v[182:183], v[182:183], v[246:247]\n\t"
    "v_pk_mul_f32 v[196:197], v[196:197], v[248:249]\n\t"
    "v_pk_mul_f32 v[198:199], v[198:199], v[248:249]\n\t"
    "v_pk_mul_f32 v[168:169], v[168:169], v[244:245]\n\t"
    "v_pk_mul_f32 v[170:171], v[170:171], v[244:245]\n\t"
    "v_pk_mul_f32 v[184:185], v[184:185], v[246:247]\n\t"
    "v_pk_mul_f32 v[186:187], v[186:187], v[246:247]\n\t"
    "v_pk_mul_f32 v[200:201], v[200:201], v[248:249]\n\t"
    "v_pk_mul_f32 v[202:203], v[202:203], v[248:249]\n\t"
    "v_pk_mul_f32 v[172:173], v[172:173], v[244:245]\n\t"
    "v_pk_mul_f32 v[174:175], v[174:175], v[244:245]\n\t"
    "v_pk_mul_f32 v[188:189], v[188:189], v[246:247]\n\t"
    "v_pk_mul_f32 v[190:191], v[190:191], v[246:247]\n\t"
    "v_pk_mul_f32 v[204:205], v[204:205], v[248:249]\n\t"
    "v_pk_mul_f32 v[206:207], v[206:207], v[248:249]\n\t"
    "s_waitcnt lgkmcnt(0)\n\t"
    "s_barrier\n\t"
    "ds_read_b128 v[32:35], v223 offset:0\n\t"
    "ds_read_b128 v[36:39], v223 offset:1024\n\t"
    "ds_read_b128 v[40:43], v223 offset:2048\n\t"
    "ds_read_b128 v[44:47], v223 offset:3072\n\t"
    "ds_read_b128 v[48:51], v223 offset:4096\n\t"
    "ds_read_b128 v[52:55], v223 offset:5120\n\t"
    "s_waitcnt lgkmcnt(4)\n\t"
    "v_pk_fma_f32 v[160:161], v[32:33], v[244:245], v[160:161]\n\t"
    "v_pk_fma_f32 v[162:163], v[34:35], v[244:245], v[162:163]\n\t"
    "ds_read_b128 v[56:59], v223 offset:6144\n\t"
    "ds_read_b128 v[60:63], v223 offset:7168\n\t"
    "v_pk_fma_f32 v[164:165], v[36:37], v[244:245], v[164:165]\n\t"
    "v_pk_fma_f32 v[166:167], v[38:39], v[244:245], v[166:167]\n\t"
    "v_cvt_pk_bf16_f32 v112, v160, v161\n\t"
    "ds_read_b128 v[64:67], v223 offset:8192\n\t"
    "ds_read_b128 v[68:71], v223 offset:9216\n\t"
    "v_cvt_pk_bf16_f32 v113, v162, v163\n\t"
    "v_cvt_pk_bf16_f32 v114, v164, v165\n\t"
    "v_cvt_pk_bf16_f32 v115, v166, v167\n\t"
    "ds_read_b128 v[72:75], v223 offset:10240\n\t"
    "ds_read_b128 v[76:79], v223 offset:11264\n\t"
    "s_waitcnt lgkmcnt(8)\n\t"
    "v_pk_fma_f32 v[168:169], v[40:41], v[244:245], v[168:169]\n\t"
    "v_pk_fma_f32 v[170:171], v[42:43], v[244:245], v[170:171]\n\t"
    "ds_read_b128 v[80:83], v222 offset:12288\n\t"
    "ds_read_b128 v[84:87], v222 offset:13312\n\t"
    "v_pk_fma_f32 v[172:173], v[44:45], v[244:245], v[172:173]\n\t"
    "v_pk_fma_f32 v[174:175], v[46:47], v[244:245], v[174:175]\n\t"
    "v_cvt_pk_bf16_f32 v116, v168, v169\n\t"
    "ds_read_b128 v[88:91], v223 offset:14336\n\t"
    "ds_read_b128 v[92:95], v223 offset:15360\n\t"
    "v_cvt_pk_bf16_f32 v117, v170, v171\n\t"
    "v_cvt_pk_bf16_f32 v118, v172, v173\n\t"
    "v_cvt_pk_bf16_f32 v119, v174, v175\n\t"
    "ds_read_b128 v[96:99], v224 offset:16384\n\t"
    "ds_read_b128 v[100:103], v224 offset:17408\n\t"
    "v_permlane16_swap_b32 v112, v114\n\t"
    "v_permlane16_swap_b32 v113, v115\n\t"
    "s_waitcnt lgkmcnt(12)\n\t"
    "v_pk_fma_f32 v[176:177], v[48:49], v[246:247], v[176:177]\n\t"
    "v_pk_fma_f32 v[178:179], v[50:51], v[246:247], v[178:179]\n\t"
    "v_pk_fma_f32 v[180:181], v[52:53], v[246:247], v[180:181]\n\t"
    "v_pk_fma_f32 v[182:183], v[54:55], v[246:247], v[182:183]\n\t"
    "v_permlane16_swap_b32 v116, v118\n\t"
    "v_permlane16_swap_b32 v117, v119\n\t"
    "v_cvt_pk_bf16_f32 v120, v176, v177\n\t"
    "v_cvt_pk_bf16_f32 v121, v178, v179\n\t"
    "v_cvt_pk_bf16_f32 v122, v180, v181\n\t"
    "v_cvt_pk_bf16_f32 v123, v182, v183\n\t"
    "s_waitcnt lgkmcnt(10)\n\t"
    "v_pk_fma_f32 v[184:185], v[56:57], v[246:247], v[184:185]\n\t"
    "v_pk_fma_f32 v[186:187], v[58:59], v[246:247], v[186:187]\n\t"
    "v_pk_fma_f32 v[188:189], v[60:61], v[246:247], v[188:189]\n\t"
    "v_pk_fma_f32 v[190:191], v[62:63], v[246:247], v[190:191]\n\t"
    "v_mov_b32 v140, v116\n\t"
    "v_mov_b32 v141, v117\n\t"
    "v_mov_b32 v142, v118\n\t"
    "v_mov_b32 v143, v119\n\t"
    "v_mov_b32_dpp v116, v112 row_ror:8 row_mask:0xf bank_mask:0x3\n\t"
    "v_mov_b32_dpp v117, v113 row_ror:8 row_mask:0xf bank_mask:0x3\n\t"
    "v_mov_b32_dpp v118, v114 row_ror:8 row_mask:0xf bank_mask:0x3\n\t"
    "v_mov_b32_dpp v119, v115 row_ror:8 row_mask:0xf bank_mask:0x3\n\t"
    "v_mov_b32_dpp v112, v140 row_ror:8 row_mask:0xf bank_mask:0xc\n\t"
    "v_mov_b32_dpp v113, v141 row_ror:8 row_mask:0xf bank_mask:0xc\n\t"
    "v_mov_b32_dpp v114, v142 row_ror:8 row_mask:0xf bank_mask:0xc\n\t"
    "v_mov_b32_dpp v115, v143 row_ror:8 row_mask:0xf bank_mask:0xc\n\t"
    "buffer_store_dwordx4 v[112:115], v229, s[48:51], s60 offen offset:0 sc1\n\t"
    "v_cvt_pk_bf16_f32 v124, v184, v185\n\t"
    "v_cvt_pk_bf16_f32 v125, v186, v187\n\t"
    "v_cvt_pk_bf16_f32 v126, v188, v189\n\t"
    "v_cvt_pk_bf16_f32 v127, v190, v191\n\t"
    "v_permlane16_swap_b32 v120, v122\n\t"
    "v_permlane16_swap_b32 v121, v123\n\t"
    "s_waitcnt lgkmcnt(8)\n\t"
    "v_pk_fma_f32 v[192:193], v[64:65], v[248:249], v[192:193]\n\t"
    "v_pk_fma_f32 v[194:195], v[66:67], v[248:249], v[194:195]\n\t"
    "v_pk_fma_f32 v[196:197], v[68:69], v[248:249], v[196:197]\n\t"
    "v_pk_fma_f32 v[198:199], v[70:71], v[248:249], v[198:199]\n\t"
    "buffer_store_dwordx4 v[116:119], v229, s[48:51], s60 offen offset:2048 sc1\n\t"
    "v_permlane16_swap_b32 v124, v126\n\t"
    "v_permlane16_swap_b32 v125, v127\n\t"
    "v_cvt_pk_bf16_f32 v128, v192, v193\n\t"
    "v_cvt_pk_bf16_f32 v129, v194, v195\n\t"
    "v_cvt_pk_bf16_f32 v130, v196, v197\n\t"
    "v_cvt_pk_bf16_f32 v131, v198, v199\n\t"
    "s_waitcnt lgkmcnt(6)\n\t"
    "v_pk_fma_f32 v[200:201], v[72:73], v[248:249], v[200:201]\n\t"
    "v_pk_fma_f32 v[202:203], v[74:75], v[248:249], v[202:203]\n\t"
    "v_pk_fma_f32 v[204:205], v[76:77], v[248:249], v[204:205]\n\t"
    "v_pk_fma_f32 v[206:207], v[78:79], v[248:249], v[206:207]\n\t"
    "v_mov_b32 v140, v124\n\t"
    "v_mov_b32 v141, v125\n\t"
    "v_mov_b32 v142, v126\n\t"
    "v_mov_b32 v143, v127\n\t"
    "v_mov_b32_dpp v124, v120 row_ror:8 row_mask:0xf bank_mask:0x3\n\t"
    "v_mov_b32_dpp v125, v121 row_ror:8 row_mask:0xf bank_mask:0x3\n\t"
    "v_mov_b32_dpp v126, v122 row_ror:8 row_mask:0xf bank_mask:0x3\n\t"
    "v_mov_b32_dpp v127, v123 row_ror:8 row_mask:0xf bank_mask:0x3\n\t"
    "v_mov_b32_dpp v120, v140 row_ror:8 row_mask:0xf bank_mask:0xc\n\t"
    "v_mov_b32_dpp v121, v141 row_ror:8 row_mask:0xf bank_mask:0xc\n\t"
    "v_mov_b32_dpp v122, v142 row_ror:8 row_mask:0xf bank_mask:0xc\n\t"
    "v_mov_b32_dpp v123, v143 row_ror:8 row_mask:0xf bank_mask:0xc\n\t"
    "buffer_store_dwordx4 v[120:123], v229, s[48:51], s61 offen offset:0 sc1\n\t"
    "v_cvt_pk_bf16_f32 v132, v200, v201\n\t"
    "v_cvt_pk_bf16_f32 v133, v202, v203\n\t"
    "v_cvt_pk_bf16_f32 v134, v204, v205\n\t"
    "v_cvt_pk_bf16_f32 v135, v206, v207\n\t"
    "v_permlane16_swap_b32 v128, v130\n\t"
    "v_permlane16_swap_b32 v129, v131\n\t"
    "s_waitcnt lgkmcnt(0)\n\t"
    "v_pk_fma_f32 v[208:209], v[80:81], v[250:251], v[208:209]\n\t"
    "v_pk_fma_f32 v[210:211], v[82:83], v[250:251], v[210:211]\n\t"
    "v_pk_fma_f32 v[208:209], v[88:89], v[250:251], v[208:209]\n\t"
    "v_pk_fma_f32 v[210:211], v[90:91], v[250:251], v[210:211]\n\t"
    "v_pk_fma_f32 v[208:209], v[96:97], v[250:251], v[208:209]\n\t"
    "buffer_store_dwordx4 v[124:127], v229, s[48:51], s61 offen offset:2048 sc1\n\t"
    "v_pk_fma_f32 v[210:211], v[98:99], v[250:251], v[210:211]\n\t"
    "v_pk_fma_f32 v[212:213], v[84:85], v[250:251], v[212:213]\n\t"
    "v_pk_fma_f32 v[214:215], v[86:87], v[250:251], v[214:215]\n\t"
    "v_pk_fma_f32 v[212:213], v[92:93], v[250:251], v[212:213]\n\t"
    "v_pk_fma_f32 v[214:215], v[94:95], v[250:251], v[214:215]\n\t"
    "v_pk_fma_f32 v[212:213], v[100:101], v[250:251], v[212:213]\n\t"
    "v_pk_fma_f32 v[214:215], v[102:103], v[250:251], v[214:215]\n\t"
    "v_permlane16_swap_b32 v132, v134\n\t"
    "v_permlane16_swap_b32 v133, v135\n\t"
    "v_cvt_pk_bf16_f32 v136, v208, v209\n\t"
    "v_cvt_pk_bf16_f32 v137, v210, v211\n\t"
    "v_cvt_pk_bf16_f32 v138, v212, v213\n\t"
    "v_cvt_pk_bf16_f32 v139, v214, v215\n\t"
    "v_mov_b32 v140, v132\n\t"
    "v_mov_b32 v141, v133\n\t"
    "v_mov_b32 v142, v134\n\t"
    "v_mov_b32 v143, v135\n\t"
    "v_mov_b32_dpp v132, v128 row_ror:8 row_mask:0xf bank_mask:0x3\n\t"
    "v_mov_b32_dpp v133, v129 row_ror:8 row_mask:0xf bank_mask:0x3\n\t"
    "v_mov_b32_dpp v134, v130 row_ror:8 row_mask:0xf bank_mask:0x3\n\t"
    "v_mov_b32_dpp v135, v131 row_ror:8 row_mask:0xf bank_mask:0x3\n\t"
    "v_mov_b32_dpp v128, v140 row_ror:8 row_mask:0xf bank_mask:0xc\n\t"
    "v_mov_b32_dpp v129, v141 row_ror:8 row_mask:0xf bank_mask:0xc\n\t"
    "v_mov_b32_dpp v130, v142 row_ror:8 row_mask:0xf bank_mask:0xc\n\t"
    "v_mov_b32_dpp v131, v143 row_ror:8 row_mask:0xf bank_mask:0xc\n\t"
    "buffer_store_dwordx4 v[128:131], v229, s[48:51], s62 offen offset:0 sc1\n\t"
    "v_permlane16_swap_b32 v136, v138\n\t"
    "v_permlane16_swap_b32 v137, v139\n\t"
    "buffer_store_dwordx4 v[132:135], v229, s[48:51], s62 offen offset:2048 sc1\n\t"
    "s_nop 0\n\t"
    "buffer_store_dwordx4 v[136:139], v228, s[48:51], s63 offen sc1\n\t"
    "v_mov_b32 v242, 0\n\t"
    "v_mov_b32 v243, 0\n\t"
    "v_pk_fma_f32 v[242:243], v[160:161], 0, v[242:243]\n\t"
    "v_pk_fma_f32 v[242:243], v[162:163], 0, v[242:243]\n\t"
    "v_pk_fma_f32 v[242:243], v[164:165], 0, v[242:243]\n\t"
    "v_pk_fma_f32 v[242:243], v[166:167], 0, v[242:243]\n\t"
    "v_pk_fma_f32 v[242:243], v[168:169], 0, v[242:243]\n\t"
    "v_pk_fma_f32 v[242:243], v[170:171], 0, v[242:243]\n\t"
    "v_pk_fma_f32 v[242:243], v[172:173], 0, v[242:243]\n\t"
    "v_pk_fma_f32 v[242:243], v[174:175], 0, v[242:243]\n\t"
    "v_pk_fma_f32 v[242:243], v[176:177], 0, v[242:243]\n\t"
    "v_pk_fma_f32 v[242:243], v[178:179], 0, v[242:243]\n\t"
    "v_pk_fma_f32 v[242:243], v[180:181], 0, v[242:243]\n\t"
    "v_pk_fma_f32 v[242:243], v[182:183], 0, v[242:243]\n\t"
    "v_pk_fma_f32 v[242:243], v[184:185], 0, v[242:243]\n\t"
    "v_pk_fma_f32 v[242:243], v[186:187], 0, v[242:243]\n\t"
    "v_pk_fma_f32 v[242:243], v[188:189], 0, v[242:243]\n\t"
    "v_pk_fma_f32 v[242:243], v[190:191], 0, v[242:243]\n\t"
    "v_pk_fma_f32 v[242:243], v[192:193], 0, v[242:243]\n\t"
    "v_pk_fma_f32 v[242:243], v[194:195], 0, v[242:243]\n\t"
    "v_pk_fma_f32 v[242:243], v[196:197], 0, v[242:243]\n\t"
    "v_pk_fma_f32 v[242:243], v[198:199], 0, v[242:243]\n\t"
    "v_pk_fma_f32 v[242:243], v[200:201], 0, v[242:243]\n\t"
    "v_pk_fma_f32 v[242:243], v[202:203], 0, v[242:243]\n\t"
    "v_pk_fma_f32 v[242:243], v[204:205], 0, v[242:243]\n\t"
    "v_pk_fma_f32 v[242:243], v[206:207], 0, v[242:243]\n\t"
    "v_pk_fma_f32 v[242:243], v[208:209], 0, v[242:243]\n\t"
    "v_pk_fma_f32 v[242:243], v[210:211], 0, v[242:243]\n\t"
    "v_pk_fma_f32 v[242:243], v[212:213], 0, v[242:243]\n\t"
    "v_pk_fma_f32 v[242:243], v[214:215], 0, v[242:243]\n\t"
    "v_add_f32 v242, v242, v243\n\t"
    "v_min_f32 v236, v232, v233\n\t"
    "v_min_f32 v237, v234, v235\n\t"
    "v_min_f32 v236, v236, v237\n\t"
    "v_add_f32 v237, v232, v233\n\t"
    "v_add_f32 v238, v234, v235\n\t"
    "v_add_f32 v237, v237, v238\n\t"
    "v_add_f32 v242, v242, v237\n\t"
    "s_mov_b32 s64, 0x17800000\n\t"
    "s_mov_b32 s65, 0x67800000\n\t"
    "v_cmp_gt_f32 s[72:73], s64, v236\n\t"
    "v_cmp_nge_f32 vcc, s65, v242\n\t"
    "s_nop 1\n\t"
    "s_or_b64 s[72:73], s[72:73], vcc\n\t"
    "s_or_b32 s70, s72, s73\n\t"
    "s_branch L_end_%=\n\t"
    "L_var1_%=:\n\t"
    "s_add_u32 s61, %7, 49152\n\t"
    "v_add_u32 v22, s61, v30\n\t"
    "v_add_u32 v23, 81920, v22\n\t"
    "v_subrev_u32 v24, 8192, v23\n\t"
    "ds_read_b128 a[120:123], v10 offset:0\n\t"
    "ds_read_b128 a[136:139], v10 offset:4096\n\t"
    "ds_read_b128 v[96:99], v22 offset:0\n\t"
    "ds_read_b128 a[124:127], v11 offset:0\n\t"
    "ds_read_b128 a[140:143], v11 offset:4096\n\t"
    "ds_read_b128 v[100:103], v22 offset:1024\n\t"
    "ds_read_b128 a[128:131], v12 offset:0\n\t"
    "ds_read_b128 a[144:147], v12 offset:4096\n\t"
    "ds_read_b128 v[104:107], v22 offset:2048\n\t"
    "ds_read_b128 a[132:135], v13 offset:0\n\t"
    "ds_read_b128 a[148:151], v13 offset:4096\n\t"
    "ds_read_b128 v[108:111], v22 offset:3072\n\t"
    "ds_read_b128 v[112:115], v22 offset:4096\n\t"
    "ds_read_b128 v[116:119], v22 offset:5120\n\t"
    "ds_read_b128 v[120:123], v22 offset:6144\n\t"
    "ds_read_b128 v[124:127], v22 offset:7168\n\t"
    "ds_read_b128 v[128:131], v23 offset:0\n\t"
    "ds_read_b128 v[132:135], v23 offset:1024\n\t"
    "ds_read_b128 v[136:139], v23 offset:2048\n\t"
    "ds_read_b128 v[140:143], v23 offset:3072\n\t"
    "ds_read_b128 v[144:147], v24 offset:4096\n\t"
    "ds_read_b128 v[148:151], v24 offset:5120\n\t"
    "ds_read_b128 v[152:155], v24 offset:6144\n\t"
    "ds_read_b128 v[156:159], v24 offset:7168\n\t"
    "s_mov_b32 s36, 16384\n\t"
    "s_mov_b32 s37, 20480\n\t"
    "s_mov_b32 s38, 24576\n\t"
    "s_mov_b32 s39, 12288\n\t"
    "s_mov_b32 s75, 0x07060302\n\t"
    "s_waitcnt lgkmcnt(15)\n\t"
    "v_mfma_f32_16x16x32_bf16 v[32:35], a[120:123], v[96:99], 0\n\t"
    "s_add_u32 s60, s54, 8192\n\t"
    "ds_read_b128 a[88:91], v10 offset:8192\n\t"
    "ds_read_b128 a[92:95], v11 offset:8192\n\t"
    "v_mfma_f32_16x16x32_bf16 v[36:39], a[136:139], v[96:99], 0\n\t"
    "s_add_u32 m0, s53, 24576\n\t"
    "ds_read_b128 a[96:99], v12 offset:8192\n\t"
    "ds_read_b128 a[100:103], v13 offset:8192\n\t"
    "v_mfma_f32_16x16x32_bf16 v[32:35], a[124:127], v[100:103], v[32:35]\n\t"
    "ds_read_b128 a[104:107], v10 offset:12288\n\t"
    "ds_read_b128 a[108:111], v11 offset:12288\n\t"
    "v_mfma_f32_16x16x32_bf16 v[36:39], a[140:143], v[100:103], v[36:39]\n\t"
    "s_waitcnt lgkmcnt(15)\n\t"
    "ds_read_b128 a[112:115], v12 offset:12288\n\t"
    "ds_read_b128 a[116:119], v13 offset:12288\n\t"
    "v_mfma_f32_16x16x32_bf16 v[32:35], a[128:131], v[104:107], v[32:35]\n\t"
    "buffer_load_dwordx4 v6, s[44:47], s60 offen offset:0 nt lds\n\t"
    "v_mfma_f32_16x16x32_bf16 v[36:39], a[144:147], v[104:107], v[36:39]\n\t"
    "v_mfma_f32_16x16x32_bf16 v[32:35], a[132:135], v[108:111], v[32:35]\n\t"
    "v_mfma_f32_16x16x32_bf16 v[36:39], a[148:151], v[108:111], v[36:39]\n\t"
    "s_waitcnt lgkmcnt(15)\n\t"
    "v_mfma_f32_16x16x32_bf16 v[40:43], a[120:123], v[112:115], 0\n\t"
    "v_mfma_f32_16x16x32_bf16 v[44:47], a[136:139], v[112:115], 0\n\t"
    "v_mfma_f32_16x16x32_bf16 v[40:43], a[124:127], v[116:119], v[40:43]\n\t"
    "buffer_load_dwordx4 v7, s[44:47], s60 offen offset:1024 nt lds\n\t"
    "v_mfma_f32_16x16x32_bf16 v[44:47], a[140:143], v[116:119], v[44:47]\n\t"
    "s_waitcnt lgkmcnt(15)\n\t"
    "v_exp_f32 v32, v32\n\t"
    "v_mfma_f32_16x16x32_bf16 v[40:43], a[128:131], v[120:123], v[40:43]\n\t"
    "v_exp_f32 v33, v33\n\t"
    "v_mfma_f32_16x16x32_bf16 v[44:47], a[144:147], v[120:123], v[44:47]\n\t"
    "v_exp_f32 v34, v34\n\t"
    "v_mfma_f32_16x16x32_bf16 v[40:43], a[132:135], v[124:127], v[40:43]\n\t"
    "v_exp_f32 v35, v35\n\t"
    "v_mfma_f32_16x16x32_bf16 v[44:47], a[148:151], v[124:127], v[44:47]\n\t"
    "s_waitcnt lgkmcnt(14)\n\t"
    "v_exp_f32 v36, v36\n\t"
    "v_mfma_f32_16x16x32_bf16 v[48:51], a[120:123], v[128:131], 0\n\t"
    "buffer_load_dwordx4 v8, s[44:47], s60 offen offset:2048 nt lds\n\t"
    "v_mfma_f32_16x16x32_bf16 v[52:55], a[136:139], v[128:131], 0\n\t"
    "v_exp_f32 v37, v37\n\t"
    "v_mfma_f32_16x16x32_bf16 v[48:51], a[124:127], v[132:135], v[48:51]\n\t"
    "v_cvt_pk_bf16_f32 v32, v32, v33\n\t"
    "v_cvt_pk_bf16_f32 v33, v34, v35\n\t"
    "v_mfma_f32_16x16x32_bf16 v[52:55], a[140:143], v[132:135], v[52:55]\n\t"
    "s_waitcnt lgkmcnt(12)\n\t"
    "v_exp_f32 v38, v38\n\t"
    "v_mfma_f32_16x16x32_bf16 v[48:51], a[128:131], v[136:139], v[48:51]\n\t"
    "v_exp_f32 v39, v39\n\t"
    "v_mfma_f32_16x16x32_bf16 v[52:55], a[144:147], v[136:139], v[52:55]\n\t"
    "v_cvt_pk_bf16_f32 v34, v36, v37\n\t"
    "v_cvt_pk_bf16_f32 v35, v38, v39\n\t"
    "v_mfma_f32_16x16x32_bf16 v[48:51], a[132:135], v[140:143], v[48:51]\n\t"
    "buffer_load_dwordx4 v9, s[44:47], s60 offen offset:3072 nt lds\n\t"
    "v_mfma_f32_16x16x32_bf16 v[52:55], a[148:151], v[140:143], v[52:55]\n\t"
    "s_waitcnt vmcnt(8) lgkmcnt(0)\n\t"
    "s_barrier\n\t"
    "v_mfma_f32_16x16x32_bf16 v[64:67], a[88:91], v[96:99], 0\n\t"
    "s_add_u32 s61, s54, 24576\n\t"
    "v_exp_f32 v40, v40\n\t"
    "v_mfma_f32_16x16x32_bf16 v[72:75], a[88:91], v[112:115], 0\n\t"
    "s_add_u32 m0, s52, 24576\n\t"
    "v_exp_f32 v41, v41\n\t"
    "v_mfma_f32_16x16x32_bf16 v[80:83], a[88:91], v[128:131], 0\n\t"
    "ds_read_b64_tr_b16 a[120:121], v14 offset:16896\n\t"
    "ds_read_b64_tr_b16 a[122:123], v14 offset:20992\n\t"
    "v_mfma_f32_16x16x32_bf16 v[88:91], a[88:91], v[144:147], 0\n\t"
    "v_exp_f32 v42, v42\n\t"
    "v_mfma_f32_16x16x32_bf16 v[64:67], a[92:95], v[100:103], v[64:67]\n\t"
    "buffer_load_dwordx4 v2, s[40:43], s61 offen offset:0 nt lds\n\t"
    "v_mfma_f32_16x16x32_bf16 v[72:75], a[92:95], v[116:119], v[72:75]\n\t"
    "ds_read_b64_tr_b16 a[124:125], v15 offset:16896\n\t"
    "v_cvt_pk_bf16_f32 v40, v40, v41\n\t"
    "v_mfma_f32_16x16x32_bf16 v[80:83], a[92:95], v[132:135], v[80:83]\n\t"
    "v_exp_f32 v43, v43\n\t"
    "v_mfma_f32_16x16x32_bf16 v[88:91], a[92:95], v[148:151], v[88:91]\n\t"
    "ds_read_b64_tr_b16 a[126:127], v15 offset:20992\n\t"
    "v_cvt_pk_bf16_f32 v41, v42, v43\n\t"
    "v_mfma_f32_16x16x32_bf16 v[64:67], a[96:99], v[104:107], v[64:67]\n\t"
    "v_exp_f32 v44, v44\n\t"
    "v_mfma_f32_16x16x32_bf16 v[72:75], a[96:99], v[120:123], v[72:75]\n\t"
    "v_exp_f32 v45, v45\n\t"
    "v_mfma_f32_16x16x32_bf16 v[80:83], a[96:99], v[136:139], v[80:83]\n\t"
    "buffer_load_dwordx4 v3, s[40:43], s61 offen offset:1024 nt lds\n\t"
    "v_mfma_f32_16x16x32_bf16 v[88:91], a[96:99], v[152:155], v[88:91]\n\t"
    "ds_read_b64_tr_b16 a[128:129], v14 offset:16384\n\t"
    "ds_read_b64_tr_b16 a[130:131], v14 offset:20480\n\t"
    "v_mfma_f32_16x16x32_bf16 v[64:67], a[100:103], v[108:111], v[64:67]\n\t"
    "ds_read_b64_tr_b16 a[132:133], v15 offset:16384\n\t"
    "v_cvt_pk_bf16_f32 v42, v44, v45\n\t"
    "v_mfma_f32_16x16x32_bf16 v[72:75], a[100:103], v[124:127], v[72:75]\n\t"
    "v_exp_f32 v46, v46\n\t"
    "v_mfma_f32_16x16x32_bf16 v[80:83], a[100:103], v[140:143], v[80:83]\n\t"
    "v_exp_f32 v47, v47\n\t"
    "v_mfma_f32_16x16x32_bf16 v[88:91], a[100:103], v[156:159], v[88:91]\n\t"
    "ds_read_b64_tr_b16 a[134:135], v15 offset:20480\n\t"
    "v_cvt_pk_bf16_f32 v43, v46, v47\n\t"
    "v_mfma_f32_16x16x32_bf16 v[68:71], a[104:107], v[96:99], 0\n\t"
    "buffer_load_dwordx4 v4, s[40:43], s61 offen offset:2048 nt lds\n\t"
    "v_mfma_f32_16x16x32_bf16 v[76:79], a[104:107], v[112:115], 0\n\t"
    "v_exp_f32 v48, v48\n\t"
    "v_mfma_f32_16x16x32_bf16 v[84:87], a[104:107], v[128:131], 0\n\t"
    "ds_read_b64_tr_b16 a[136:137], v16 offset:17920\n\t"
    "ds_read_b64_tr_b16 a[138:139], v16 offset:22016\n\t"
    "v_mfma_f32_16x16x32_bf16 v[92:95], a[104:107], v[144:147], 0\n\t"
    "v_exp_f32 v49, v49\n\t"
    "v_mfma_f32_16x16x32_bf16 v[68:71], a[108:111], v[100:103], v[68:71]\n\t"
    "s_waitcnt vmcnt(7)\n\t"
    "s_barrier\n\t"
    "v_mfma_f32_16x16x32_bf16 v[76:79], a[108:111], v[116:119], v[76:79]\n\t"
    "ds_read_b64_tr_b16 a[140:141], v17 offset:17920\n\t"
    "v_cvt_pk_bf16_f32 v48, v48, v49\n\t"
    "v_mfma_f32_16x16x32_bf16 v[84:87], a[108:111], v[132:135], v[84:87]\n\t"
    "buffer_load_dwordx4 v5, s[40:43], s61 offen offset:3072 nt lds\n\t"
    "v_mfma_f32_16x16x32_bf16 v[92:95], a[108:111], v[148:151], v[92:95]\n\t"
    "s_add_u32 s60, s54, 16384\n\t"
    "v_exp_f32 v50, v50\n\t"
    "v_mfma_f32_16x16x32_bf16 v[68:71], a[112:115], v[104:107], v[68:71]\n\t"
    "s_add_u32 m0, s53, 32768\n\t"
    "ds_read_b64_tr_b16 a[142:143], v17 offset:22016\n\t"
    "ds_read_b64_tr_b16 a[144:145], v16 offset:17408\n\t"
    "v_mfma_f32_16x16x32_bf16 v[76:79], a[112:115], v[120:123], v[76:79]\n\t"
    "v_exp_f32 v51, v51\n\t"
    "v_mfma_f32_16x16x32_bf16 v[84:87], a[112:115], v[136:139], v[84:87]\n\t"
    "ds_read_b64_tr_b16 a[146:147], v16 offset:21504\n\t"
    "v_cvt_pk_bf16_f32 v49, v50, v51\n\t"
    "v_mfma_f32_16x16x32_bf16 v[92:95], a[112:115], v[152:155], v[92:95]\n\t"
    "v_exp_f32 v52, v52\n\t"
    "v_mfma_f32_16x16x32_bf16 v[68:71], a[116:119], v[108:111], v[68:71]\n\t"
    "ds_read_b64_tr_b16 a[148:149], v17 offset:17408\n\t"
    "ds_read_b128 a[88:91], v10 offset:16384\n\t"
    "v_mfma_f32_16x16x32_bf16 v[76:79], a[116:119], v[124:127], v[76:79]\n\t"
    "buffer_load_dwordx4 v6, s[44:47], s60 offen offset:0 nt lds\n\t"
    "v_mfma_f32_16x16x32_bf16 v[84:87], a[116:119], v[140:143], v[84:87]\n\t"
    "ds_read_b64_tr_b16 a[150:151], v17 offset:21504\n\t"
    "ds_read_b128 a[92:95], v11 offset:16384\n\t"
    "v_mfma_f32_16x16x32_bf16 v[92:95], a[116:119], v[156:159], v[92:95]\n\t"
    "s_waitcnt lgkmcnt(7)\n\t"
    "v_exp_f32 v53, v53\n\t"
    "v_mfma_f32_16x16x32_bf16 v[160:163], a[120:123], v[32:35], 0\n\t"
    "v_exp_f32 v54, v54\n\t"
    "v_mfma_f32_16x16x32_bf16 v[164:167], a[124:127], v[32:35], 0\n\t"
    "v_exp_f32 v55, v55\n\t"
    "v_mfma_f32_16x16x32_bf16 v[168:171], a[128:131], v[32:35], 0\n\t"
    "v_cvt_pk_bf16_f32 v50, v52, v53\n\t"
    "v_cvt_pk_bf16_f32 v51, v54, v55\n\t"
    "v_mfma_f32_16x16x32_bf16 v[172:175], a[132:135], v[32:35], 0\n\t"
    "buffer_load_dwordx4 v7, s[44:47], s60 offen offset:1024 nt lds\n\t"
    "v_mfma_f32_16x16x32_bf16 a[0:3], a[136:139], v[32:35], 0\n\t"
    "s_waitcnt lgkmcnt(2)\n\t"
    "ds_read_b128 a[96:99], v12 offset:16384\n\t"
    "ds_read_b128 a[100:103], v13 offset:16384\n\t"
    "v_mfma_f32_16x16x32_bf16 a[4:7], a[140:143], v[32:35], 0\n\t"
    "v_exp_f32 v64, v64\n\t"
    "v_mfma_f32_16x16x32_bf16 a[8:11], a[144:147], v[32:35], 0\n\t"
    "s_waitcnt lgkmcnt(2)\n\t"
    "v_exp_f32 v65, v65\n\t"
    "v_mfma_f32_16x16x32_bf16 a[12:15], a[148:151], v[32:35], 0\n\t"
    "ds_read_b128 a[104:107], v10 offset:20480\n\t"
    "v_cvt_pk_bf16_f32 v64, v64, v65\n\t"
    "v_mfma_f32_16x16x32_bf16 a[72:75], v[216:219], v[32:35], 0\n\t"
    "v_exp_f32 v66, v66\n\t"
    "v_mfma_f32_16x16x32_bf16 v[176:179], a[120:123], v[40:43], 0\n\t"
    "buffer_load_dwordx4 v8, s[44:47], s60 offen offset:2048 nt lds\n\t"
    "v_mfma_f32_16x16x32_bf16 v[180:183], a[124:127], v[40:43], 0\n\t"
    "v_exp_f32 v67, v67\n\t"
    "v_mfma_f32_16x16x32_bf16 v[184:187], a[128:131], v[40:43], 0\n\t"
    "ds_read_b128 a[108:111], v11 offset:20480\n\t"
    "ds_read_b128 a[112:115], v12 offset:20480\n\t"
    "v_mfma_f32_16x16x32_bf16 v[188:191], a[132:135], v[40:43], 0\n\t"
    "v_exp_f32 v68, v68\n\t"
    "v_mfma_f32_16x16x32_bf16 a[16:19], a[136:139], v[40:43], 0\n\t"
    "ds_read_b128 a[116:119], v13 offset:20480\n\t"
    "v_cvt_pk_bf16_f32 v65, v66, v67\n\t"
    "v_mfma_f32_16x16x32_bf16 a[20:23], a[140:143], v[40:43], 0\n\t"
    "v_exp_f32 v69, v69\n\t"
    "v_mfma_f32_16x16x32_bf16 a[24:27], a[144:147], v[40:43], 0\n\t"
    "buffer_load_dwordx4 v9, s[44:47], s60 offen offset:3072 nt lds\n\t"
    "v_mfma_f32_16x16x32_bf16 a[28:31], a[148:151], v[40:43], 0\n\t"
    "s_add_u32 s61, s54, 32768\n\t"
    "v_exp_f32 v70, v70\n\t"
    "v_mfma_f32_16x16x32_bf16 a[76:79], v[216:219], v[40:43], 0\n\t"
    "s_add_u32 m0, s52, 32768\n\t"
    "v_exp_f32 v71, v71\n\t"
    "v_mfma_f32_16x16x32_bf16 v[192:195], a[120:123], v[48:51], 0\n\t"
    "v_cvt_pk_bf16_f32 v66, v68, v69\n\t"
    "v_cvt_pk_bf16_f32 v67, v70, v71\n\t"
    "v_mfma_f32_16x16x32_bf16 v[196:199], a[124:127], v[48:51], 0\n\t"
    "v_exp_f32 v72, v72\n\t"
    "v_mfma_f32_16x16x32_bf16 v[200:203], a[128:131], v[48:51], 0\n\t"
    "v_exp_f32 v73, v73\n\t"
    "v_mfma_f32_16x16x32_bf16 v[204:207], a[132:135], v[48:51], 0\n\t"
    "buffer_load_dwordx4 v2, s[40:43], s61 offen offset:0 nt lds\n\t"
    "v_mfma_f32_16x16x32_bf16 a[32:35], a[136:139], v[48:51], 0\n\t"
    "v_exp_f32 v74, v74\n\t"
    "v_mfma_f32_16x16x32_bf16 a[36:39], a[140:143], v[48:51], 0\n\t"
    "v_exp_f32 v75, v75\n\t"
    "v_mfma_f32_16x16x32_bf16 a[40:43], a[144:147], v[48:51], 0\n\t"
    "v_exp_f32 v76, v76\n\t"
    "v_mfma_f32_16x16x32_bf16 a[44:47], a[148:151], v[48:51], 0\n\t"
    "v_exp_f32 v77, v77\n\t"
    "v_mfma_f32_16x16x32_bf16 a[80:83], v[216:219], v[48:51], 0\n\t"
    "s_waitcnt vmcnt(9) lgkmcnt(0)\n\t"
    "s_barrier\n\t"
    "v_mfma_f32_16x16x32_bf16 v[32:35], a[88:91], v[96:99], 0\n\t"
    "buffer_load_dwordx4 v3, s[40:43], s61 offen offset:1024 nt lds\n\t"
    "v_mfma_f32_16x16x32_bf16 v[40:43], a[88:91], v[112:115], 0\n\t"
    "ds_read_b64_tr_b16 a[120:121], v14 offset:25088\n\t"
    "ds_read_b64_tr_b16 a[122:123], v14 offset:29184\n\t"
    "v_mfma_f32_16x16x32_bf16 v[48:51], a[88:91], v[128:131], 0\n\t"
    "v_cvt_pk_bf16_f32 v72, v72, v73\n\t"
    "v_cvt_pk_bf16_f32 v73, v74, v75\n\t"
    "v_mfma_f32_16x16x32_bf16 v[32:35], a[92:95], v[100:103], v[32:35]\n\t"
    "ds_read_b64_tr_b16 a[124:125], v15 offset:25088\n\t"
    "v_cvt_pk_bf16_f32 v74, v76, v77\n\t"
    "v_mfma_f32_16x16x32_bf16 v[40:43], a[92:95], v[116:119], v[40:43]\n\t"
    "v_exp_f32 v78, v78\n\t"
    "v_mfma_f32_16x16x32_bf16 v[48:51], a[92:95], v[132:135], v[48:51]\n\t"
    "v_exp_f32 v79, v79\n\t"
    "v_mfma_f32_16x16x32_bf16 v[32:35], a[96:99], v[104:107], v[32:35]\n\t"
    "buffer_load_dwordx4 v4, s[40:43], s61 offen offset:2048 nt lds\n\t"
    "v_mfma_f32_16x16x32_bf16 v[40:43], a[96:99], v[120:123], v[40:43]\n\t"
    "ds_read_b64_tr_b16 a[126:127], v15 offset:29184\n\t"
    "ds_read_b64_tr_b16 a[128:129], v14 offset:24576\n\t"
    "v_mfma_f32_16x16x32_bf16 v[48:51], a[96:99], v[136:139], v[48:51]\n\t"
    "ds_read_b64_tr_b16 a[130:131], v14 offset:28672\n\t"
    "v_cvt_pk_bf16_f32 v75, v78, v79\n\t"
    "v_mfma_f32_16x16x32_bf16 v[32:35], a[100:103], v[108:111], v[32:35]\n\t"
    "v_exp_f32 v80, v80\n\t"
    "v_mfma_f32_16x16x32_bf16 v[40:43], a[100:103], v[124:127], v[40:43]\n\t"
    "ds_read_b64_tr_b16 a[132:133], v15 offset:24576\n\t"
    "ds_read_b64_tr_b16 a[134:135], v15 offset:28672\n\t"
    "v_mfma_f32_16x16x32_bf16 v[48:51], a[100:103], v[140:143], v[48:51]\n\t"
    "v_exp_f32 v81, v81\n\t"
    "v_mfma_f32_16x16x32_bf16 v[36:39], a[104:107], v[96:99], 0\n\t"
    "ds_read_b64_tr_b16 a[136:137], v16 offset:26112\n\t"
    "v_cvt_pk_bf16_f32 v80, v80, v81\n\t"
    "v_mfma_f32_16x16x32_bf16 v[44:47], a[104:107], v[112:115], 0\n\t"
    "buffer_load_dwordx4 v5, s[40:43], s61 offen offset:3072 nt lds\n\t"
    "v_mfma_f32_16x16x32_bf16 v[52:55], a[104:107], v[128:131], 0\n\t"
    "s_add_u32 s60, s54, 24576\n\t"
    "v_exp_f32 v82, v82\n\t"
    "v_mfma_f32_16x16x32_bf16 v[36:39], a[108:111], v[100:103], v[36:39]\n\t"
    "s_add_u32 m0, s53, 0\n\t"
    "ds_read_b64_tr_b16 a[138:139], v16 offset:30208\n\t"
    "ds_read_b64_tr_b16 a[140:141], v17 offset:26112\n\t"
    "v_mfma_f32_16x16x32_bf16 v[44:47], a[108:111], v[116:119], v[44:47]\n\t"
    "v_exp_f32 v83, v83\n\t"
    "v_mfma_f32_16x16x32_bf16 v[52:55], a[108:111], v[132:135], v[52:55]\n\t"
    "ds_read_b64_tr_b16 a[142:143], v17 offset:30208\n\t"
    "v_cvt_pk_bf16_f32 v81, v82, v83\n\t"
    "v_mfma_f32_16x16x32_bf16 v[36:39], a[112:115], v[104:107], v[36:39]\n\t"
    "v_exp_f32 v84, v84\n\t"
    "v_mfma_f32_16x16x32_bf16 v[44:47], a[112:115], v[120:123], v[44:47]\n\t"
    "buffer_load_dwordx4 v6, s[44:47], s60 offen offset:0 nt lds\n\t"
    "v_mfma_f32_16x16x32_bf16 v[52:55], a[112:115], v[136:139], v[52:55]\n\t"
    "s_waitcnt vmcnt(9)\n\t"
    "s_barrier\n\t"
    "v_mfma_f32_16x16x32_bf16 v[36:39], a[116:119], v[108:111], v[36:39]\n\t"
    "ds_read_b64_tr_b16 a[144:145], v16 offset:25600\n\t"
    "ds_read_b64_tr_b16 a[146:147], v16 offset:29696\n\t"
    "v_mfma_f32_16x16x32_bf16 v[44:47], a[116:119], v[124:127], v[44:47]\n\t"
    "ds_read_b64_tr_b16 a[148:149], v17 offset:25600\n\t"
    "ds_read_b64_tr_b16 a[150:151], v17 offset:29696\n\t"
    "v_mfma_f32_16x16x32_bf16 v[52:55], a[116:119], v[140:143], v[52:55]\n\t"
    "s_waitcnt lgkmcnt(5)\n\t"
    "v_exp_f32 v85, v85\n\t"
    "v_mfma_f32_16x16x32_bf16 v[160:163], a[120:123], v[64:67], v[160:163]\n\t"
    "v_exp_f32 v86, v86\n\t"
    "v_mfma_f32_16x16x32_bf16 v[164:167], a[124:127], v[64:67], v[164:167]\n\t"
    "buffer_load_dwordx4 v7, s[44:47], s60 offen offset:1024 nt lds\n\t"
    "v_mfma_f32_16x16x32_bf16 v[168:171], a[128:131], v[64:67], v[168:171]\n\t"
    "v_exp_f32 v87, v87\n\t"
    "v_mfma_f32_16x16x32_bf16 v[172:175], a[132:135], v[64:67], v[172:175]\n\t"
    "v_cvt_pk_bf16_f32 v82, v84, v85\n\t"
    "v_cvt_pk_bf16_f32 v83, v86, v87\n\t"
    "v_mfma_f32_16x16x32_bf16 a[0:3], a[136:139], v[64:67], a[0:3]\n\t"
    "s_waitcnt lgkmcnt(4)\n\t"
    "v_exp_f32 v88, v88\n\t"
    "v_mfma_f32_16x16x32_bf16 a[4:7], a[140:143], v[64:67], a[4:7]\n\t"
    "s_waitcnt lgkmcnt(2)\n\t"
    "v_exp_f32 v89, v89\n\t"
    "v_mfma_f32_16x16x32_bf16 a[8:11], a[144:147], v[64:67], a[8:11]\n\t"
    "s_waitcnt lgkmcnt(0)\n\t"
    "v_exp_f32 v90, v90\n\t"
    "v_mfma_f32_16x16x32_bf16 a[12:15], a[148:151], v[64:67], a[12:15]\n\t"
    "buffer_load_dwordx4 v8, s[44:47], s60 offen offset:2048 nt lds\n\t"
    "v_mfma_f32_16x16x32_bf16 a[72:75], v[216:219], v[64:67], a[72:75]\n\t"
    "v_exp_f32 v91, v91\n\t"
    "v_mfma_f32_16x16x32_bf16 v[176:179], a[120:123], v[72:75], v[176:179]\n\t"
    "v_exp_f32 v92, v92\n\t"
    "v_mfma_f32_16x16x32_bf16 v[180:183], a[124:127], v[72:75], v[180:183]\n\t"
    "v_exp_f32 v93, v93\n\t"
    "v_mfma_f32_16x16x32_bf16 v[184:187], a[128:131], v[72:75], v[184:187]\n\t"
    "v_cvt_pk_bf16_f32 v88, v88, v89\n\t"
    "v_cvt_pk_bf16_f32 v89, v90, v91\n\t"
    "v_mfma_f32_16x16x32_bf16 v[188:191], a[132:135], v[72:75], v[188:191]\n\t"
    "v_exp_f32 v94, v94\n\t"
    "v_mfma_f32_16x16x32_bf16 a[16:19], a[136:139], v[72:75], a[16:19]\n\t"
    "buffer_load_dwordx4 v9, s[44:47], s60 offen offset:3072 nt lds\n\t"
    "v_mfma_f32_16x16x32_bf16 a[20:23], a[140:143], v[72:75], a[20:23]\n\t"
    "s_add_u32 s61, s54, 40960\n\t"
    "v_exp_f32 v95, v95\n\t"
    "v_mfma_f32_16x16x32_bf16 a[24:27], a[144:147], v[72:75], a[24:27]\n\t"
    "s_add_u32 m0, s52, 0\n\t"
    "v_cvt_pk_bf16_f32 v90, v92, v93\n\t"
    "v_cvt_pk_bf16_f32 v91, v94, v95\n\t"
    "v_mfma_f32_16x16x32_bf16 a[28:31], a[148:151], v[72:75], a[28:31]\n\t"
    "ds_read_b128 a[88:91], v10 offset:24576\n\t"
    "ds_read_b128 a[92:95], v11 offset:24576\n\t"
    "v_mfma_f32_16x16x32_bf16 a[76:79], v[216:219], v[72:75], a[76:79]\n\t"
    "ds_read_b128 a[96:99], v12 offset:24576\n\t"
    "ds_read_b128 a[100:103], v13 offset:24576\n\t"
    "v_mfma_f32_16x16x32_bf16 v[192:195], a[120:123], v[80:83], v[192:195]\n\t"
    "ds_read_b128 a[104:107], v10 offset:28672\n\t"
    "ds_read_b128 a[108:111], v11 offset:28672\n\t"
    "v_mfma_f32_16x16x32_bf16 v[196:199], a[124:127], v[80:83], v[196:199]\n\t"
    "buffer_load_dwordx4 v2, s[40:43], s61 offen offset:0 nt lds\n\t"
    "v_mfma_f32_16x16x32_bf16 v[200:203], a[128:131], v[80:83], v[200:203]\n\t"
    "v_exp_f32 v32, v32\n\t"
    "v_mfma_f32_16x16x32_bf16 v[204:207], a[132:135], v[80:83], v[204:207]\n\t"
    "ds_read_b128 a[112:115], v12 offset:28672\n\t"
    "ds_read_b128 a[116:119], v13 offset:28672\n\t"
    "v_mfma_f32_16x16x32_bf16 a[32:35], a[136:139], v[80:83], a[32:35]\n\t"
    "v_exp_f32 v33, v33\n\t"
    "v_mfma_f32_16x16x32_bf16 a[36:39], a[140:143], v[80:83], a[36:39]\n\t"
    "v_exp_f32 v34, v34\n\t"
    "v_mfma_f32_16x16x32_bf16 a[40:43], a[144:147], v[80:83], a[40:43]\n\t"
    "v_exp_f32 v35, v35\n\t"
    "v_mfma_f32_16x16x32_bf16 a[44:47], a[148:151], v[80:83], a[44:47]\n\t"
    "buffer_load_dwordx4 v3, s[40:43], s61 offen offset:1024 nt lds\n\t"
    "v_mfma_f32_16x16x32_bf16 a[80:83], v[216:219], v[80:83], a[80:83]\n\t"
    "v_exp_f32 v36, v36\n\t"
    "v_mfma_f32_16x16x32_bf16 v[208:211], a[120:123], v[88:91], 0\n\t"
    "v_exp_f32 v37, v37\n\t"
    "v_mfma_f32_16x16x32_bf16 v[212:215], a[124:127], v[88:91], 0\n\t"
    "v_cvt_pk_bf16_f32 v32, v32, v33\n\t"
    "v_cvt_pk_bf16_f32 v33, v34, v35\n\t"
    "v_mfma_f32_16x16x32_bf16 a[48:51], a[128:131], v[88:91], 0\n\t"
    "v_exp_f32 v38, v38\n\t"
    "v_mfma_f32_16x16x32_bf16 a[52:55], a[132:135], v[88:91], 0\n\t"
    "v_exp_f32 v39, v39\n\t"
    "v_mfma_f32_16x16x32_bf16 a[56:59], a[136:139], v[88:91], 0\n\t"
    "buffer_load_dwordx4 v4, s[40:43], s61 offen offset:2048 nt lds\n\t"
    "v_mfma_f32_16x16x32_bf16 a[60:63], a[140:143], v[88:91], 0\n\t"
    "v_cvt_pk_bf16_f32 v34, v36, v37\n\t"
    "v_cvt_pk_bf16_f32 v35, v38, v39\n\t"
    "v_mfma_f32_16x16x32_bf16 a[64:67], a[144:147], v[88:91], 0\n\t"
    "v_exp_f32 v40, v40\n\t"
    "v_mfma_f32_16x16x32_bf16 a[68:71], a[148:151], v[88:91], 0\n\t"
    "v_exp_f32 v41, v41\n\t"
    "v_mfma_f32_16x16x32_bf16 a[84:87], v[216:219], v[88:91], 0\n\t"
    "s_waitcnt vmcnt(11) lgkmcnt(0)\n\t"
    "s_barrier\n\t"
    "v_mfma_f32_16x16x32_bf16 v[64:67], a[88:91], v[96:99], 0\n\t"
    "ds_read_b64_tr_b16 a[120:121], v14 offset:33280\n\t"
    "v_cvt_pk_bf16_f32 v40, v40, v41\n\t"
    "v_mfma_f32_16x16x32_bf16 v[72:75], a[88:91], v[112:115], 0\n\t"
    "buffer_load_dwordx4 v5, s[40:43], s61 offen offset:3072 nt lds\n\t"
    "v_mfma_f32_16x16x32_bf16 v[80:83], a[88:91], v[128:131], 0\n\t"
    "s_add_u32 s60, s54, 32768\n\t"
    "v_exp_f32 v42, v42\n\t"
    "v_mfma_f32_16x16x32_bf16 v[88:91], a[88:91], v[144:147], 0\n\t"
    "s_add_u32 m0, s53, 8192\n\t"
    "v_exp_f32 v43, v43\n\t"
    "v_mfma_f32_16x16x32_bf16 v[64:67], a[92:95], v[100:103], v[64:67]\n\t"
    "ds_read_b64_tr_b16 a[122:123], v14 offset:37376\n\t"
    "ds_read_b64_tr_b16 a[124:125], v15 offset:33280\n\t"
    "v_mfma_f32_16x16x32_bf16 v[72:75], a[92:95], v[116:119], v[72:75]\n\t"
    "v_exp_f32 v44, v44\n\t"
    "v_mfma_f32_16x16x32_bf16 v[80:83], a[92:95], v[132:135], v[80:83]\n\t"
    "ds_read_b64_tr_b16 a[126:127], v15 offset:37376\n\t"
    "v_cvt_pk_bf16_f32 v41, v42, v43\n\t"
    "v_mfma_f32_16x16x32_bf16 v[88:91], a[92:95], v[148:151], v[88:91]\n\t"
    "buffer_load_dwordx4 v6, s[44:47], s60 offen offset:0 nt lds\n\t"
    "v_mfma_f32_16x16x32_bf16 v[64:67], a[96:99], v[104:107], v[64:67]\n\t"
    "v_exp_f32 v45, v45\n\t"
    "v_mfma_f32_16x16x32_bf16 v[72:75], a[96:99], v[120:123], v[72:75]\n\t"
    "ds_read_b64_tr_b16 a[128:129], v14 offset:32768\n\t"
    "v_cvt_pk_bf16_f32 v42, v44, v45\n\t"
    "v_mfma_f32_16x16x32_bf16 v[80:83], a[96:99], v[136:139], v[80:83]\n\t"
    "v_exp_f32 v46, v46\n\t"
    "v_mfma_f32_16x16x32_bf16 v[88:91], a[96:99], v[152:155], v[88:91]\n\t"
    "v_exp_f32 v47, v47\n\t"
    "v_mfma_f32_16x16x32_bf16 v[64:67], a[100:103], v[108:111], v[64:67]\n\t"
    "ds_read_b64_tr_b16 a[130:131], v14 offset:36864\n\t"
    "ds_read_b64_tr_b16 a[132:133], v15 offset:32768\n\t"
    "v_mfma_f32_16x16x32_bf16 v[72:75], a[100:103], v[124:127], v[72:75]\n\t"
    "buffer_load_dwordx4 v7, s[44:47], s60 offen offset:1024 nt lds\n\t"
    "v_mfma_f32_16x16x32_bf16 v[80:83], a[100:103], v[140:143], v[80:83]\n\t"
    "ds_read_b64_tr_b16 a[134:135], v15 offset:36864\n\t"
    "v_cvt_pk_bf16_f32 v43, v46, v47\n\t"
    "v_mfma_f32_16x16x32_bf16 v[88:91], a[100:103], v[156:159], v[88:91]\n\t"
    "v_exp_f32 v48, v48\n\t"
    "v_mfma_f32_16x16x32_bf16 v[68:71], a[104:107], v[96:99], 0\n\t"
    "v_exp_f32 v49, v49\n\t"
    "v_mfma_f32_16x16x32_bf16 v[76:79], a[104:107], v[112:115], 0\n\t"
    "ds_read_b64_tr_b16 a[136:137], v16 offset:34304\n\t"
    "v_cvt_pk_bf16_f32 v48, v48, v49\n\t"
    "v_mfma_f32_16x16x32_bf16 v[84:87], a[104:107], v[128:131], 0\n\t"
    "v_exp_f32 v50, v50\n\t"
    "v_mfma_f32_16x16x32_bf16 v[92:95], a[104:107], v[144:147], 0\n\t"
    "buffer_load_dwordx4 v8, s[44:47], s60 offen offset:2048 nt lds\n\t"
    "v_mfma_f32_16x16x32_bf16 v[68:71], a[108:111], v[100:103], v[68:71]\n\t"
    "s_waitcnt vmcnt(11)\n\t"
    "s_barrier\n\t"
    "v_mfma_f32_16x16x32_bf16 v[76:79], a[108:111], v[116:119], v[76:79]\n\t"
    "ds_read_b64_tr_b16 a[138:139], v16 offset:38400\n\t"
    "ds_read_b64_tr_b16 a[140:141], v17 offset:34304\n\t"
    "v_mfma_f32_16x16x32_bf16 v[84:87], a[108:111], v[132:135], v[84:87]\n\t"
    "v_exp_f32 v51, v51\n\t"
    "v_mfma_f32_16x16x32_bf16 v[92:95], a[108:111], v[148:151], v[92:95]\n\t"
    "ds_read_b64_tr_b16 a[142:143], v17 offset:38400\n\t"
    "v_cvt_pk_bf16_f32 v49, v50, v51\n\t"
    "v_mfma_f32_16x16x32_bf16 v[68:71], a[112:115], v[104:107], v[68:71]\n\t"
    "v_exp_f32 v52, v52\n\t"
    "v_mfma_f32_16x16x32_bf16 v[76:79], a[112:115], v[120:123], v[76:79]\n\t"
    "buffer_load_dwordx4 v9, s[44:47], s60 offen offset:3072 nt lds\n\t"
    "v_mfma_f32_16x16x32_bf16 v[84:87], a[112:115], v[136:139], v[84:87]\n\t"
    "s_add_u32 s61, s54, 49152\n\t"
    "ds_read_b64_tr_b16 a[144:145], v16 offset:33792\n\t"
    "ds_read_b64_tr_b16 a[146:147], v16 offset:37888\n\t"
    "v_mfma_f32_16x16x32_bf16 v[92:95], a[112:115], v[152:155], v[92:95]\n\t"
    "s_add_u32 m0, s52, 8192\n\t"
    "v_exp_f32 v53, v53\n\t"
    "v_mfma_f32_16x16x32_bf16 v[68:71], a[116:119], v[108:111], v[68:71]\n\t"
    "ds_read_b64_tr_b16 a[148:149], v17 offset:33792\n\t"
    "v_cvt_pk_bf16_f32 v50, v52, v53\n\t"
    "v_mfma_f32_16x16x32_bf16 v[76:79], a[116:119], v[124:127], v[76:79]\n\t"
    "v_exp_f32 v54, v54\n\t"
    "v_mfma_f32_16x16x32_bf16 v[84:87], a[116:119], v[140:143], v[84:87]\n\t"
    "ds_read_b64_tr_b16 a[150:151], v17 offset:37888\n\t"
    "ds_read_b128 a[88:91], v10 offset:32768\n\t"
    "v_mfma_f32_16x16x32_bf16 v[92:95], a[116:119], v[156:159], v[92:95]\n\t"
    "s_waitcnt lgkmcnt(5)\n\t"
    "buffer_load_dwordx4 v2, s[40:43], s61 offen offset:0 nt lds\n\t"
    "v_mfma_f32_16x16x32_bf16 v[160:163], a[120:123], v[32:35], v[160:163]\n\t"
    "v_exp_f32 v55, v55\n\t"
    "v_mfma_f32_16x16x32_bf16 v[164:167], a[124:127], v[32:35], v[164:167]\n\t"
    "ds_read_b128 a[92:95], v11 offset:32768\n\t"
    "v_cvt_pk_bf16_f32 v51, v54, v55\n\t"
    "v_mfma_f32_16x16x32_bf16 v[168:171], a[128:131], v[32:35], v[168:171]\n\t"
    "v_exp_f32 v64, v64\n\t"
    "v_mfma_f32_16x16x32_bf16 v[172:175], a[132:135], v[32:35], v[172:175]\n\t"
    "ds_read_b128 a[96:99], v12 offset:32768\n\t"
    "ds_read_b128 a[100:103], v13 offset:32768\n\t"
    "v_mfma_f32_16x16x32_bf16 a[0:3], a[136:139], v[32:35], a[0:3]\n\t"
    "v_exp_f32 v65, v65\n\t"
    "v_mfma_f32_16x16x32_bf16 a[4:7], a[140:143], v[32:35], a[4:7]\n\t"
    "s_waitcnt lgkmcnt(5)\n\t"
    "buffer_load_dwordx4 v3, s[40:43], s61 offen offset:1024 nt lds\n\t"
    "v_mfma_f32_16x16x32_bf16 a[8:11], a[144:147], v[32:35], a[8:11]\n\t"
    "s_waitcnt lgkmcnt(3)\n\t"
    "ds_read_b128 a[104:107], v10 offset:36864\n\t"
    "v_cvt_pk_bf16_f32 v64, v64, v65\n\t"
    "v_mfma_f32_16x16x32_bf16 a[12:15], a[148:151], v[32:35], a[12:15]\n\t"
    "v_exp_f32 v66, v66\n\t"
    "v_mfma_f32_16x16x32_bf16 a[72:75], v[216:219], v[32:35], a[72:75]\n\t"
    "v_exp_f32 v67, v67\n\t"
    "v_mfma_f32_16x16x32_bf16 v[176:179], a[120:123], v[40:43], v[176:179]\n\t"
    "ds_read_b128 a[108:111], v11 offset:36864\n\t"
    "v_cvt_pk_bf16_f32 v65, v66, v67\n\t"
    "v_mfma_f32_16x16x32_bf16 v[180:183], a[124:127], v[40:43], v[180:183]\n\t"
    "v_exp_f32 v68, v68\n\t"
    "v_mfma_f32_16x16x32_bf16 v[184:187], a[128:131], v[40:43], v[184:187]\n\t"
    "buffer_load_dwordx4 v4, s[40:43], s61 offen offset:2048 nt lds\n\t"
    "v_mfma_f32_16x16x32_bf16 v[188:191], a[132:135], v[40:43], v[188:191]\n\t"
    "v_exp_f32 v69, v69\n\t"
    "v_mfma_f32_16x16x32_bf16 a[16:19], a[136:139], v[40:43], a[16:19]\n\t"
    "ds_read_b128 a[112:115], v12 offset:36864\n\t"
    "ds_read_b128 a[116:119], v13 offset:36864\n\t"
    "v_mfma_f32_16x16x32_bf16 a[20:23], a[140:143], v[40:43], a[20:23]\n\t"
    "v_exp_f32 v70, v70\n\t"
    "v_mfma_f32_16x16x32_bf16 a[24:27], a[144:147], v[40:43], a[24:27]\n\t"
    "v_exp_f32 v71, v71\n\t"
    "v_mfma_f32_16x16x32_bf16 a[28:31], a[148:151], v[40:43], a[28:31]\n\t"
    "v_cvt_pk_bf16_f32 v66, v68, v69\n\t"
    "v_cvt_pk_bf16_f32 v67, v70, v71\n\t"
    "v_mfma_f32_16x16x32_bf16 a[76:79], v[216:219], v[40:43], a[76:79]\n\t"
    "buffer_load_dwordx4 v5, s[40:43], s61 offen offset:3072 nt lds\n\t"
    "v_mfma_f32_16x16x32_bf16 v[192:195], a[120:123], v[48:51], v[192:195]\n\t"
    "s_add_u32 s60, s54, 40960\n\t"
    "v_exp_f32 v72, v72\n\t"
    "v_mfma_f32_16x16x32_bf16 v[196:199], a[124:127], v[48:51], v[196:199]\n\t"
    "s_add_u32 m0, s53, 16384\n\t"
    "v_exp_f32 v73, v73\n\t"
    "v_mfma_f32_16x16x32_bf16 v[200:203], a[128:131], v[48:51], v[200:203]\n\t"
    "v_exp_f32 v74, v74\n\t"
    "v_mfma_f32_16x16x32_bf16 v[204:207], a[132:135], v[48:51], v[204:207]\n\t"
    "v_exp_f32 v75, v75\n\t"
    "v_mfma_f32_16x16x32_bf16 a[32:35], a[136:139], v[48:51], a[32:35]\n\t"
    "v_exp_f32 v76, v76\n\t"
    "v_mfma_f32_16x16x32_bf16 a[36:39], a[140:143], v[48:51], a[36:39]\n\t"
    "buffer_load_dwordx4 v6, s[44:47], s60 offen offset:0 nt lds\n\t"
    "v_mfma_f32_16x16x32_bf16 a[40:43], a[144:147], v[48:51], a[40:43]\n\t"
    "v_exp_f32 v77, v77\n\t"
    "v_mfma_f32_16x16x32_bf16 a[44:47], a[148:151], v[48:51], a[44:47]\n\t"
    "v_cvt_pk_bf16_f32 v72, v72, v73\n\t"
    "v_cvt_pk_bf16_f32 v73, v74, v75\n\t"
    "v_mfma_f32_16x16x32_bf16 a[80:83], v[216:219], v[48:51], a[80:83]\n\t"
    "s_waitcnt vmcnt(9) lgkmcnt(0)\n\t"
    "s_barrier\n\t"
    "v_mfma_f32_16x16x32_bf16 v[32:35], a[88:91], v[96:99], 0\n\t"
    "ds_read_b64_tr_b16 a[120:121], v14 offset:512\n\t"
    "v_cvt_pk_bf16_f32 v74, v76, v77\n\t"
    "v_mfma_f32_16x16x32_bf16 v[40:43], a[88:91], v[112:115], 0\n\t"
    "v_exp_f32 v78, v78\n\t"
    "v_mfma_f32_16x16x32_bf16 v[48:51], a[88:91], v[128:131], 0\n\t"
    "buffer_load_dwordx4 v7, s[44:47], s60 offen offset:1024 nt lds\n\t"
    "v_mfma_f32_16x16x32_bf16 v[32:35], a[92:95], v[100:103], v[32:35]\n\t"
    "ds_read_b64_tr_b16 a[122:123], v14 offset:4608\n\t"
    "ds_read_b64_tr_b16 a[124:125], v15 offset:512\n\t"
    "v_mfma_f32_16x16x32_bf16 v[40:43], a[92:95], v[116:119], v[40:43]\n\t"
    "v_exp_f32 v79, v79\n\t"
    "v_mfma_f32_16x16x32_bf16 v[48:51], a[92:95], v[132:135], v[48:51]\n\t"
    "ds_read_b64_tr_b16 a[126:127], v15 offset:4608\n\t"
    "v_cvt_pk_bf16_f32 v75, v78, v79\n\t"
    "v_mfma_f32_16x16x32_bf16 v[32:35], a[96:99], v[104:107], v[32:35]\n\t"
    "v_exp_f32 v80, v80\n\t"
    "v_mfma_f32_16x16x32_bf16 v[40:43], a[96:99], v[120:123], v[40:43]\n\t"
    "ds_read_b64_tr_b16 a[128:129], v14 offset:0\n\t"
    "ds_read_b64_tr_b16 a[130:131], v14 offset:4096\n\t"
    "v_mfma_f32_16x16x32_bf16 v[48:51], a[96:99], v[136:139], v[48:51]\n\t"
    "buffer_load_dwordx4 v8, s[44:47], s60 offen offset:2048 nt lds\n\t"
    "v_mfma_f32_16x16x32_bf16 v[32:35], a[100:103], v[108:111], v[32:35]\n\t"
    "v_exp_f32 v81, v81\n\t"
    "v_mfma_f32_16x16x32_bf16 v[40:43], a[100:103], v[124:127], v[40:43]\n\t"
    "ds_read_b64_tr_b16 a[132:133], v15 offset:0\n\t"
    "ds_read_b64_tr_b16 a[134:135], v15 offset:4096\n\t"
    "v_mfma_f32_16x16x32_bf16 v[48:51], a[100:103], v[140:143], v[48:51]\n\t"
    "v_exp_f32 v82, v82\n\t"
    "v_mfma_f32_16x16x32_bf16 v[36:39], a[104:107], v[96:99], 0\n\t"
    "ds_read_b64_tr_b16 a[136:137], v16 offset:1536\n\t"
    "v_cvt_pk_bf16_f32 v80, v80, v81\n\t"
    "v_mfma_f32_16x16x32_bf16 v[44:47], a[104:107], v[112:115], 0\n\t"
    "v_exp_f32 v83, v83\n\t"
    "v_mfma_f32_16x16x32_bf16 v[52:55], a[104:107], v[128:131], 0\n\t"
    "buffer_load_dwordx4 v9, s[44:47], s60 offen offset:3072 nt lds\n\t"
    "v_mfma_f32_16x16x32_bf16 v[36:39], a[108:111], v[100:103], v[36:39]\n\t"
    "s_add_u32 s61, s54, 57344\n\t"
    "ds_read_b64_tr_b16 a[138:139], v16 offset:5632\n\t"
    "ds_read_b64_tr_b16 a[140:141], v17 offset:1536\n\t"
    "v_mfma_f32_16x16x32_bf16 v[44:47], a[108:111], v[116:119], v[44:47]\n\t"
    "s_add_u32 m0, s52, 16384\n\t"
    "ds_read_b64_tr_b16 a[142:143], v17 offset:5632\n\t"
    "v_cvt_pk_bf16_f32 v81, v82, v83\n\t"
    "v_mfma_f32_16x16x32_bf16 v[52:55], a[108:111], v[132:135], v[52:55]\n\t"
    "v_exp_f32 v84, v84\n\t"
    "v_mfma_f32_16x16x32_bf16 v[36:39], a[112:115], v[104:107], v[36:39]\n\t"
    "v_exp_f32 v85, v85\n\t"
    "v_mfma_f32_16x16x32_bf16 v[44:47], a[112:115], v[120:123], v[44:47]\n\t"
    "ds_read_b64_tr_b16 a[144:145], v16 offset:1024\n\t"
    "ds_read_b64_tr_b16 a[146:147], v16 offset:5120\n\t"
    "v_mfma_f32_16x16x32_bf16 v[52:55], a[112:115], v[136:139], v[52:55]\n\t"
    "buffer_load_dwordx4 v2, s[40:43], s61 offen offset:0 nt lds\n\t"
    "v_mfma_f32_16x16x32_bf16 v[36:39], a[116:119], v[108:111], v[36:39]\n\t"
    "ds_read_b64_tr_b16 a[148:149], v17 offset:1024\n\t"
    "v_cvt_pk_bf16_f32 v82, v84, v85\n\t"
    "v_mfma_f32_16x16x32_bf16 v[44:47], a[116:119], v[124:127], v[44:47]\n\t"
    "ds_read_b64_tr_b16 a[150:151], v17 offset:5120\n\t"
    "ds_read_b128 a[88:91], v10 offset:0\n\t"
    "v_mfma_f32_16x16x32_bf16 v[52:55], a[116:119], v[140:143], v[52:55]\n\t"
    "s_waitcnt lgkmcnt(6)\n\t"
    "v_exp_f32 v86, v86\n\t"
    "v_mfma_f32_16x16x32_bf16 v[160:163], a[120:123], v[64:67], v[160:163]\n\t"
    "v_exp_f32 v87, v87\n\t"
    "v_mfma_f32_16x16x32_bf16 v[164:167], a[124:127], v[64:67], v[164:167]\n\t"
    "ds_read_b128 a[92:95], v11 offset:0\n\t"
    "v_cvt_pk_bf16_f32 v83, v86, v87\n\t"
    "v_mfma_f32_16x16x32_bf16 v[168:171], a[128:131], v[64:67], v[168:171]\n\t"
    "buffer_load_dwordx4 v3, s[40:43], s61 offen offset:1024 nt lds\n\t"
    "v_mfma_f32_16x16x32_bf16 v[172:175], a[132:135], v[64:67], v[172:175]\n\t"
    "v_exp_f32 v88, v88\n\t"
    "v_mfma_f32_16x16x32_bf16 a[0:3], a[136:139], v[64:67], a[0:3]\n\t"
    "s_waitcnt lgkmcnt(4)\n\t"
    "v_exp_f32 v89, v89\n\t"
    "v_mfma_f32_16x16x32_bf16 a[4:7], a[140:143], v[64:67], a[4:7]\n\t"
    "v_exp_f32 v90, v90\n\t"
    "v_mfma_f32_16x16x32_bf16 a[8:11], a[144:147], v[64:67], a[8:11]\n\t"
    "s_waitcnt lgkmcnt(1)\n\t"
    "v_exp_f32 v91, v91\n\t"
    "v_mfma_f32_16x16x32_bf16 a[12:15], a[148:151], v[64:67], a[12:15]\n\t"
    "v_exp_f32 v92, v92\n\t"
    "v_mfma_f32_16x16x32_bf16 a[72:75], v[216:219], v[64:67], a[72:75]\n\t"
    "buffer_load_dwordx4 v4, s[40:43], s61 offen offset:2048 nt lds\n\t"
    "v_mfma_f32_16x16x32_bf16 v[176:179], a[120:123], v[72:75], v[176:179]\n\t"
    "v_exp_f32 v93, v93\n\t"
    "v_mfma_f32_16x16x32_bf16 v[180:183], a[124:127], v[72:75], v[180:183]\n\t"
    "v_cvt_pk_bf16_f32 v88, v88, v89\n\t"
    "v_cvt_pk_bf16_f32 v89, v90, v91\n\t"
    "v_mfma_f32_16x16x32_bf16 v[184:187], a[128:131], v[72:75], v[184:187]\n\t"
    "v_exp_f32 v94, v94\n\t"
    "v_mfma_f32_16x16x32_bf16 v[188:191], a[132:135], v[72:75], v[188:191]\n\t"
    "v_exp_f32 v95, v95\n\t"
    "v_mfma_f32_16x16x32_bf16 a[16:19], a[136:139], v[72:75], a[16:19]\n\t"
    "v_cvt_pk_bf16_f32 v90, v92, v93\n\t"
    "v_cvt_pk_bf16_f32 v91, v94, v95\n\t"
    "v_mfma_f32_16x16x32_bf16 a[20:23], a[140:143], v[72:75], a[20:23]\n\t"
    "buffer_load_dwordx4 v5, s[40:43], s61 offen offset:3072 nt lds\n\t"
    "v_mfma_f32_16x16x32_bf16 a[24:27], a[144:147], v[72:75], a[24:27]\n\t"
    "s_add_u32 s60, s54, 49152\n\t"
    "ds_read_b128 a[96:99], v12 offset:0\n\t"
    "ds_read_b128 a[100:103], v13 offset:0\n\t"
    "v_mfma_f32_16x16x32_bf16 a[28:31], a[148:151], v[72:75], a[28:31]\n\t"
    "s_add_u32 m0, s53, 24576\n\t"
    "ds_read_b128 a[104:107], v10 offset:4096\n\t"
    "ds_read_b128 a[108:111], v11 offset:4096\n\t"
    "v_mfma_f32_16x16x32_bf16 a[76:79], v[216:219], v[72:75], a[76:79]\n\t"
    "v_exp_f32 v32, v32\n\t"
    "v_mfma_f32_16x16x32_bf16 v[192:195], a[120:123], v[80:83], v[192:195]\n\t"
    "v_exp_f32 v33, v33\n\t"
    "v_mfma_f32_16x16x32_bf16 v[196:199], a[124:127], v[80:83], v[196:199]\n\t"
    "ds_read_b128 a[112:115], v12 offset:4096\n\t"
    "v_cvt_pk_bf16_f32 v32, v32, v33\n\t"
    "v_mfma_f32_16x16x32_bf16 v[200:203], a[128:131], v[80:83], v[200:203]\n\t"
    "buffer_load_dwordx4 v6, s[44:47], s60 offen offset:0 nt lds\n\t"
    "v_mfma_f32_16x16x32_bf16 v[204:207], a[132:135], v[80:83], v[204:207]\n\t"
    "v_exp_f32 v34, v34\n\t"
    "v_mfma_f32_16x16x32_bf16 a[32:35], a[136:139], v[80:83], a[32:35]\n\t"
    "v_exp_f32 v35, v35\n\t"
    "v_mfma_f32_16x16x32_bf16 a[36:39], a[140:143], v[80:83], a[36:39]\n\t"
    "ds_read_b128 a[116:119], v13 offset:4096\n\t"
    "v_cvt_pk_bf16_f32 v33, v34, v35\n\t"
    "v_mfma_f32_16x16x32_bf16 a[40:43], a[144:147], v[80:83], a[40:43]\n\t"
    "v_exp_f32 v36, v36\n\t"
    "v_mfma_f32_16x16x32_bf16 a[44:47], a[148:151], v[80:83], a[44:47]\n\t"
    "v_exp_f32 v37, v37\n\t"
    "v_mfma_f32_16x16x32_bf16 a[80:83], v[216:219], v[80:83], a[80:83]\n\t"
    "buffer_load_dwordx4 v7, s[44:47], s60 offen offset:1024 nt lds\n\t"
    "v_mfma_f32_16x16x32_bf16 v[208:211], a[120:123], v[88:91], v[208:211]\n\t"
    "v_exp_f32 v38, v38\n\t"
    "v_mfma_f32_16x16x32_bf16 v[212:215], a[124:127], v[88:91], v[212:215]\n\t"
    "v_exp_f32 v39, v39\n\t"
    "v_mfma_f32_16x16x32_bf16 a[48:51], a[128:131], v[88:91], a[48:51]\n\t"
    "v_cvt_pk_bf16_f32 v34, v36, v37\n\t"
    "v_cvt_pk_bf16_f32 v35, v38, v39\n\t"
    "v_mfma_f32_16x16x32_bf16 a[52:55], a[132:135], v[88:91], a[52:55]\n\t"
    "v_exp_f32 v40, v40\n\t"
    "v_mfma_f32_16x16x32_bf16 a[56:59], a[136:139], v[88:91], a[56:59]\n\t"
    "v_exp_f32 v41, v41\n\t"
    "v_mfma_f32_16x16x32_bf16 a[60:63], a[140:143], v[88:91], a[60:63]\n\t"
    "buffer_load_dwordx4 v8, s[44:47], s60 offen offset:2048 nt lds\n\t"
    "v_mfma_f32_16x16x32_bf16 a[64:67], a[144:147], v[88:91], a[64:67]\n\t"
    "v_exp_f32 v42, v42\n\t"
    "v_mfma_f32_16x16x32_bf16 a[68:71], a[148:151], v[88:91], a[68:71]\n\t"
    "v_exp_f32 v43, v43\n\t"
    "v_mfma_f32_16x16x32_bf16 a[84:87], v[216:219], v[88:91], a[84:87]\n\t"
    "s_waitcnt vmcnt(11) lgkmcnt(0)\n\t"
    "s_barrier\n\t"
    "v_mfma_f32_16x16x32_bf16 v[64:67], a[88:91], v[96:99], 0\n\t"
    "ds_read_b64_tr_b16 a[120:121], v14 offset:8704\n\t"
    "v_cvt_pk_bf16_f32 v40, v40, v41\n\t"
    "v_mfma_f32_16x16x32_bf16 v[72:75], a[88:91], v[112:115], 0\n\t"
    "v_exp_f32 v44, v44\n\t"
    "v_mfma_f32_16x16x32_bf16 v[80:83], a[88:91], v[128:131], 0\n\t"
    "buffer_load_dwordx4 v9, s[44:47], s60 offen offset:3072 nt lds\n\t"
    "v_mfma_f32_16x16x32_bf16 v[88:91], a[88:91], v[144:147], 0\n\t"
    "s_add_u32 s61, s54, 65536\n\t"
    "ds_read_b64_tr_b16 a[122:123], v14 offset:12800\n\t"
    "v_cvt_pk_bf16_f32 v41, v42, v43\n\t"
    "v_mfma_f32_16x16x32_bf16 v[64:67], a[92:95], v[100:103], v[64:67]\n\t"
    "s_add_u32 m0, s52, 24576\n\t"
    "v_exp_f32 v45, v45\n\t"
    "v_mfma_f32_16x16x32_bf16 v[72:75], a[92:95], v[116:119], v[72:75]\n\t"
    "ds_read_b64_tr_b16 a[124:125], v15 offset:8704\n\t"
    "v_cvt_pk_bf16_f32 v42, v44, v45\n\t"
    "v_mfma_f32_16x16x32_bf16 v[80:83], a[92:95], v[132:135], v[80:83]\n\t"
    "v_exp_f32 v46, v46\n\t"
    "v_mfma_f32_16x16x32_bf16 v[88:91], a[92:95], v[148:151], v[88:91]\n\t"
    "v_exp_f32 v47, v47\n\t"
    "v_mfma_f32_16x16x32_bf16 v[64:67], a[96:99], v[104:107], v[64:67]\n\t"
    "buffer_load_dwordx4 v2, s[40:43], s61 offen offset:0 nt lds\n\t"
    "v_mfma_f32_16x16x32_bf16 v[72:75], a[96:99], v[120:123], v[72:75]\n\t"
    "ds_read_b64_tr_b16 a[126:127], v15 offset:12800\n\t"
    "ds_read_b64_tr_b16 a[128:129], v14 offset:8192\n\t"
    "v_mfma_f32_16x16x32_bf16 v[80:83], a[96:99], v[136:139], v[80:83]\n\t"
    "ds_read_b64_tr_b16 a[130:131], v14 offset:12288\n\t"
    "v_cvt_pk_bf16_f32 v43, v46, v47\n\t"
    "v_mfma_f32_16x16x32_bf16 v[88:91], a[96:99], v[152:155], v[88:91]\n\t"
    "v_exp_f32 v48, v48\n\t"
    "v_mfma_f32_16x16x32_bf16 v[64:67], a[100:103], v[108:111], v[64:67]\n\t"
    "v_exp_f32 v49, v49\n\t"
    "v_mfma_f32_16x16x32_bf16 v[72:75], a[100:103], v[124:127], v[72:75]\n\t"
    "ds_read_b64_tr_b16 a[132:133], v15 offset:8192\n\t"
    "v_cvt_pk_bf16_f32 v48, v48, v49\n\t"
    "v_mfma_f32_16x16x32_bf16 v[80:83], a[100:103], v[140:143], v[80:83]\n\t"
    "buffer_load_dwordx4 v3, s[40:43], s61 offen offset:1024 nt lds\n\t"
    "v_mfma_f32_16x16x32_bf16 v[88:91], a[100:103], v[156:159], v[88:91]\n\t"
    "v_exp_f32 v50, v50\n\t"
    "v_mfma_f32_16x16x32_bf16 v[68:71], a[104:107], v[96:99], 0\n\t"
    "ds_read_b64_tr_b16 a[134:135], v15 offset:12288\n\t"
    "ds_read_b64_tr_b16 a[136:137], v16 offset:9728\n\t"
    "v_mfma_f32_16x16x32_bf16 v[76:79], a[104:107], v[112:115], 0\n\t"
    "v_exp_f32 v51, v51\n\t"
    "v_mfma_f32_16x16x32_bf16 v[84:87], a[104:107], v[128:131], 0\n\t"
    "ds_read_b64_tr_b16 a[138:139], v16 offset:13824\n\t"
    "v_cvt_pk_bf16_f32 v49, v50, v51\n\t"
    "v_mfma_f32_16x16x32_bf16 v[92:95], a[104:107], v[144:147], 0\n\t"
    "v_exp_f32 v52, v52\n\t"
    "v_mfma_f32_16x16x32_bf16 v[68:71], a[108:111], v[100:103], v[68:71]\n\t"
    "buffer_load_dwordx4 v4, s[40:43], s61 offen offset:2048 nt lds\n\t"
    "v_mfma_f32_16x16x32_bf16 v[76:79], a[108:111], v[116:119], v[76:79]\n\t"
    "v_exp_f32 v53, v53\n\t"
    "v_mfma_f32_16x16x32_bf16 v[84:87], a[108:111], v[132:135], v[84:87]\n\t"
    "ds_read_b64_tr_b16 a[140:141], v17 offset:9728\n\t"
    "ds_read_b64_tr_b16 a[142:143], v17 offset:13824\n\t"
    "v_mfma_f32_16x16x32_bf16 v[92:95], a[108:111], v[148:151], v[92:95]\n\t"
    "v_exp_f32 v54, v54\n\t"
    "v_mfma_f32_16x16x32_bf16 v[68:71], a[112:115], v[104:107], v[68:71]\n\t"
    "ds_read_b64_tr_b16 a[144:145], v16 offset:9216\n\t"
    "v_cvt_pk_bf16_f32 v50, v52, v53\n\t"
    "v_mfma_f32_16x16x32_bf16 v[76:79], a[112:115], v[120:123], v[76:79]\n\t"
    "v_exp_f32 v55, v55\n\t"
    "v_mfma_f32_16x16x32_bf16 v[84:87], a[112:115], v[136:139], v[84:87]\n\t"
    "ds_read_b64_tr_b16 a[146:147], v16 offset:13312\n\t"
    "v_cvt_pk_bf16_f32 v51, v54, v55\n\t"
    "v_mfma_f32_16x16x32_bf16 v[92:95], a[112:115], v[152:155], v[92:95]\n\t"
    "buffer_load_dwordx4 v5, s[40:43], s61 offen offset:3072 nt lds\n\t"
    "v_mfma_f32_16x16x32_bf16 v[68:71], a[116:119], v[108:111], v[68:71]\n\t"
    "s_add_u32 s60, s54, 57344\n\t"
    "ds_read_b64_tr_b16 a[148:149], v17 offset:9216\n\t"
    "ds_read_b128 a[88:91], v10 offset:8192\n\t"
    "v_mfma_f32_16x16x32_bf16 v[76:79], a[116:119], v[124:127], v[76:79]\n\t"
    "s_add_u32 m0, s53, 32768\n\t"
    "v_exp_f32 v64, v64\n\t"
    "v_mfma_f32_16x16x32_bf16 v[84:87], a[116:119], v[140:143], v[84:87]\n\t"
    "ds_read_b64_tr_b16 a[150:151], v17 offset:13312\n\t"
    "ds_read_b128 a[92:95], v11 offset:8192\n\t"
    "v_mfma_f32_16x16x32_bf16 v[92:95], a[116:119], v[156:159], v[92:95]\n\t"
    "s_waitcnt lgkmcnt(6)\n\t"
    "v_exp_f32 v65, v65\n\t"
    "v_mfma_f32_16x16x32_bf16 v[160:163], a[120:123], v[32:35], v[160:163]\n\t"
    "v_exp_f32 v66, v66\n\t"
    "v_mfma_f32_16x16x32_bf16 v[164:167], a[124:127], v[32:35], v[164:167]\n\t"
    "ds_read_b128 a[96:99], v12 offset:8192\n\t"
    "v_cvt_pk_bf16_f32 v64, v64, v65\n\t"
    "v_mfma_f32_16x16x32_bf16 v[168:171], a[128:131], v[32:35], v[168:171]\n\t"
    "v_exp_f32 v67, v67\n\t"
    "v_mfma_f32_16x16x32_bf16 v[172:175], a[132:135], v[32:35], v[172:175]\n\t"
    "ds_read_b128 a[100:103], v13 offset:8192\n\t"
    "v_cvt_pk_bf16_f32 v65, v66, v67\n\t"
    "v_mfma_f32_16x16x32_bf16 a[0:3], a[136:139], v[32:35], a[0:3]\n\t"
    "v_exp_f32 v68, v68\n\t"
    "v_mfma_f32_16x16x32_bf16 a[4:7], a[140:143], v[32:35], a[4:7]\n\t"
    "s_waitcnt lgkmcnt(4)\n\t"
    "v_exp_f32 v69, v69\n\t"
    "v_mfma_f32_16x16x32_bf16 a[8:11], a[144:147], v[32:35], a[8:11]\n\t"
    "s_waitcnt lgkmcnt(2)\n\t"
    "ds_read_b128 a[104:107], v10 offset:12288\n\t"
    "v_cvt_pk_bf16_f32 v66, v68, v69\n\t"
    "v_mfma_f32_16x16x32_bf16 a[12:15], a[148:151], v[32:35], a[12:15]\n\t"
    "v_exp_f32 v70, v70\n\t"
    "v_mfma_f32_16x16x32_bf16 a[72:75], v[216:219], v[32:35], a[72:75]\n\t"
    "v_exp_f32 v71, v71\n\t"
    "v_mfma_f32_16x16x32_bf16 v[176:179], a[120:123], v[40:43], v[176:179]\n\t"
    "ds_read_b128 a[108:111], v11 offset:12288\n\t"
    "v_cvt_pk_bf16_f32 v67, v70, v71\n\t"
    "v_mfma_f32_16x16x32_bf16 v[180:183], a[124:127], v[40:43], v[180:183]\n\t"
    "v_exp_f32 v72, v72\n\t"
    "v_mfma_f32_16x16x32_bf16 v[184:187], a[128:131], v[40:43], v[184:187]\n\t"
    "buffer_load_dwordx4 v6, s[44:47], s60 offen offset:0 nt lds\n\t"
    "v_mfma_f32_16x16x32_bf16 v[188:191], a[132:135], v[40:43], v[188:191]\n\t"
    "v_exp_f32 v73, v73\n\t"
    "v_mfma_f32_16x16x32_bf16 a[16:19], a[136:139], v[40:43], a[16:19]\n\t"
    "ds_read_b128 a[112:115], v12 offset:12288\n\t"
    "ds_read_b128 a[116:119], v13 offset:12288\n\t"
    "v_mfma_f32_16x16x32_bf16 a[20:23], a[140:143], v[40:43], a[20:23]\n\t"
    "v_exp_f32 v74, v74\n\t"
    "v_mfma_f32_16x16x32_bf16 a[24:27], a[144:147], v[40:43], a[24:27]\n\t"
    "v_exp_f32 v75, v75\n\t"
    "v_mfma_f32_16x16x32_bf16 a[28:31], a[148:151], v[40:43], a[28:31]\n\t"
    "v_exp_f32 v76, v76\n\t"
    "v_mfma_f32_16x16x32_bf16 a[76:79], v[216:219], v[40:43], a[76:79]\n\t"
    "buffer_load_dwordx4 v7, s[44:47], s60 offen offset:1024 nt lds\n\t"
    "v_mfma_f32_16x16x32_bf16 v[192:195], a[120:123], v[48:51], v[192:195]\n\t"
    "v_exp_f32 v77, v77\n\t"
    "v_mfma_f32_16x16x32_bf16 v[196:199], a[124:127], v[48:51], v[196:199]\n\t"
    "v_cvt_pk_bf16_f32 v72, v72, v73\n\t"
    "v_cvt_pk_bf16_f32 v73, v74, v75\n\t"
    "v_mfma_f32_16x16x32_bf16 v[200:203], a[128:131], v[48:51], v[200:203]\n\t"
    "v_exp_f32 v78, v78\n\t"
    "v_mfma_f32_16x16x32_bf16 v[204:207], a[132:135], v[48:51], v[204:207]\n\t"
    "v_exp_f32 v79, v79\n\t"
    "v_mfma_f32_16x16x32_bf16 a[32:35], a[136:139], v[48:51], a[32:35]\n\t"
    "v_cvt_pk_bf16_f32 v74, v76, v77\n\t"
    "v_cvt_pk_bf16_f32 v75, v78, v79\n\t"
    "v_mfma_f32_16x16x32_bf16 a[36:39], a[140:143], v[48:51], a[36:39]\n\t"
    "buffer_load_dwordx4 v8, s[44:47], s60 offen offset:2048 nt lds\n\t"
    "v_mfma_f32_16x16x32_bf16 a[40:43], a[144:147], v[48:51], a[40:43]\n\t"
    "v_exp_f32 v80, v80\n\t"
    "v_mfma_f32_16x16x32_bf16 a[44:47], a[148:151], v[48:51], a[44:47]\n\t"
    "v_exp_f32 v81, v81\n\t"
    "v_mfma_f32_16x16x32_bf16 a[80:83], v[216:219], v[48:51], a[80:83]\n\t"
    "s_waitcnt vmcnt(11) lgkmcnt(0)\n\t"
    "s_barrier\n\t"
    "v_mfma_f32_16x16x32_bf16 v[32:35], a[88:91], v[96:99], 0\n\t"
    "ds_read_b64_tr_b16 a[120:121], v14 offset:16896\n\t"
    "v_cvt_pk_bf16_f32 v80, v80, v81\n\t"
    "v_mfma_f32_16x16x32_bf16 v[40:43], a[88:91], v[112:115], 0\n\t"
    "v_exp_f32 v82, v82\n\t"
    "v_mfma_f32_16x16x32_bf16 v[48:51], a[88:91], v[128:131], 0\n\t"
    "buffer_load_dwordx4 v9, s[44:47], s60 offen offset:3072 nt lds\n\t"
    "v_mfma_f32_16x16x32_bf16 v[32:35], a[92:95], v[100:103], v[32:35]\n\t"
    "s_add_u32 s61, s54, 73728\n\t"
    "ds_read_b64_tr_b16 a[122:123], v14 offset:20992\n\t"
    "ds_read_b64_tr_b16 a[124:125], v15 offset:16896\n\t"
    "v_mfma_f32_16x16x32_bf16 v[40:43], a[92:95], v[116:119], v[40:43]\n\t"
    "s_add_u32 m0, s52, 32768\n\t"
    "v_exp_f32 v83, v83\n\t"
    "v_mfma_f32_16x16x32_bf16 v[48:51], a[92:95], v[132:135], v[48:51]\n\t"
    "ds_read_b64_tr_b16 a[126:127], v15 offset:20992\n\t"
    "v_cvt_pk_bf16_f32 v81, v82, v83\n\t"
    "v_mfma_f32_16x16x32_bf16 v[32:35], a[96:99], v[104:107], v[32:35]\n\t"
    "v_exp_f32 v84, v84\n\t"
    "v_mfma_f32_16x16x32_bf16 v[40:43], a[96:99], v[120:123], v[40:43]\n\t"
    "ds_read_b64_tr_b16 a[128:129], v14 offset:16384\n\t"
    "ds_read_b64_tr_b16 a[130:131], v14 offset:20480\n\t"
    "v_mfma_f32_16x16x32_bf16 v[48:51], a[96:99], v[136:139], v[48:51]\n\t"
    "buffer_load_dwordx4 v2, s[40:43], s61 offen offset:0 nt lds\n\t"
    "v_mfma_f32_16x16x32_bf16 v[32:35], a[100:103], v[108:111], v[32:35]\n\t"
    "v_exp_f32 v85, v85\n\t"
    "v_mfma_f32_16x16x32_bf16 v[40:43], a[100:103], v[124:127], v[40:43]\n\t"
    "ds_read_b64_tr_b16 a[132:133], v15 offset:16384\n\t"
    "ds_read_b64_tr_b16 a[134:135], v15 offset:20480\n\t"
    "v_mfma_f32_16x16x32_bf16 v[48:51], a[100:103], v[140:143], v[48:51]\n\t"
    "v_exp_f32 v86, v86\n\t"
    "v_mfma_f32_16x16x32_bf16 v[36:39], a[104:107], v[96:99], 0\n\t"
    "ds_read_b64_tr_b16 a[136:137], v16 offset:17920\n\t"
    "v_cvt_pk_bf16_f32 v82, v84, v85\n\t"
    "v_mfma_f32_16x16x32_bf16 v[44:47], a[104:107], v[112:115], 0\n\t"
    "v_exp_f32 v87, v87\n\t"
    "v_mfma_f32_16x16x32_bf16 v[52:55], a[104:107], v[128:131], 0\n\t"
    "ds_read_b64_tr_b16 a[138:139], v16 offset:22016\n\t"
    "v_cvt_pk_bf16_f32 v83, v86, v87\n\t"
    "v_mfma_f32_16x16x32_bf16 v[36:39], a[108:111], v[100:103], v[36:39]\n\t"
    "buffer_load_dwordx4 v3, s[40:43], s61 offen offset:1024 nt lds\n\t"
    "v_mfma_f32_16x16x32_bf16 v[44:47], a[108:111], v[116:119], v[44:47]\n\t"
    "ds_read_b64_tr_b16 a[140:141], v17 offset:17920\n\t"
    "ds_read_b64_tr_b16 a[142:143], v17 offset:22016\n\t"
    "v_mfma_f32_16x16x32_bf16 v[52:55], a[108:111], v[132:135], v[52:55]\n\t"
    "v_exp_f32 v88, v88\n\t"
    "v_mfma_f32_16x16x32_bf16 v[36:39], a[112:115], v[104:107], v[36:39]\n\t"
    "v_exp_f32 v89, v89\n\t"
    "v_mfma_f32_16x16x32_bf16 v[44:47], a[112:115], v[120:123], v[44:47]\n\t"
    "ds_read_b64_tr_b16 a[144:145], v16 offset:17408\n\t"
    "ds_read_b64_tr_b16 a[146:147], v16 offset:21504\n\t"
    "v_mfma_f32_16x16x32_bf16 v[52:55], a[112:115], v[136:139], v[52:55]\n\t"
    "v_exp_f32 v90, v90\n\t"
    "v_mfma_f32_16x16x32_bf16 v[36:39], a[116:119], v[108:111], v[36:39]\n\t"
    "ds_read_b64_tr_b16 a[148:149], v17 offset:17408\n\t"
    "v_cvt_pk_bf16_f32 v88, v88, v89\n\t"
    "v_mfma_f32_16x16x32_bf16 v[44:47], a[116:119], v[124:127], v[44:47]\n\t"
    "ds_read_b64_tr_b16 a[150:151], v17 offset:21504\n\t"
    "ds_read_b128 a[88:91], v10 offset:16384\n\t"
    "v_mfma_f32_16x16x32_bf16 v[52:55], a[116:119], v[140:143], v[52:55]\n\t"
    "s_waitcnt lgkmcnt(7)\n\t"
    "buffer_load_dwordx4 v4, s[40:43], s61 offen offset:2048 nt lds\n\t"
    "v_mfma_f32_16x16x32_bf16 v[160:163], a[120:123], v[64:67], v[160:163]\n\t"
    "v_exp_f32 v91, v91\n\t"
    "v_mfma_f32_16x16x32_bf16 v[164:167], a[124:127], v[64:67], v[164:167]\n\t"
    "v_exp_f32 v92, v92\n\t"
    "v_mfma_f32_16x16x32_bf16 v[168:171], a[128:131], v[64:67], v[168:171]\n\t"
    "v_exp_f32 v93, v93\n\t"
    "v_mfma_f32_16x16x32_bf16 v[172:175], a[132:135], v[64:67], v[172:175]\n\t"
    "v_cvt_pk_bf16_f32 v89, v90, v91\n\t"
    "v_cvt_pk_bf16_f32 v90, v92, v93\n\t"
    "v_mfma_f32_16x16x32_bf16 a[0:3], a[136:139], v[64:67], a[0:3]\n\t"
    "s_waitcnt lgkmcnt(3)\n\t"
    "v_exp_f32 v94, v94\n\t"
    "v_mfma_f32_16x16x32_bf16 a[4:7], a[140:143], v[64:67], a[4:7]\n\t"
    "s_waitcnt lgkmcnt(1)\n\t"
    "buffer_load_dwordx4 v5, s[40:43], s61 offen offset:3072 nt lds\n\t"
    "v_mfma_f32_16x16x32_bf16 a[8:11], a[144:147], v[64:67], a[8:11]\n\t"
    "s_add_u32 s60, s54, 65536\n\t"
    "v_exp_f32 v95, v95\n\t"
    "v_mfma_f32_16x16x32_bf16 a[12:15], a[148:151], v[64:67], a[12:15]\n\t"
    "s_add_u32 m0, s53, 0\n\t"
    "ds_read_b128 a[92:95], v11 offset:16384\n\t"
    "v_cvt_pk_bf16_f32 v91, v94, v95\n\t"
    "v_mfma_f32_16x16x32_bf16 a[72:75], v[216:219], v[64:67], a[72:75]\n\t"
    "ds_read_b128 a[96:99], v12 offset:16384\n\t"
    "ds_read_b128 a[100:103], v13 offset:16384\n\t"
    "v_mfma_f32_16x16x32_bf16 v[176:179], a[120:123], v[72:75], v[176:179]\n\t"
    "v_exp_f32 v32, v32\n\t"
    "v_mfma_f32_16x16x32_bf16 v[180:183], a[124:127], v[72:75], v[180:183]\n\t"
    "v_exp_f32 v33, v33\n\t"
    "v_mfma_f32_16x16x32_bf16 v[184:187], a[128:131], v[72:75], v[184:187]\n\t"
    "ds_read_b128 a[104:107], v10 offset:20480\n\t"
    "v_cvt_pk_bf16_f32 v32, v32, v33\n\t"
    "v_mfma_f32_16x16x32_bf16 v[188:191], a[132:135], v[72:75], v[188:191]\n\t"
    "v_exp_f32 v34, v34\n\t"
    "v_mfma_f32_16x16x32_bf16 a[16:19], a[136:139], v[72:75], a[16:19]\n\t"
    "v_exp_f32 v35, v35\n\t"
    "v_mfma_f32_16x16x32_bf16 a[20:23], a[140:143], v[72:75], a[20:23]\n\t"
    "ds_read_b128 a[108:111], v11 offset:20480\n\t"
    "v_cvt_pk_bf16_f32 v33, v34, v35\n\t"
    "v_mfma_f32_16x16x32_bf16 a[24:27], a[144:147], v[72:75], a[24:27]\n\t"
    "v_exp_f32 v36, v36\n\t"
    "v_mfma_f32_16x16x32_bf16 a[28:31], a[148:151], v[72:75], a[28:31]\n\t"
    "v_exp_f32 v37, v37\n\t"
    "v_mfma_f32_16x16x32_bf16 a[76:79], v[216:219], v[72:75], a[76:79]\n\t"
    "ds_read_b128 a[112:115], v12 offset:20480\n\t"
    "v_cvt_pk_bf16_f32 v34, v36, v37\n\t"
    "v_mfma_f32_16x16x32_bf16 v[192:195], a[120:123], v[80:83], v[192:195]\n\t"
    "v_exp_f32 v38, v38\n\t"
    "v_mfma_f32_16x16x32_bf16 v[196:199], a[124:127], v[80:83], v[196:199]\n\t"
    "v_exp_f32 v39, v39\n\t"
    "v_mfma_f32_16x16x32_bf16 v[200:203], a[128:131], v[80:83], v[200:203]\n\t"
    "buffer_load_dwordx4 v6, s[44:47], s60 offen offset:0 nt lds\n\t"
    "v_mfma_f32_16x16x32_bf16 v[204:207], a[132:135], v[80:83], v[204:207]\n\t"
    "ds_read_b128 a[116:119], v13 offset:20480\n\t"
    "v_cvt_pk_bf16_f32 v35, v38, v39\n\t"
    "v_mfma_f32_16x16x32_bf16 a[32:35], a[136:139], v[80:83], a[32:35]\n\t"
    "v_exp_f32 v40, v40\n\t"
    "v_mfma_f32_16x16x32_bf16 a[36:39], a[140:143], v[80:83], a[36:39]\n\t"
    "v_exp_f32 v41, v41\n\t"
    "v_mfma_f32_16x16x32_bf16 a[40:43], a[144:147], v[80:83], a[40:43]\n\t"
    "v_exp_f32 v42, v42\n\t"
    "v_mfma_f32_16x16x32_bf16 a[44:47], a[148:151], v[80:83], a[44:47]\n\t"
    "v_exp_f32 v43, v43\n\t"
    "v_mfma_f32_16x16x32_bf16 a[80:83], v[216:219], v[80:83], a[80:83]\n\t"
    "buffer_load_dwordx4 v7, s[44:47], s60 offen offset:1024 nt lds\n\t"
    "v_mfma_f32_16x16x32_bf16 v[208:211], a[120:123], v[88:91], v[208:211]\n\t"
    "v_exp_f32 v44, v44\n\t"
    "v_mfma_f32_16x16x32_bf16 v[212:215], a[124:127], v[88:91], v[212:215]\n\t"
    "v_exp_f32 v45, v45\n\t"
    "v_mfma_f32_16x16x32_bf16 a[48:51], a[128:131], v[88:91], a[48:51]\n\t"
    "v_cvt_pk_bf16_f32 v40, v40, v41\n\t"
    "v_cvt_pk_bf16_f32 v41, v42, v43\n\t"
    "v_mfma_f32_16x16x32_bf16 a[52:55], a[132:135], v[88:91], a[52:55]\n\t"
    "v_exp_f32 v46, v46\n\t"
    "v_mfma_f32_16x16x32_bf16 a[56:59], a[136:139], v[88:91], a[56:59]\n\t"
    "v_exp_f32 v47, v47\n\t"
    "v_mfma_f32_16x16x32_bf16 a[60:63], a[140:143], v[88:91], a[60:63]\n\t"
    "buffer_load_dwordx4 v8, s[44:47], s60 offen offset:2048 nt lds\n\t"
    "v_mfma_f32_16x16x32_bf16 a[64:67], a[144:147], v[88:91], a[64:67]\n\t"
    "v_cvt_pk_bf16_f32 v42, v44, v45\n\t"
    "v_cvt_pk_bf16_f32 v43, v46, v47\n\t"
    "v_mfma_f32_16x16x32_bf16 a[68:71], a[148:151], v[88:91], a[68:71]\n\t"
    "v_exp_f32 v48, v48\n\t"
    "v_mfma_f32_16x16x32_bf16 a[84:87], v[216:219], v[88:91], a[84:87]\n\t"
    "s_waitcnt vmcnt(11) lgkmcnt(0)\n\t"
    "s_barrier\n\t"
    "v_mfma_f32_16x16x32_bf16 v[64:67], a[88:91], v[96:99], 0\n\t"
    "v_exp_f32 v49, v49\n\t"
    "v_mfma_f32_16x16x32_bf16 v[72:75], a[88:91], v[112:115], 0\n\t"
    "ds_read_b64_tr_b16 a[120:121], v14 offset:25088\n\t"
    "v_cvt_pk_bf16_f32 v48, v48, v49\n\t"
    "v_mfma_f32_16x16x32_bf16 v[80:83], a[88:91], v[128:131], 0\n\t"
    "buffer_load_dwordx4 v9, s[44:47], s60 offen offset:3072 nt lds\n\t"
    "v_mfma_f32_16x16x32_bf16 v[88:91], a[88:91], v[144:147], 0\n\t"
    "s_add_u32 s61, s54, 81920\n\t"
    "v_exp_f32 v50, v50\n\t"
    "v_mfma_f32_16x16x32_bf16 v[64:67], a[92:95], v[100:103], v[64:67]\n\t"
    "s_add_u32 m0, s52, 0\n\t"
    "ds_read_b64_tr_b16 a[122:123], v14 offset:29184\n\t"
    "ds_read_b64_tr_b16 a[124:125], v15 offset:25088\n\t"
    "v_mfma_f32_16x16x32_bf16 v[72:75], a[92:95], v[116:119], v[72:75]\n\t"
    "v_exp_f32 v51, v51\n\t"
    "v_mfma_f32_16x16x32_bf16 v[80:83], a[92:95], v[132:135], v[80:83]\n\t"
    "ds_read_b64_tr_b16 a[126:127], v15 offset:29184\n\t"
    "v_cvt_pk_bf16_f32 v49, v50, v51\n\t"
    "v_mfma_f32_16x16x32_bf16 v[88:91], a[92:95], v[148:151], v[88:91]\n\t"
    "v_exp_f32 v52, v52\n\t"
    "v_mfma_f32_16x16x32_bf16 v[64:67], a[96:99], v[104:107], v[64:67]\n\t"
    "buffer_load_dwordx4 v2, s[40:43], s61 offen offset:0 nt lds\n\t"
    "v_mfma_f32_16x16x32_bf16 v[72:75], a[96:99], v[120:123], v[72:75]\n\t"
    "v_exp_f32 v53, v53\n\t"
    "v_mfma_f32_16x16x32_bf16 v[80:83], a[96:99], v[136:139], v[80:83]\n\t"
    "ds_read_b64_tr_b16 a[128:129], v14 offset:24576\n\t"
    "ds_read_b64_tr_b16 a[130:131], v14 offset:28672\n\t"
    "v_mfma_f32_16x16x32_bf16 v[88:91], a[96:99], v[152:155], v[88:91]\n\t"
    "v_exp_f32 v54, v54\n\t"
    "v_mfma_f32_16x16x32_bf16 v[64:67], a[100:103], v[108:111], v[64:67]\n\t"
    "ds_read_b64_tr_b16 a[132:133], v15 offset:24576\n\t"
    "v_cvt_pk_bf16_f32 v50, v52, v53\n\t"
    "v_mfma_f32_16x16x32_bf16 v[72:75], a[100:103], v[124:127], v[72:75]\n\t"
    "v_exp_f32 v55, v55\n\t"
    "v_mfma_f32_16x16x32_bf16 v[80:83], a[100:103], v[140:143], v[80:83]\n\t"
    "buffer_load_dwordx4 v3, s[40:43], s61 offen offset:1024 nt lds\n\t"
    "v_mfma_f32_16x16x32_bf16 v[88:91], a[100:103], v[156:159], v[88:91]\n\t"
    "ds_read_b64_tr_b16 a[134:135], v15 offset:28672\n\t"
    "v_cvt_pk_bf16_f32 v51, v54, v55\n\t"
    "v_mfma_f32_16x16x32_bf16 v[68:71], a[104:107], v[96:99], 0\n\t"
    "ds_read_b64_tr_b16 a[136:137], v16 offset:26112\n\t"
    "v_mfma_f32_16x16x32_bf16 v[76:79], a[104:107], v[112:115], 0\n\t"
    "v_exp_f32 v64, v64\n\t"
    "v_mfma_f32_16x16x32_bf16 v[84:87], a[104:107], v[128:131], 0\n\t"
    "v_exp_f32 v65, v65\n\t"
    "v_mfma_f32_16x16x32_bf16 v[92:95], a[104:107], v[144:147], 0\n\t"
    "ds_read_b64_tr_b16 a[138:139], v16 offset:30208\n\t"
    "v_cvt_pk_bf16_f32 v64, v64, v65\n\t"
    "v_mfma_f32_16x16x32_bf16 v[68:71], a[108:111], v[100:103], v[68:71]\n\t"
    "buffer_load_dwordx4 v4, s[40:43], s61 offen offset:2048 nt lds\n\t"
    "v_mfma_f32_16x16x32_bf16 v[76:79], a[108:111], v[116:119], v[76:79]\n\t"
    "v_exp_f32 v66, v66\n\t"
    "v_mfma_f32_16x16x32_bf16 v[84:87], a[108:111], v[132:135], v[84:87]\n\t"
    "ds_read_b64_tr_b16 a[140:141], v17 offset:26112\n\t"
    "ds_read_b64_tr_b16 a[142:143], v17 offset:30208\n\t"
    "v_mfma_f32_16x16x32_bf16 v[92:95], a[108:111], v[148:151], v[92:95]\n\t"
    "v_exp_f32 v67, v67\n\t"
    "v_mfma_f32_16x16x32_bf16 v[68:71], a[112:115], v[104:107], v[68:71]\n\t"
    "ds_read_b64_tr_b16 a[144:145], v16 offset:25600\n\t"
    "v_cvt_pk_bf16_f32 v65, v66, v67\n\t"
    "v_mfma_f32_16x16x32_bf16 v[76:79], a[112:115], v[120:123], v[76:79]\n\t"
    "v_exp_f32 v72, v72\n\t"
    "v_mfma_f32_16x16x32_bf16 v[84:87], a[112:115], v[136:139], v[84:87]\n\t"
    "buffer_load_dwordx4 v5, s[40:43], s61 offen offset:3072 nt lds\n\t"
    "v_mfma_f32_16x16x32_bf16 v[92:95], a[112:115], v[152:155], v[92:95]\n\t"
    "s_add_u32 s60, s54, 73728\n\t"
    "v_exp_f32 v73, v73\n\t"
    "v_mfma_f32_16x16x32_bf16 v[68:71], a[116:119], v[108:111], v[68:71]\n\t"
    "s_add_u32 m0, s53, 8192\n\t"
    "ds_read_b64_tr_b16 a[146:147], v16 offset:29696\n\t"
    "ds_read_b64_tr_b16 a[148:149], v17 offset:25600\n\t"
    "v_mfma_f32_16x16x32_bf16 v[76:79], a[116:119], v[124:127], v[76:79]\n\t"
    "ds_read_b128 a[88:91], v10 offset:24576\n\t"
    "v_cvt_pk_bf16_f32 v72, v72, v73\n\t"
    "v_mfma_f32_16x16x32_bf16 v[84:87], a[116:119], v[140:143], v[84:87]\n\t"
    "ds_read_b64_tr_b16 a[150:151], v17 offset:29696\n\t"
    "ds_read_b128 a[92:95], v11 offset:24576\n\t"
    "v_mfma_f32_16x16x32_bf16 v[92:95], a[116:119], v[156:159], v[92:95]\n\t"
    "s_waitcnt lgkmcnt(6)\n\t"
    "v_exp_f32 v74, v74\n\t"
    "v_mfma_f32_16x16x32_bf16 v[160:163], a[120:123], v[32:35], v[160:163]\n\t"
    "v_exp_f32 v75, v75\n\t"
    "v_mfma_f32_16x16x32_bf16 v[164:167], a[124:127], v[32:35], v[164:167]\n\t"
    "ds_read_b128 a[96:99], v12 offset:24576\n\t"
    "v_cvt_pk_bf16_f32 v73, v74, v75\n\t"
    "v_mfma_f32_16x16x32_bf16 v[168:171], a[128:131], v[32:35], v[168:171]\n\t"
    "v_exp_f32 v68, v68\n\t"
    "v_mfma_f32_16x16x32_bf16 v[172:175], a[132:135], v[32:35], v[172:175]\n\t"
    "v_exp_f32 v69, v69\n\t"
    "v_mfma_f32_16x16x32_bf16 a[0:3], a[136:139], v[32:35], a[0:3]\n\t"
    "ds_read_b128 a[100:103], v13 offset:24576\n\t"
    "v_cvt_pk_bf16_f32 v66, v68, v69\n\t"
    "v_mfma_f32_16x16x32_bf16 a[4:7], a[140:143], v[32:35], a[4:7]\n\t"
    "s_waitcnt lgkmcnt(4)\n\t"
    "v_exp_f32 v70, v70\n\t"
    "v_mfma_f32_16x16x32_bf16 a[8:11], a[144:147], v[32:35], a[8:11]\n\t"
    "s_waitcnt lgkmcnt(2)\n\t"
    "v_exp_f32 v71, v71\n\t"
    "v_mfma_f32_16x16x32_bf16 a[12:15], a[148:151], v[32:35], a[12:15]\n\t"
    "ds_read_b128 a[104:107], v10 offset:28672\n\t"
    "v_cvt_pk_bf16_f32 v67, v70, v71\n\t"
    "v_mfma_f32_16x16x32_bf16 a[72:75], v[216:219], v[32:35], a[72:75]\n\t"
    "v_exp_f32 v76, v76\n\t"
    "v_mfma_f32_16x16x32_bf16 v[176:179], a[120:123], v[40:43], v[176:179]\n\t"
    "v_exp_f32 v77, v77\n\t"
    "v_mfma_f32_16x16x32_bf16 v[180:183], a[124:127], v[40:43], v[180:183]\n\t"
    "ds_read_b128 a[108:111], v11 offset:28672\n\t"
    "v_cvt_pk_bf16_f32 v74, v76, v77\n\t"
    "v_mfma_f32_16x16x32_bf16 v[184:187], a[128:131], v[40:43], v[184:187]\n\t"
    "buffer_load_dwordx4 v6, s[44:47], s60 offen offset:0 nt lds\n\t"
    "v_mfma_f32_16x16x32_bf16 v[188:191], a[132:135], v[40:43], v[188:191]\n\t"
    "v_exp_f32 v78, v78\n\t"
    "v_mfma_f32_16x16x32_bf16 a[16:19], a[136:139], v[40:43], a[16:19]\n\t"
    "ds_read_b128 a[112:115], v12 offset:28672\n\t"
    "ds_read_b128 a[116:119], v13 offset:28672\n\t"
    "v_mfma_f32_16x16x32_bf16 a[20:23], a[140:143], v[40:43], a[20:23]\n\t"
    "v_exp_f32 v79, v79\n\t"
    "v_mfma_f32_16x16x32_bf16 a[24:27], a[144:147], v[40:43], a[24:27]\n\t"
    "v_exp_f32 v80, v80\n\t"
    "v_mfma_f32_16x16x32_bf16 a[28:31], a[148:151], v[40:43], a[28:31]\n\t"
    "v_exp_f32 v81, v81\n\t"
    "v_mfma_f32_16x16x32_bf16 a[76:79], v[216:219], v[40:43], a[76:79]\n\t"
    "buffer_load_dwordx4 v7, s[44:47], s60 offen offset:1024 nt lds\n\t"
    "v_mfma_f32_16x16x32_bf16 v[192:195], a[120:123], v[48:51], v[192:195]\n\t"
    "v_cvt_pk_bf16_f32 v75, v78, v79\n\t"
    "v_cvt_pk_bf16_f32 v80, v80, v81\n\t"
    "v_mfma_f32_16x16x32_bf16 v[196:199], a[124:127], v[48:51], v[196:199]\n\t"
    "v_exp_f32 v82, v82\n\t"
    "v_mfma_f32_16x16x32_bf16 v[200:203], a[128:131], v[48:51], v[200:203]\n\t"
    "v_exp_f32 v83, v83\n\t"
    "v_mfma_f32_16x16x32_bf16 v[204:207], a[132:135], v[48:51], v[204:207]\n\t"
    "v_exp_f32 v84, v84\n\t"
    "v_mfma_f32_16x16x32_bf16 a[32:35], a[136:139], v[48:51], a[32:35]\n\t"
    "v_exp_f32 v85, v85\n\t"
    "v_mfma_f32_16x16x32_bf16 a[36:39], a[140:143], v[48:51], a[36:39]\n\t"
    "buffer_load_dwordx4 v8, s[44:47], s60 offen offset:2048 nt lds\n\t"
    "v_mfma_f32_16x16x32_bf16 a[40:43], a[144:147], v[48:51], a[40:43]\n\t"
    "v_cvt_pk_bf16_f32 v81, v82, v83\n\t"
    "v_cvt_pk_bf16_f32 v82, v84, v85\n\t"
    "v_mfma_f32_16x16x32_bf16 a[44:47], a[148:151], v[48:51], a[44:47]\n\t"
    "v_exp_f32 v86, v86\n\t"
    "v_mfma_f32_16x16x32_bf16 a[80:83], v[216:219], v[48:51], a[80:83]\n\t"
    "s_waitcnt vmcnt(11) lgkmcnt(0)\n\t"
    "s_barrier\n\t"
    "v_mfma_f32_16x16x32_bf16 v[32:35], a[88:91], v[96:99], 0\n\t"
    "v_exp_f32 v87, v87\n\t"
    "v_mfma_f32_16x16x32_bf16 v[40:43], a[88:91], v[112:115], 0\n\t"
    "ds_read_b64_tr_b16 a[120:121], v14 offset:33280\n\t"
    "ds_read_b64_tr_b16 a[122:123], v14 offset:37376\n\t"
    "v_mfma_f32_16x16x32_bf16 v[48:51], a[88:91], v[128:131], 0\n\t"
    "buffer_load_dwordx4 v9, s[44:47], s60 offen offset:3072 nt lds\n\t"
    "v_mfma_f32_16x16x32_bf16 v[32:35], a[92:95], v[100:103], v[32:35]\n\t"
    "s_add_u32 s61, s54, 90112\n\t"
    "ds_read_b64_tr_b16 a[124:125], v15 offset:33280\n\t"
    "v_cvt_pk_bf16_f32 v83, v86, v87\n\t"
    "v_mfma_f32_16x16x32_bf16 v[40:43], a[92:95], v[116:119], v[40:43]\n\t"
    "s_add_u32 m0, s52, 8192\n\t"
    "v_exp_f32 v88, v88\n\t"
    "v_mfma_f32_16x16x32_bf16 v[48:51], a[92:95], v[132:135], v[48:51]\n\t"
    "v_exp_f32 v89, v89\n\t"
    "v_mfma_f32_16x16x32_bf16 v[32:35], a[96:99], v[104:107], v[32:35]\n\t"
    "ds_read_b64_tr_b16 a[126:127], v15 offset:37376\n\t"
    "ds_read_b64_tr_b16 a[128:129], v14 offset:32768\n\t"
    "v_mfma_f32_16x16x32_bf16 v[40:43], a[96:99], v[120:123], v[40:43]\n\t"
    "ds_read_b64_tr_b16 a[130:131], v14 offset:36864\n\t"
    "v_cvt_pk_bf16_f32 v88, v88, v89\n\t"
    "v_mfma_f32_16x16x32_bf16 v[48:51], a[96:99], v[136:139], v[48:51]\n\t"
    "buffer_load_dwordx4 v2, s[40:43], s61 offen offset:0 nt lds\n\t"
    "v_mfma_f32_16x16x32_bf16 v[32:35], a[100:103], v[108:111], v[32:35]\n\t"
    "v_exp_f32 v90, v90\n\t"
    "v_mfma_f32_16x16x32_bf16 v[40:43], a[100:103], v[124:127], v[40:43]\n\t"
    "ds_read_b64_tr_b16 a[132:133], v15 offset:32768\n\t"
    "ds_read_b64_tr_b16 a[134:135], v15 offset:36864\n\t"
    "v_mfma_f32_16x16x32_bf16 v[48:51], a[100:103], v[140:143], v[48:51]\n\t"
    "v_exp_f32 v91, v91\n\t"
    "v_mfma_f32_16x16x32_bf16 v[36:39], a[104:107], v[96:99], 0\n\t"
    "ds_read_b64_tr_b16 a[136:137], v16 offset:34304\n\t"
    "v_cvt_pk_bf16_f32 v89, v90, v91\n\t"
    "v_mfma_f32_16x16x32_bf16 v[44:47], a[104:107], v[112:115], 0\n\t"
    "v_exp_f32 v92, v92\n\t"
    "v_mfma_f32_16x16x32_bf16 v[52:55], a[104:107], v[128:131], 0\n\t"
    "buffer_load_dwordx4 v3, s[40:43], s61 offen offset:1024 nt lds\n\t"
    "v_mfma_f32_16x16x32_bf16 v[36:39], a[108:111], v[100:103], v[36:39]\n\t"
    "ds_read_b64_tr_b16 a[138:139], v16 offset:38400\n\t"
    "ds_read_b64_tr_b16 a[140:141], v17 offset:34304\n\t"
    "v_mfma_f32_16x16x32_bf16 v[44:47], a[108:111], v[116:119], v[44:47]\n\t"
    "v_exp_f32 v93, v93\n\t"
    "v_mfma_f32_16x16x32_bf16 v[52:55], a[108:111], v[132:135], v[52:55]\n\t"
    "ds_read_b64_tr_b16 a[142:143], v17 offset:38400\n\t"
    "v_cvt_pk_bf16_f32 v90, v92, v93\n\t"
    "v_mfma_f32_16x16x32_bf16 v[36:39], a[112:115], v[104:107], v[36:39]\n\t"
    "v_exp_f32 v94, v94\n\t"
    "v_mfma_f32_16x16x32_bf16 v[44:47], a[112:115], v[120:123], v[44:47]\n\t"
    "ds_read_b64_tr_b16 a[144:145], v16 offset:33792\n\t"
    "ds_read_b64_tr_b16 a[146:147], v16 offset:37888\n\t"
    "v_mfma_f32_16x16x32_bf16 v[52:55], a[112:115], v[136:139], v[52:55]\n\t"
    "buffer_load_dwordx4 v4, s[40:43], s61 offen offset:2048 nt lds\n\t"
    "v_mfma_f32_16x16x32_bf16 v[36:39], a[116:119], v[108:111], v[36:39]\n\t"
    "ds_read_b64_tr_b16 a[148:149], v17 offset:33792\n\t"
    "ds_read_b128 a[88:91], v10 offset:32768\n\t"
    "v_mfma_f32_16x16x32_bf16 v[44:47], a[116:119], v[124:127], v[44:47]\n\t"
    "ds_read_b64_tr_b16 a[150:151], v17 offset:37888\n\t"
    "v_mfma_f32_16x16x32_bf16 v[52:55], a[116:119], v[140:143], v[52:55]\n\t"
    "s_waitcnt lgkmcnt(6)\n\t"
    "v_exp_f32 v95, v95\n\t"
    "v_mfma_f32_16x16x32_bf16 v[160:163], a[120:123], v[64:67], v[160:163]\n\t"
    "ds_read_b128 a[92:95], v11 offset:32768\n\t"
    "v_cvt_pk_bf16_f32 v91, v94, v95\n\t"
    "v_mfma_f32_16x16x32_bf16 v[164:167], a[124:127], v[64:67], v[164:167]\n\t"
    "v_exp_f32 v32, v32\n\t"
    "v_mfma_f32_16x16x32_bf16 v[168:171], a[128:131], v[64:67], v[168:171]\n\t"
    "s_waitcnt lgkmcnt(6)\n\t"
    "buffer_load_dwordx4 v5, s[40:43], s61 offen offset:3072 nt lds\n\t"
    "v_mfma_f32_16x16x32_bf16 v[172:175], a[132:135], v[64:67], v[172:175]\n\t"
    "s_add_u32 s60, s54, 81920\n\t"
    "v_exp_f32 v33, v33\n\t"
    "v_mfma_f32_16x16x32_bf16 a[0:3], a[136:139], v[64:67], a[0:3]\n\t"
    "s_add_u32 m0, s53, 16384\n\t"
    "ds_read_b128 a[96:99], v12 offset:32768\n\t"
    "v_cvt_pk_bf16_f32 v32, v32, v33\n\t"
    "v_mfma_f32_16x16x32_bf16 a[4:7], a[140:143], v[64:67], a[4:7]\n\t"
    "s_waitcnt lgkmcnt(3)\n\t"
    "v_exp_f32 v34, v34\n\t"
    "v_mfma_f32_16x16x32_bf16 a[8:11], a[144:147], v[64:67], a[8:11]\n\t"
    "s_waitcnt lgkmcnt(2)\n\t"
    "v_exp_f32 v35, v35\n\t"
    "v_mfma_f32_16x16x32_bf16 a[12:15], a[148:151], v[64:67], a[12:15]\n\t"
    "ds_read_b128 a[100:103], v13 offset:32768\n\t"
    "v_cvt_pk_bf16_f32 v33, v34, v35\n\t"
    "v_mfma_f32_16x16x32_bf16 a[72:75], v[216:219], v[64:67], a[72:75]\n\t"
    "v_exp_f32 v36, v36\n\t"
    "v_mfma_f32_16x16x32_bf16 v[176:179], a[120:123], v[72:75], v[176:179]\n\t"
    "v_exp_f32 v37, v37\n\t"
    "v_mfma_f32_16x16x32_bf16 v[180:183], a[124:127], v[72:75], v[180:183]\n\t"
    "ds_read_b128 a[104:107], v10 offset:36864\n\t"
    "v_cvt_pk_bf16_f32 v34, v36, v37\n\t"
    "v_mfma_f32_16x16x32_bf16 v[184:187], a[128:131], v[72:75], v[184:187]\n\t"
    "v_exp_f32 v38, v38\n\t"
    "v_mfma_f32_16x16x32_bf16 v[188:191], a[132:135], v[72:75], v[188:191]\n\t"
    "v_exp_f32 v39, v39\n\t"
    "v_mfma_f32_16x16x32_bf16 a[16:19], a[136:139], v[72:75], a[16:19]\n\t"
    "v_exp_f32 v40, v40\n\t"
    "v_mfma_f32_16x16x32_bf16 a[20:23], a[140:143], v[72:75], a[20:23]\n\t"
    "ds_read_b128 a[108:111], v11 offset:36864\n\t"
    "v_cvt_pk_bf16_f32 v35, v38, v39\n\t"
    "v_mfma_f32_16x16x32_bf16 a[24:27], a[144:147], v[72:75], a[24:27]\n\t"
    "v_exp_f32 v41, v41\n\t"
    "v_mfma_f32_16x16x32_bf16 a[28:31], a[148:151], v[72:75], a[28:31]\n\t"
    "v_exp_f32 v42, v42\n\t"
    "v_mfma_f32_16x16x32_bf16 a[76:79], v[216:219], v[72:75], a[76:79]\n\t"
    "ds_read_b128 a[112:115], v12 offset:36864\n\t"
    "v_cvt_pk_bf16_f32 v40, v40, v41\n\t"
    "v_mfma_f32_16x16x32_bf16 v[192:195], a[120:123], v[80:83], v[192:195]\n\t"
    "v_exp_f32 v43, v43\n\t"
    "v_mfma_f32_16x16x32_bf16 v[196:199], a[124:127], v[80:83], v[196:199]\n\t"
    "v_exp_f32 v44, v44\n\t"
    "v_mfma_f32_16x16x32_bf16 v[200:203], a[128:131], v[80:83], v[200:203]\n\t"
    "buffer_load_dwordx4 v6, s[44:47], s60 offen offset:0 nt lds\n\t"
    "v_mfma_f32_16x16x32_bf16 v[204:207], a[132:135], v[80:83], v[204:207]\n\t"
    "ds_read_b128 a[116:119], v13 offset:36864\n\t"
    "v_cvt_pk_bf16_f32 v41, v42, v43\n\t"
    "v_mfma_f32_16x16x32_bf16 a[32:35], a[136:139], v[80:83], a[32:35]\n\t"
    "v_exp_f32 v45, v45\n\t"
    "v_mfma_f32_16x16x32_bf16 a[36:39], a[140:143], v[80:83], a[36:39]\n\t"
    "v_exp_f32 v46, v46\n\t"
    "v_mfma_f32_16x16x32_bf16 a[40:43], a[144:147], v[80:83], a[40:43]\n\t"
    "v_exp_f32 v47, v47\n\t"
    "v_mfma_f32_16x16x32_bf16 a[44:47], a[148:151], v[80:83], a[44:47]\n\t"
    "v_cvt_pk_bf16_f32 v42, v44, v45\n\t"
    "v_cvt_pk_bf16_f32 v43, v46, v47\n\t"
    "v_mfma_f32_16x16x32_bf16 a[80:83], v[216:219], v[80:83], a[80:83]\n\t"
    "buffer_load_dwordx4 v7, s[44:47], s60 offen offset:1024 nt lds\n\t"
    "v_mfma_f32_16x16x32_bf16 v[208:211], a[120:123], v[88:91], v[208:211]\n\t"
    "v_exp_f32 v48, v48\n\t"
    "v_mfma_f32_16x16x32_bf16 v[212:215], a[124:127], v[88:91], v[212:215]\n\t"
    "v_exp_f32 v49, v49\n\t"
    "v_mfma_f32_16x16x32_bf16 a[48:51], a[128:131], v[88:91], a[48:51]\n\t"
    "v_exp_f32 v50, v50\n\t"
    "v_mfma_f32_16x16x32_bf16 a[52:55], a[132:135], v[88:91], a[52:55]\n\t"
    "v_exp_f32 v51, v51\n\t"
    "v_mfma_f32_16x16x32_bf16 a[56:59], a[136:139], v[88:91], a[56:59]\n\t"
    "v_exp_f32 v52, v52\n\t"
    "v_mfma_f32_16x16x32_bf16 a[60:63], a[140:143], v[88:91], a[60:63]\n\t"
    "buffer_load_dwordx4 v8, s[44:47], s60 offen offset:2048 nt lds\n\t"
    "v_mfma_f32_16x16x32_bf16 a[64:67], a[144:147], v[88:91], a[64:67]\n\t"
    "v_exp_f32 v53, v53\n\t"
    "v_mfma_f32_16x16x32_bf16 a[68:71], a[148:151], v[88:91], a[68:71]\n\t"
    "v_cvt_pk_bf16_f32 v48, v48, v49\n\t"
    "v_cvt_pk_bf16_f32 v49, v50, v51\n\t"
    "v_mfma_f32_16x16x32_bf16 a[84:87], v[216:219], v[88:91], a[84:87]\n\t"
    "s_waitcnt vmcnt(11) lgkmcnt(0)\n\t"
    "s_barrier\n\t"
    "v_mfma_f32_16x16x32_bf16 v[64:67], a[88:91], v[96:99], 0\n\t"
    "ds_read_b64_tr_b16 a[120:121], v14 offset:512\n\t"
    "v_cvt_pk_bf16_f32 v50, v52, v53\n\t"
    "v_mfma_f32_16x16x32_bf16 v[72:75], a[88:91], v[112:115], 0\n\t"
    "v_exp_f32 v54, v54\n\t"
    "v_mfma_f32_16x16x32_bf16 v[80:83], a[88:91], v[128:131], 0\n\t"
    "buffer_load_dwordx4 v9, s[44:47], s60 offen offset:3072 nt lds\n\t"
    "v_mfma_f32_16x16x32_bf16 v[88:91], a[88:91], v[144:147], 0\n\t"
    "s_add_u32 s61, s54, 98304\n\t"
    "v_exp_f32 v55, v55\n\t"
    "v_mfma_f32_16x16x32_bf16 v[64:67], a[92:95], v[100:103], v[64:67]\n\t"
    "s_add_u32 m0, s52, 16384\n\t"
    "ds_read_b64_tr_b16 a[122:123], v14 offset:4608\n\t"
    "ds_read_b64_tr_b16 a[124:125], v15 offset:512\n\t"
    "v_mfma_f32_16x16x32_bf16 v[72:75], a[92:95], v[116:119], v[72:75]\n\t"
    "v_cvt_pk_bf16_f32 v51, v54, v55\n\t"
    "v_mfma_f32_16x16x32_bf16 v[80:83], a[92:95], v[132:135], v[80:83]\n\t"
    "ds_read_b64_tr_b16 a[126:127], v15 offset:4608\n\t"
    "v_mfma_f32_16x16x32_bf16 v[88:91], a[92:95], v[148:151], v[88:91]\n\t"
    "v_mfma_f32_16x16x32_bf16 v[64:67], a[96:99], v[104:107], v[64:67]\n\t"
    "buffer_load_dwordx4 v2, s[40:43], s61 offen offset:0 nt lds\n\t"
    "v_mfma_f32_16x16x32_bf16 v[72:75], a[96:99], v[120:123], v[72:75]\n\t"
    "ds_read_b64_tr_b16 a[128:129], v14 offset:0\n\t"
    "v_mfma_f32_16x16x32_bf16 v[80:83], a[96:99], v[136:139], v[80:83]\n\t"
    "ds_read_b64_tr_b16 a[130:131], v14 offset:4096\n\t"
    "v_mfma_f32_16x16x32_bf16 v[88:91], a[96:99], v[152:155], v[88:91]\n\t"
    "v_mfma_f32_16x16x32_bf16 v[64:67], a[100:103], v[108:111], v[64:67]\n\t"
    "ds_read_b64_tr_b16 a[132:133], v15 offset:0\n\t"
    "v_mfma_f32_16x16x32_bf16 v[72:75], a[100:103], v[124:127], v[72:75]\n\t"
    "v_mfma_f32_16x16x32_bf16 v[80:83], a[100:103], v[140:143], v[80:83]\n\t"
    "buffer_load_dwordx4 v3, s[40:43], s61 offen offset:1024 nt lds\n\t"
    "v_mfma_f32_16x16x32_bf16 v[88:91], a[100:103], v[156:159], v[88:91]\n\t"
    "ds_read_b64_tr_b16 a[134:135], v15 offset:4096\n\t"
    "v_mfma_f32_16x16x32_bf16 v[68:71], a[104:107], v[96:99], 0\n\t"
    "ds_read_b64_tr_b16 a[136:137], v16 offset:1536\n\t"
    "v_mfma_f32_16x16x32_bf16 v[76:79], a[104:107], v[112:115], 0\n\t"
    "v_exp_f32 v64, v64\n\t"
    "v_mfma_f32_16x16x32_bf16 v[84:87], a[104:107], v[128:131], 0\n\t"
    "v_exp_f32 v65, v65\n\t"
    "v_mfma_f32_16x16x32_bf16 v[92:95], a[104:107], v[144:147], 0\n\t"
    "ds_read_b64_tr_b16 a[138:139], v16 offset:5632\n\t"
    "v_cvt_pk_bf16_f32 v64, v64, v65\n\t"
    "v_mfma_f32_16x16x32_bf16 v[68:71], a[108:111], v[100:103], v[68:71]\n\t"
    "buffer_load_dwordx4 v4, s[40:43], s61 offen offset:2048 nt lds\n\t"
    "v_mfma_f32_16x16x32_bf16 v[76:79], a[108:111], v[116:119], v[76:79]\n\t"
    "v_exp_f32 v66, v66\n\t"
    "v_mfma_f32_16x16x32_bf16 v[84:87], a[108:111], v[132:135], v[84:87]\n\t"
    "ds_read_b64_tr_b16 a[140:141], v17 offset:1536\n\t"
    "ds_read_b64_tr_b16 a[142:143], v17 offset:5632\n\t"
    "v_mfma_f32_16x16x32_bf16 v[92:95], a[108:111], v[148:151], v[92:95]\n\t"
    "v_exp_f32 v67, v67\n\t"
    "v_mfma_f32_16x16x32_bf16 v[68:71], a[112:115], v[104:107], v[68:71]\n\t"
    "ds_read_b64_tr_b16 a[144:145], v16 offset:1024\n\t"
    "v_cvt_pk_bf16_f32 v65, v66, v67\n\t"
    "v_mfma_f32_16x16x32_bf16 v[76:79], a[112:115], v[120:123], v[76:79]\n\t"
    "v_exp_f32 v72, v72\n\t"
    "v_mfma_f32_16x16x32_bf16 v[84:87], a[112:115], v[136:139], v[84:87]\n\t"
    "buffer_load_dwordx4 v5, s[40:43], s61 offen offset:3072 nt lds\n\t"
    "v_mfma_f32_16x16x32_bf16 v[92:95], a[112:115], v[152:155], v[92:95]\n\t"
    "s_add_u32 s60, s54, 90112\n\t"
    "v_exp_f32 v73, v73\n\t"
    "v_mfma_f32_16x16x32_bf16 v[68:71], a[116:119], v[108:111], v[68:71]\n\t"
    "s_add_u32 m0, s53, 24576\n\t"
    "ds_read_b64_tr_b16 a[146:147], v16 offset:5120\n\t"
    "ds_read_b64_tr_b16 a[148:149], v17 offset:1024\n\t"
    "v_mfma_f32_16x16x32_bf16 v[76:79], a[116:119], v[124:127], v[76:79]\n\t"
    "ds_read_b128 a[88:91], v10 offset:0\n\t"
    "v_cvt_pk_bf16_f32 v72, v72, v73\n\t"
    "v_mfma_f32_16x16x32_bf16 v[84:87], a[116:119], v[140:143], v[84:87]\n\t"
    "ds_read_b64_tr_b16 a[150:151], v17 offset:5120\n\t"
    "ds_read_b128 a[92:95], v11 offset:0\n\t"
    "v_mfma_f32_16x16x32_bf16 v[92:95], a[116:119], v[156:159], v[92:95]\n\t"
    "s_waitcnt lgkmcnt(6)\n\t"
    "v_exp_f32 v74, v74\n\t"
    "v_mfma_f32_16x16x32_bf16 v[160:163], a[120:123], v[32:35], v[160:163]\n\t"
    "v_exp_f32 v75, v75\n\t"
    "v_mfma_f32_16x16x32_bf16 v[164:167], a[124:127], v[32:35], v[164:167]\n\t"
    "ds_read_b128 a[96:99], v12 offset:0\n\t"
    "v_cvt_pk_bf16_f32 v73, v74, v75\n\t"
    "v_mfma_f32_16x16x32_bf16 v[168:171], a[128:131], v[32:35], v[168:171]\n\t"
    "v_exp_f32 v68, v68\n\t"
    "v_mfma_f32_16x16x32_bf16 v[172:175], a[132:135], v[32:35], v[172:175]\n\t"
    "v_exp_f32 v69, v69\n\t"
    "v_mfma_f32_16x16x32_bf16 a[0:3], a[136:139], v[32:35], a[0:3]\n\t"
    "ds_read_b128 a[100:103], v13 offset:0\n\t"
    "v_cvt_pk_bf16_f32 v66, v68, v69\n\t"
    "v_mfma_f32_16x16x32_bf16 a[4:7], a[140:143], v[32:35], a[4:7]\n\t"
    "s_waitcnt lgkmcnt(4)\n\t"
    "v_exp_f32 v70, v70\n\t"
    "v_mfma_f32_16x16x32_bf16 a[8:11], a[144:147], v[32:35], a[8:11]\n\t"
    "s_waitcnt lgkmcnt(2)\n\t"
    "v_exp_f32 v71, v71\n\t"
    "v_mfma_f32_16x16x32_bf16 a[12:15], a[148:151], v[32:35], a[12:15]\n\t"
    "ds_read_b128 a[104:107], v10 offset:4096\n\t"
    "v_cvt_pk_bf16_f32 v67, v70, v71\n\t"
    "v_mfma_f32_16x16x32_bf16 a[72:75], v[216:219], v[32:35], a[72:75]\n\t"
    "v_exp_f32 v76, v76\n\t"
    "v_mfma_f32_16x16x32_bf16 v[176:179], a[120:123], v[40:43], v[176:179]\n\t"
    "v_exp_f32 v77, v77\n\t"
    "v_mfma_f32_16x16x32_bf16 v[180:183], a[124:127], v[40:43], v[180:183]\n\t"
    "ds_read_b128 a[108:111], v11 offset:4096\n\t"
    "v_cvt_pk_bf16_f32 v74, v76, v77\n\t"
    "v_mfma_f32_16x16x32_bf16 v[184:187], a[128:131], v[40:43], v[184:187]\n\t"
    "buffer_load_dwordx4 v6, s[44:47], s60 offen offset:0 nt lds\n\t"
    "v_mfma_f32_16x16x32_bf16 v[188:191], a[132:135], v[40:43], v[188:191]\n\t"
    "v_exp_f32 v78, v78\n\t"
    "v_mfma_f32_16x16x32_bf16 a[16:19], a[136:139], v[40:43], a[16:19]\n\t"
    "ds_read_b128 a[112:115], v12 offset:4096\n\t"
    "ds_read_b128 a[116:119], v13 offset:4096\n\t"
    "v_mfma_f32_16x16x32_bf16 a[20:23], a[140:143], v[40:43], a[20:23]\n\t"
    "v_exp_f32 v79, v79\n\t"
    "v_mfma_f32_16x16x32_bf16 a[24:27], a[144:147], v[40:43], a[24:27]\n\t"
    "v_exp_f32 v80, v80\n\t"
    "v_mfma_f32_16x16x32_bf16 a[28:31], a[148:151], v[40:43], a[28:31]\n\t"
    "v_exp_f32 v81, v81\n\t"
    "v_mfma_f32_16x16x32_bf16 a[76:79], v[216:219], v[40:43], a[76:79]\n\t"
    "buffer_load_dwordx4 v7, s[44:47], s60 offen offset:1024 nt lds\n\t"
    "v_mfma_f32_16x16x32_bf16 v[192:195], a[120:123], v[48:51], v[192:195]\n\t"
    "v_cvt_pk_bf16_f32 v75, v78, v79\n\t"
    "v_cvt_pk_bf16_f32 v80, v80, v81\n\t"
    "v_mfma_f32_16x16x32_bf16 v[196:199], a[124:127], v[48:51], v[196:199]\n\t"
    "v_exp_f32 v82, v82\n\t"
    "v_mfma_f32_16x16x32_bf16 v[200:203], a[128:131], v[48:51], v[200:203]\n\t"
    "v_exp_f32 v83, v83\n\t"
    "v_mfma_f32_16x16x32_bf16 v[204:207], a[132:135], v[48:51], v[204:207]\n\t"
    "v_exp_f32 v84, v84\n\t"
    "v_mfma_f32_16x16x32_bf16 a[32:35], a[136:139], v[48:51], a[32:35]\n\t"
    "v_exp_f32 v85, v85\n\t"
    "v_mfma_f32_16x16x32_bf16 a[36:39], a[140:143], v[48:51], a[36:39]\n\t"
    "buffer_load_dwordx4 v8, s[44:47], s60 offen offset:2048 nt lds\n\t"
    "v_mfma_f32_16x16x32_bf16 a[40:43], a[144:147], v[48:51], a[40:43]\n\t"
    "v_cvt_pk_bf16_f32 v81, v82, v83\n\t"
    "v_cvt_pk_bf16_f32 v82, v84, v85\n\t"
    "v_mfma_f32_16x16x32_bf16 a[44:47], a[148:151], v[48:51], a[44:47]\n\t"
    "v_exp_f32 v86, v86\n\t"
    "v_mfma_f32_16x16x32_bf16 a[80:83], v[216:219], v[48:51], a[80:83]\n\t"
    "s_waitcnt vmcnt(11) lgkmcnt(0)\n\t"
    "s_barrier\n\t"
    "v_mfma_f32_16x16x32_bf16 v[32:35], a[88:91], v[96:99], 0\n\t"
    "v_exp_f32 v87, v87\n\t"
    "v_mfma_f32_16x16x32_bf16 v[40:43], a[88:91], v[112:115], 0\n\t"
    "ds_read_b64_tr_b16 a[120:121], v14 offset:8704\n\t"
    "ds_read_b64_tr_b16 a[122:123], v14 offset:12800\n\t"
    "v_mfma_f32_16x16x32_bf16 v[48:51], a[88:91], v[128:131], 0\n\t"
    "buffer_load_dwordx4 v9, s[44:47], s60 offen offset:3072 nt lds\n\t"
    "v_mfma_f32_16x16x32_bf16 v[32:35], a[92:95], v[100:103], v[32:35]\n\t"
    "s_add_u32 s61, s54, 106496\n\t"
    "ds_read_b64_tr_b16 a[124:125], v15 offset:8704\n\t"
    "v_cvt_pk_bf16_f32 v83, v86, v87\n\t"
    "v_mfma_f32_16x16x32_bf16 v[40:43], a[92:95], v[116:119], v[40:43]\n\t"
    "s_add_u32 m0, s52, 24576\n\t"
    "v_exp_f32 v88, v88\n\t"
    "v_mfma_f32_16x16x32_bf16 v[48:51], a[92:95], v[132:135], v[48:51]\n\t"
    "v_exp_f32 v89, v89\n\t"
    "v_mfma_f32_16x16x32_bf16 v[32:35], a[96:99], v[104:107], v[32:35]\n\t"
    "ds_read_b64_tr_b16 a[126:127], v15 offset:12800\n\t"
    "ds_read_b64_tr_b16 a[128:129], v14 offset:8192\n\t"
    "v_mfma_f32_16x16x32_bf16 v[40:43], a[96:99], v[120:123], v[40:43]\n\t"
    "ds_read_b64_tr_b16 a[130:131], v14 offset:12288\n\t"
    "v_cvt_pk_bf16_f32 v88, v88, v89\n\t"
    "v_mfma_f32_16x16x32_bf16 v[48:51], a[96:99], v[136:139], v[48:51]\n\t"
    "buffer_load_dwordx4 v2, s[40:43], s61 offen offset:0 nt lds\n\t"
    "v_mfma_f32_16x16x32_bf16 v[32:35], a[100:103], v[108:111], v[32:35]\n\t"
    "v_exp_f32 v90, v90\n\t"
    "v_mfma_f32_16x16x32_bf16 v[40:43], a[100:103], v[124:127], v[40:43]\n\t"
    "ds_read_b64_tr_b16 a[132:133], v15 offset:8192\n\t"
    "ds_read_b64_tr_b16 a[134:135], v15 offset:12288\n\t"
    "v_mfma_f32_16x16x32_bf16 v[48:51], a[100:103], v[140:143], v[48:51]\n\t"
    "v_exp_f32 v91, v91\n\t"
    "v_mfma_f32_16x16x32_bf16 v[36:39], a[104:107], v[96:99], 0\n\t"
    "ds_read_b64_tr_b16 a[136:137], v16 offset:9728\n\t"
    "v_cvt_pk_bf16_f32 v89, v90, v91\n\t"
    "v_mfma_f32_16x16x32_bf16 v[44:47], a[104:107], v[112:115], 0\n\t"
    "v_exp_f32 v92, v92\n\t"
    "v_mfma_f32_16x16x32_bf16 v[52:55], a[104:107], v[128:131], 0\n\t"
    "buffer_load_dwordx4 v3, s[40:43], s61 offen offset:1024 nt lds\n\t"
    "v_mfma_f32_16x16x32_bf16 v[36:39], a[108:111], v[100:103], v[36:39]\n\t"
    "ds_read_b64_tr_b16 a[138:139], v16 offset:13824\n\t"
    "ds_read_b64_tr_b16 a[140:141], v17 offset:9728\n\t"
    "v_mfma_f32_16x16x32_bf16 v[44:47], a[108:111], v[116:119], v[44:47]\n\t"
    "v_exp_f32 v93, v93\n\t"
    "v_mfma_f32_16x16x32_bf16 v[52:55], a[108:111], v[132:135], v[52:55]\n\t"
    "ds_read_b64_tr_b16 a[142:143], v17 offset:13824\n\t"
    "v_cvt_pk_bf16_f32 v90, v92, v93\n\t"
    "v_mfma_f32_16x16x32_bf16 v[36:39], a[112:115], v[104:107], v[36:39]\n\t"
    "v_exp_f32 v94, v94\n\t"
    "v_mfma_f32_16x16x32_bf16 v[44:47], a[112:115], v[120:123], v[44:47]\n\t"
    "ds_read_b64_tr_b16 a[144:145], v16 offset:9216\n\t"
    "ds_read_b64_tr_b16 a[146:147], v16 offset:13312\n\t"
    "v_mfma_f32_16x16x32_bf16 v[52:55], a[112:115], v[136:139], v[52:55]\n\t"
    "buffer_load_dwordx4 v4, s[40:43], s61 offen offset:2048 nt lds\n\t"
    "v_mfma_f32_16x16x32_bf16 v[36:39], a[116:119], v[108:111], v[36:39]\n\t"
    "ds_read_b64_tr_b16 a[148:149], v17 offset:9216\n\t"
    "ds_read_b128 a[88:91], v10 offset:8192\n\t"
    "v_mfma_f32_16x16x32_bf16 v[44:47], a[116:119], v[124:127], v[44:47]\n\t"
    "ds_read_b64_tr_b16 a[150:151], v17 offset:13312\n\t"
    "v_mfma_f32_16x16x32_bf16 v[52:55], a[116:119], v[140:143], v[52:55]\n\t"
    "s_waitcnt lgkmcnt(6)\n\t"
    "v_exp_f32 v95, v95\n\t"
    "v_mfma_f32_16x16x32_bf16 v[160:163], a[120:123], v[64:67], v[160:163]\n\t"
    "ds_read_b128 a[92:95], v11 offset:8192\n\t"
    "v_cvt_pk_bf16_f32 v91, v94, v95\n\t"
    "v_mfma_f32_16x16x32_bf16 v[164:167], a[124:127], v[64:67], v[164:167]\n\t"
    "v_exp_f32 v32, v32\n\t"
    "v_mfma_f32_16x16x32_bf16 v[168:171], a[128:131], v[64:67], v[168:171]\n\t"
    "s_waitcnt lgkmcnt(6)\n\t"
    "buffer_load_dwordx4 v5, s[40:43], s61 offen offset:3072 nt lds\n\t"
    "v_mfma_f32_16x16x32_bf16 v[172:175], a[132:135], v[64:67], v[172:175]\n\t"
    "s_add_u32 s60, s54, 98304\n\t"
    "v_exp_f32 v33, v33\n\t"
    "v_mfma_f32_16x16x32_bf16 a[0:3], a[136:139], v[64:67], a[0:3]\n\t"
    "s_add_u32 m0, s53, 32768\n\t"
    "ds_read_b128 a[96:99], v12 offset:8192\n\t"
    "v_cvt_pk_bf16_f32 v32, v32, v33\n\t"
    "v_mfma_f32_16x16x32_bf16 a[4:7], a[140:143], v[64:67], a[4:7]\n\t"
    "s_waitcnt lgkmcnt(3)\n\t"
    "v_exp_f32 v34, v34\n\t"
    "v_mfma_f32_16x16x32_bf16 a[8:11], a[144:147], v[64:67], a[8:11]\n\t"
    "s_waitcnt lgkmcnt(2)\n\t"
    "v_exp_f32 v35, v35\n\t"
    "v_mfma_f32_16x16x32_bf16 a[12:15], a[148:151], v[64:67], a[12:15]\n\t"
    "ds_read_b128 a[100:103], v13 offset:8192\n\t"
    "v_cvt_pk_bf16_f32 v33, v34, v35\n\t"
    "v_mfma_f32_16x16x32_bf16 a[72:75], v[216:219], v[64:67], a[72:75]\n\t"
    "v_exp_f32 v36, v36\n\t"
    "v_mfma_f32_16x16x32_bf16 v[176:179], a[120:123], v[72:75], v[176:179]\n\t"
    "v_exp_f32 v37, v37\n\t"
    "v_mfma_f32_16x16x32_bf16 v[180:183], a[124:127], v[72:75], v[180:183]\n\t"
    "ds_read_b128 a[104:107], v10 offset:12288\n\t"
    "v_cvt_pk_bf16_f32 v34, v36, v37\n\t"
    "v_mfma_f32_16x16x32_bf16 v[184:187], a[128:131], v[72:75], v[184:187]\n\t"
    "v_exp_f32 v38, v38\n\t"
    "v_mfma_f32_16x16x32_bf16 v[188:191], a[132:135], v[72:75], v[188:191]\n\t"
    "v_exp_f32 v39, v39\n\t"
    "v_mfma_f32_16x16x32_bf16 a[16:19], a[136:139], v[72:75], a[16:19]\n\t"
    "v_exp_f32 v40, v40\n\t"
    "v_mfma_f32_16x16x32_bf16 a[20:23], a[140:143], v[72:75], a[20:23]\n\t"
    "ds_read_b128 a[108:111], v11 offset:12288\n\t"
    "v_cvt_pk_bf16_f32 v35, v38, v39\n\t"
    "v_mfma_f32_16x16x32_bf16 a[24:27], a[144:147], v[72:75], a[24:27]\n\t"
    "v_exp_f32 v41, v41\n\t"
    "v_mfma_f32_16x16x32_bf16 a[28:31], a[148:151], v[72:75], a[28:31]\n\t"
    "v_exp_f32 v42, v42\n\t"
    "v_mfma_f32_16x16x32_bf16 a[76:79], v[216:219], v[72:75], a[76:79]\n\t"
    "ds_read_b128 a[112:115], v12 offset:12288\n\t"
    "v_cvt_pk_bf16_f32 v40, v40, v41\n\t"
    "v_mfma_f32_16x16x32_bf16 v[192:195], a[120:123], v[80:83], v[192:195]\n\t"
    "v_exp_f32 v43, v43\n\t"
    "v_mfma_f32_16x16x32_bf16 v[196:199], a[124:127], v[80:83], v[196:199]\n\t"
    "v_exp_f32 v44, v44\n\t"
    "v_mfma_f32_16x16x32_bf16 v[200:203], a[128:131], v[80:83], v[200:203]\n\t"
    "buffer_load_dwordx4 v6, s[44:47], s60 offen offset:0 nt lds\n\t"
    "v_mfma_f32_16x16x32_bf16 v[204:207], a[132:135], v[80:83], v[204:207]\n\t"
    "ds_read_b128 a[116:119], v13 offset:12288\n\t"
    "v_cvt_pk_bf16_f32 v41, v42, v43\n\t"
    "v_mfma_f32_16x16x32_bf16 a[32:35], a[136:139], v[80:83], a[32:35]\n\t"
    "v_exp_f32 v45, v45\n\t"
    "v_mfma_f32_16x16x32_bf16 a[36:39], a[140:143], v[80:83], a[36:39]\n\t"
    "v_exp_f32 v46, v46\n\t"
    "v_mfma_f32_16x16x32_bf16 a[40:43], a[144:147], v[80:83], a[40:43]\n\t"
    "v_exp_f32 v47, v47\n\t"
    "v_mfma_f32_16x16x32_bf16 a[44:47], a[148:151], v[80:83], a[44:47]\n\t"
    "v_cvt_pk_bf16_f32 v42, v44, v45\n\t"
    "v_cvt_pk_bf16_f32 v43, v46, v47\n\t"
    "v_mfma_f32_16x16x32_bf16 a[80:83], v[216:219], v[80:83], a[80:83]\n\t"
    "buffer_load_dwordx4 v7, s[44:47], s60 offen offset:1024 nt lds\n\t"
    "v_mfma_f32_16x16x32_bf16 v[208:211], a[120:123], v[88:91], v[208:211]\n\t"
    "v_exp_f32 v48, v48\n\t"
    "v_mfma_f32_16x16x32_bf16 v[212:215], a[124:127], v[88:91], v[212:215]\n\t"
    "v_exp_f32 v49, v49\n\t"
    "v_mfma_f32_16x16x32_bf16 a[48:51], a[128:131], v[88:91], a[48:51]\n\t"
    "v_exp_f32 v50, v50\n\t"
    "v_mfma_f32_16x16x32_bf16 a[52:55], a[132:135], v[88:91], a[52:55]\n\t"
    "v_exp_f32 v51, v51\n\t"
    "v_mfma_f32_16x16x32_bf16 a[56:59], a[136:139], v[88:91], a[56:59]\n\t"
    "v_exp_f32 v52, v52\n\t"
    "v_mfma_f32_16x16x32_bf16 a[60:63], a[140:143], v[88:91], a[60:63]\n\t"
    "buffer_load_dwordx4 v8, s[44:47], s60 offen offset:2048 nt lds\n\t"
    "v_mfma_f32_16x16x32_bf16 a[64:67], a[144:147], v[88:91], a[64:67]\n\t"
    "v_exp_f32 v53, v53\n\t"
    "v_mfma_f32_16x16x32_bf16 a[68:71], a[148:151], v[88:91], a[68:71]\n\t"
    "v_cvt_pk_bf16_f32 v48, v48, v49\n\t"
    "v_cvt_pk_bf16_f32 v49, v50, v51\n\t"
    "v_mfma_f32_16x16x32_bf16 a[84:87], v[216:219], v[88:91], a[84:87]\n\t"
    "s_waitcnt vmcnt(11) lgkmcnt(0)\n\t"
    "s_barrier\n\t"
    "v_mfma_f32_16x16x32_bf16 v[64:67], a[88:91], v[96:99], 0\n\t"
    "ds_read_b64_tr_b16 a[120:121], v14 offset:16896\n\t"
    "v_cvt_pk_bf16_f32 v50, v52, v53\n\t"
    "v_mfma_f32_16x16x32_bf16 v[72:75], a[88:91], v[112:115], 0\n\t"
    "v_exp_f32 v54, v54\n\t"
    "v_mfma_f32_16x16x32_bf16 v[80:83], a[88:91], v[128:131], 0\n\t"
    "buffer_load_dwordx4 v9, s[44:47], s60 offen offset:3072 nt lds\n\t"
    "v_mfma_f32_16x16x32_bf16 v[88:91], a[88:91], v[144:147], 0\n\t"
    "s_add_u32 s61, s54, 114688\n\t"
    "v_exp_f32 v55, v55\n\t"
    "v_mfma_f32_16x16x32_bf16 v[64:67], a[92:95], v[100:103], v[64:67]\n\t"
    "s_add_u32 m0, s52, 32768\n\t"
    "ds_read_b64_tr_b16 a[122:123], v14 offset:20992\n\t"
    "ds_read_b64_tr_b16 a[124:125], v15 offset:16896\n\t"
    "v_mfma_f32_16x16x32_bf16 v[72:75], a[92:95], v[116:119], v[72:75]\n\t"
    "v_cvt_pk_bf16_f32 v51, v54, v55\n\t"
    "v_mfma_f32_16x16x32_bf16 v[80:83], a[92:95], v[132:135], v[80:83]\n\t"
    "ds_read_b64_tr_b16 a[126:127], v15 offset:20992\n\t"
    "v_mfma_f32_16x16x32_bf16 v[88:91], a[92:95], v[148:151], v[88:91]\n\t"
    "v_mfma_f32_16x16x32_bf16 v[64:67], a[96:99], v[104:107], v[64:67]\n\t"
    "buffer_load_dwordx4 v2, s[40:43], s61 offen offset:0 nt lds\n\t"
    "v_mfma_f32_16x16x32_bf16 v[72:75], a[96:99], v[120:123], v[72:75]\n\t"
    "ds_read_b64_tr_b16 a[128:129], v14 offset:16384\n\t"
    "v_mfma_f32_16x16x32_bf16 v[80:83], a[96:99], v[136:139], v[80:83]\n\t"
    "ds_read_b64_tr_b16 a[130:131], v14 offset:20480\n\t"
    "v_mfma_f32_16x16x32_bf16 v[88:91], a[96:99], v[152:155], v[88:91]\n\t"
    "v_mfma_f32_16x16x32_bf16 v[64:67], a[100:103], v[108:111], v[64:67]\n\t"
    "ds_read_b64_tr_b16 a[132:133], v15 offset:16384\n\t"
    "v_mfma_f32_16x16x32_bf16 v[72:75], a[100:103], v[124:127], v[72:75]\n\t"
    "v_mfma_f32_16x16x32_bf16 v[80:83], a[100:103], v[140:143], v[80:83]\n\t"
    "buffer_load_dwordx4 v3, s[40:43], s61 offen offset:1024 nt lds\n\t"
    "v_mfma_f32_16x16x32_bf16 v[88:91], a[100:103], v[156:159], v[88:91]\n\t"
    "ds_read_b64_tr_b16 a[134:135], v15 offset:20480\n\t"
    "v_mfma_f32_16x16x32_bf16 v[68:71], a[104:107], v[96:99], 0\n\t"
    "ds_read_b64_tr_b16 a[136:137], v16 offset:17920\n\t"
    "v_mfma_f32_16x16x32_bf16 v[76:79], a[104:107], v[112:115], 0\n\t"
    "v_exp_f32 v64, v64\n\t"
    "v_mfma_f32_16x16x32_bf16 v[84:87], a[104:107], v[128:131], 0\n\t"
    "v_exp_f32 v65, v65\n\t"
    "v_mfma_f32_16x16x32_bf16 v[92:95], a[104:107], v[144:147], 0\n\t"
    "ds_read_b64_tr_b16 a[138:139], v16 offset:22016\n\t"
    "v_cvt_pk_bf16_f32 v64, v64, v65\n\t"
    "v_mfma_f32_16x16x32_bf16 v[68:71], a[108:111], v[100:103], v[68:71]\n\t"
    "buffer_load_dwordx4 v4, s[40:43], s61 offen offset:2048 nt lds\n\t"
    "v_mfma_f32_16x16x32_bf16 v[76:79], a[108:111], v[116:119], v[76:79]\n\t"
    "v_exp_f32 v66, v66\n\t"
    "v_mfma_f32_16x16x32_bf16 v[84:87], a[108:111], v[132:135], v[84:87]\n\t"
    "ds_read_b64_tr_b16 a[140:141], v17 offset:17920\n\t"
    "ds_read_b64_tr_b16 a[142:143], v17 offset:22016\n\t"
    "v_mfma_f32_16x16x32_bf16 v[92:95], a[108:111], v[148:151], v[92:95]\n\t"
    "v_exp_f32 v67, v67\n\t"
    "v_mfma_f32_16x16x32_bf16 v[68:71], a[112:115], v[104:107], v[68:71]\n\t"
    "ds_read_b64_tr_b16 a[144:145], v16 offset:17408\n\t"
    "v_cvt_pk_bf16_f32 v65, v66, v67\n\t"
    "v_mfma_f32_16x16x32_bf16 v[76:79], a[112:115], v[120:123], v[76:79]\n\t"
    "v_exp_f32 v72, v72\n\t"
    "v_mfma_f32_16x16x32_bf16 v[84:87], a[112:115], v[136:139], v[84:87]\n\t"
    "buffer_load_dwordx4 v5, s[40:43], s61 offen offset:3072 nt lds\n\t"
    "v_mfma_f32_16x16x32_bf16 v[92:95], a[112:115], v[152:155], v[92:95]\n\t"
    "s_add_u32 s60, s54, 106496\n\t"
    "v_exp_f32 v73, v73\n\t"
    "v_mfma_f32_16x16x32_bf16 v[68:71], a[116:119], v[108:111], v[68:71]\n\t"
    "s_add_u32 m0, s53, 0\n\t"
    "ds_read_b64_tr_b16 a[146:147], v16 offset:21504\n\t"
    "ds_read_b64_tr_b16 a[148:149], v17 offset:17408\n\t"
    "v_mfma_f32_16x16x32_bf16 v[76:79], a[116:119], v[124:127], v[76:79]\n\t"
    "ds_read_b128 a[88:91], v10 offset:16384\n\t"
    "v_cvt_pk_bf16_f32 v72, v72, v73\n\t"
    "v_mfma_f32_16x16x32_bf16 v[84:87], a[116:119], v[140:143], v[84:87]\n\t"
    "ds_read_b64_tr_b16 a[150:151], v17 offset:21504\n\t"
    "ds_read_b128 a[92:95], v11 offset:16384\n\t"
    "v_mfma_f32_16x16x32_bf16 v[92:95], a[116:119], v[156:159], v[92:95]\n\t"
    "s_waitcnt lgkmcnt(6)\n\t"
    "v_exp_f32 v74, v74\n\t"
    "v_mfma_f32_16x16x32_bf16 v[160:163], a[120:123], v[32:35], v[160:163]\n\t"
    "v_exp_f32 v75, v75\n\t"
    "v_mfma_f32_16x16x32_bf16 v[164:167], a[124:127], v[32:35], v[164:167]\n\t"
    "ds_read_b128 a[96:99], v12 offset:16384\n\t"
    "v_cvt_pk_bf16_f32 v73, v74, v75\n\t"
    "v_mfma_f32_16x16x32_bf16 v[168:171], a[128:131], v[32:35], v[168:171]\n\t"
    "v_exp_f32 v68, v68\n\t"
    "v_mfma_f32_16x16x32_bf16 v[172:175], a[132:135], v[32:35], v[172:175]\n\t"
    "v_exp_f32 v69, v69\n\t"
    "v_mfma_f32_16x16x32_bf16 a[0:3], a[136:139], v[32:35], a[0:3]\n\t"
    "ds_read_b128 a[100:103], v13 offset:16384\n\t"
    "v_cvt_pk_bf16_f32 v66, v68, v69\n\t"
    "v_mfma_f32_16x16x32_bf16 a[4:7], a[140:143], v[32:35], a[4:7]\n\t"
    "s_waitcnt lgkmcnt(4)\n\t"
    "v_exp_f32 v70, v70\n\t"
    "v_mfma_f32_16x16x32_bf16 a[8:11], a[144:147], v[32:35], a[8:11]\n\t"
    "s_waitcnt lgkmcnt(2)\n\t"
    "v_exp_f32 v71, v71\n\t"
    "v_mfma_f32_16x16x32_bf16 a[12:15], a[148:151], v[32:35], a[12:15]\n\t"
    "ds_read_b128 a[104:107], v10 offset:20480\n\t"
    "v_cvt_pk_bf16_f32 v67, v70, v71\n\t"
    "v_mfma_f32_16x16x32_bf16 a[72:75], v[216:219], v[32:35], a[72:75]\n\t"
    "v_exp_f32 v76, v76\n\t"
    "v_mfma_f32_16x16x32_bf16 v[176:179], a[120:123], v[40:43], v[176:179]\n\t"
    "v_exp_f32 v77, v77\n\t"
    "v_mfma_f32_16x16x32_bf16 v[180:183], a[124:127], v[40:43], v[180:183]\n\t"
    "ds_read_b128 a[108:111], v11 offset:20480\n\t"
    "v_cvt_pk_bf16_f32 v74, v76, v77\n\t"
    "v_mfma_f32_16x16x32_bf16 v[184:187], a[128:131], v[40:43], v[184:187]\n\t"
    "buffer_load_dwordx4 v6, s[44:47], s60 offen offset:0 nt lds\n\t"
    "v_mfma_f32_16x16x32_bf16 v[188:191], a[132:135], v[40:43], v[188:191]\n\t"
    "v_exp_f32 v78, v78\n\t"
    "v_mfma_f32_16x16x32_bf16 a[16:19], a[136:139], v[40:43], a[16:19]\n\t"
    "ds_read_b128 a[112:115], v12 offset:20480\n\t"
    "ds_read_b128 a[116:119], v13 offset:20480\n\t"
    "v_mfma_f32_16x16x32_bf16 a[20:23], a[140:143], v[40:43], a[20:23]\n\t"
    "v_exp_f32 v79, v79\n\t"
    "v_mfma_f32_16x16x32_bf16 a[24:27], a[144:147], v[40:43], a[24:27]\n\t"
    "v_exp_f32 v80, v80\n\t"
    "v_mfma_f32_16x16x32_bf16 a[28:31], a[148:151], v[40:43], a[28:31]\n\t"
    "v_exp_f32 v81, v81\n\t"
    "v_mfma_f32_16x16x32_bf16 a[76:79], v[216:219], v[40:43], a[76:79]\n\t"
    "buffer_load_dwordx4 v7, s[44:47], s60 offen offset:1024 nt lds\n\t"
    "v_mfma_f32_16x16x32_bf16 v[192:195], a[120:123], v[48:51], v[192:195]\n\t"
    "v_cvt_pk_bf16_f32 v75, v78, v79\n\t"
    "v_cvt_pk_bf16_f32 v80, v80, v81\n\t"
    "v_mfma_f32_16x16x32_bf16 v[196:199], a[124:127], v[48:51], v[196:199]\n\t"
    "v_exp_f32 v82, v82\n\t"
    "v_mfma_f32_16x16x32_bf16 v[200:203], a[128:131], v[48:51], v[200:203]\n\t"
    "v_exp_f32 v83, v83\n\t"
    "v_mfma_f32_16x16x32_bf16 v[204:207], a[132:135], v[48:51], v[204:207]\n\t"
    "v_exp_f32 v84, v84\n\t"
    "v_mfma_f32_16x16x32_bf16 a[32:35], a[136:139], v[48:51], a[32:35]\n\t"
    "v_exp_f32 v85, v85\n\t"
    "v_mfma_f32_16x16x32_bf16 a[36:39], a[140:143], v[48:51], a[36:39]\n\t"
    "buffer_load_dwordx4 v8, s[44:47], s60 offen offset:2048 nt lds\n\t"
    "v_mfma_f32_16x16x32_bf16 a[40:43], a[144:147], v[48:51], a[40:43]\n\t"
    "v_cvt_pk_bf16_f32 v81, v82, v83\n\t"
    "v_cvt_pk_bf16_f32 v82, v84, v85\n\t"
    "v_mfma_f32_16x16x32_bf16 a[44:47], a[148:151], v[48:51], a[44:47]\n\t"
    "v_exp_f32 v86, v86\n\t"
    "v_mfma_f32_16x16x32_bf16 a[80:83], v[216:219], v[48:51], a[80:83]\n\t"
    "s_waitcnt vmcnt(11) lgkmcnt(0)\n\t"
    "s_barrier\n\t"
    "v_mfma_f32_16x16x32_bf16 v[32:35], a[88:91], v[96:99], 0\n\t"
    "v_exp_f32 v87, v87\n\t"
    "v_mfma_f32_16x16x32_bf16 v[40:43], a[88:91], v[112:115], 0\n\t"
    "ds_read_b64_tr_b16 a[120:121], v14 offset:25088\n\t"
    "ds_read_b64_tr_b16 a[122:123], v14 offset:29184\n\t"
    "v_mfma_f32_16x16x32_bf16 v[48:51], a[88:91], v[128:131], 0\n\t"
    "buffer_load_dwordx4 v9, s[44:47], s60 offen offset:3072 nt lds\n\t"
    "v_mfma_f32_16x16x32_bf16 v[32:35], a[92:95], v[100:103], v[32:35]\n\t"
    "s_add_u32 s61, s54, 122880\n\t"
    "ds_read_b64_tr_b16 a[124:125], v15 offset:25088\n\t"
    "v_cvt_pk_bf16_f32 v83, v86, v87\n\t"
    "v_mfma_f32_16x16x32_bf16 v[40:43], a[92:95], v[116:119], v[40:43]\n\t"
    "s_add_u32 m0, s52, 0\n\t"
    "v_exp_f32 v88, v88\n\t"
    "v_mfma_f32_16x16x32_bf16 v[48:51], a[92:95], v[132:135], v[48:51]\n\t"
    "v_exp_f32 v89, v89\n\t"
    "v_mfma_f32_16x16x32_bf16 v[32:35], a[96:99], v[104:107], v[32:35]\n\t"
    "ds_read_b64_tr_b16 a[126:127], v15 offset:29184\n\t"
    "ds_read_b64_tr_b16 a[128:129], v14 offset:24576\n\t"
    "v_mfma_f32_16x16x32_bf16 v[40:43], a[96:99], v[120:123], v[40:43]\n\t"
    "ds_read_b64_tr_b16 a[130:131], v14 offset:28672\n\t"
    "v_cvt_pk_bf16_f32 v88, v88, v89\n\t"
    "v_mfma_f32_16x16x32_bf16 v[48:51], a[96:99], v[136:139], v[48:51]\n\t"
    "buffer_load_dwordx4 v2, s[40:43], s61 offen offset:0 nt lds\n\t"
    "v_mfma_f32_16x16x32_bf16 v[32:35], a[100:103], v[108:111], v[32:35]\n\t"
    "v_exp_f32 v90, v90\n\t"
    "v_mfma_f32_16x16x32_bf16 v[40:43], a[100:103], v[124:127], v[40:43]\n\t"
    "ds_read_b64_tr_b16 a[132:133], v15 offset:24576\n\t"
    "ds_read_b64_tr_b16 a[134:135], v15 offset:28672\n\t"
    "v_mfma_f32_16x16x32_bf16 v[48:51], a[100:103], v[140:143], v[48:51]\n\t"
    "v_exp_f32 v91, v91\n\t"
    "v_mfma_f32_16x16x32_bf16 v[36:39], a[104:107], v[96:99], 0\n\t"
    "ds_read_b64_tr_b16 a[136:137], v16 offset:26112\n\t"
    "v_cvt_pk_bf16_f32 v89, v90, v91\n\t"
    "v_mfma_f32_16x16x32_bf16 v[44:47], a[104:107], v[112:115], 0\n\t"
    "v_exp_f32 v92, v92\n\t"
    "v_mfma_f32_16x16x32_bf16 v[52:55], a[104:107], v[128:131], 0\n\t"
    "buffer_load_dwordx4 v3, s[40:43], s61 offen offset:1024 nt lds\n\t"
    "v_mfma_f32_16x16x32_bf16 v[36:39], a[108:111], v[100:103], v[36:39]\n\t"
    "ds_read_b64_tr_b16 a[138:139], v16 offset:30208\n\t"
    "ds_read_b64_tr_b16 a[140:141], v17 offset:26112\n\t"
    "v_mfma_f32_16x16x32_bf16 v[44:47], a[108:111], v[116:119], v[44:47]\n\t"
    "v_exp_f32 v93, v93\n\t"
    "v_mfma_f32_16x16x32_bf16 v[52:55], a[108:111], v[132:135], v[52:55]\n\t"
    "ds_read_b64_tr_b16 a[142:143], v17 offset:30208\n\t"
    "v_cvt_pk_bf16_f32 v90, v92, v93\n\t"
    "v_mfma_f32_16x16x32_bf16 v[36:39], a[112:115], v[104:107], v[36:39]\n\t"
    "v_exp_f32 v94, v94\n\t"
    "v_mfma_f32_16x16x32_bf16 v[44:47], a[112:115], v[120:123], v[44:47]\n\t"
    "ds_read_b64_tr_b16 a[144:145], v16 offset:25600\n\t"
    "ds_read_b64_tr_b16 a[146:147], v16 offset:29696\n\t"
    "v_mfma_f32_16x16x32_bf16 v[52:55], a[112:115], v[136:139], v[52:55]\n\t"
    "buffer_load_dwordx4 v4, s[40:43], s61 offen offset:2048 nt lds\n\t"
    "v_mfma_f32_16x16x32_bf16 v[36:39], a[116:119], v[108:111], v[36:39]\n\t"
    "ds_read_b64_tr_b16 a[148:149], v17 offset:25600\n\t"
    "ds_read_b128 a[88:91], v10 offset:24576\n\t"
    "v_mfma_f32_16x16x32_bf16 v[44:47], a[116:119], v[124:127], v[44:47]\n\t"
    "ds_read_b64_tr_b16 a[150:151], v17 offset:29696\n\t"
    "v_mfma_f32_16x16x32_bf16 v[52:55], a[116:119], v[140:143], v[52:55]\n\t"
    "s_waitcnt lgkmcnt(6)\n\t"
    "v_exp_f32 v95, v95\n\t"
    "v_mfma_f32_16x16x32_bf16 v[160:163], a[120:123], v[64:67], v[160:163]\n\t"
    "ds_read_b128 a[92:95], v11 offset:24576\n\t"
    "v_cvt_pk_bf16_f32 v91, v94, v95\n\t"
    "v_mfma_f32_16x16x32_bf16 v[164:167], a[124:127], v[64:67], v[164:167]\n\t"
    "v_exp_f32 v32, v32\n\t"
    "v_mfma_f32_16x16x32_bf16 v[168:171], a[128:131], v[64:67], v[168:171]\n\t"
    "s_waitcnt lgkmcnt(6)\n\t"
    "buffer_load_dwordx4 v5, s[40:43], s61 offen offset:3072 nt lds\n\t"
    "v_mfma_f32_16x16x32_bf16 v[172:175], a[132:135], v[64:67], v[172:175]\n\t"
    "s_add_u32 s60, s54, 114688\n\t"
    "v_exp_f32 v33, v33\n\t"
    "v_mfma_f32_16x16x32_bf16 a[0:3], a[136:139], v[64:67], a[0:3]\n\t"
    "s_add_u32 m0, s53, 8192\n\t"
    "ds_read_b128 a[96:99], v12 offset:24576\n\t"
    "v_cvt_pk_bf16_f32 v32, v32, v33\n\t"
    "v_mfma_f32_16x16x32_bf16 a[4:7], a[140:143], v[64:67], a[4:7]\n\t"
    "s_waitcnt lgkmcnt(3)\n\t"
    "v_exp_f32 v34, v34\n\t"
    "v_mfma_f32_16x16x32_bf16 a[8:11], a[144:147], v[64:67], a[8:11]\n\t"
    "s_waitcnt lgkmcnt(2)\n\t"
    "v_exp_f32 v35, v35\n\t"
    "v_mfma_f32_16x16x32_bf16 a[12:15], a[148:151], v[64:67], a[12:15]\n\t"
    "ds_read_b128 a[100:103], v13 offset:24576\n\t"
    "v_cvt_pk_bf16_f32 v33, v34, v35\n\t"
    "v_mfma_f32_16x16x32_bf16 a[72:75], v[216:219], v[64:67], a[72:75]\n\t"
    "v_exp_f32 v36, v36\n\t"
    "v_mfma_f32_16x16x32_bf16 v[176:179], a[120:123], v[72:75], v[176:179]\n\t"
    "v_exp_f32 v37, v37\n\t"
    "v_mfma_f32_16x16x32_bf16 v[180:183], a[124:127], v[72:75], v[180:183]\n\t"
    "ds_read_b128 a[104:107], v10 offset:28672\n\t"
    "v_cvt_pk_bf16_f32 v34, v36, v37\n\t"
    "v_mfma_f32_16x16x32_bf16 v[184:187], a[128:131], v[72:75], v[184:187]\n\t"
    "v_exp_f32 v38, v38\n\t"
    "v_mfma_f32_16x16x32_bf16 v[188:191], a[132:135], v[72:75], v[188:191]\n\t"
    "v_exp_f32 v39, v39\n\t"
    "v_mfma_f32_16x16x32_bf16 a[16:19], a[136:139], v[72:75], a[16:19]\n\t"
    "v_exp_f32 v40, v40\n\t"
    "v_mfma_f32_16x16x32_bf16 a[20:23], a[140:143], v[72:75], a[20:23]\n\t"
    "ds_read_b128 a[108:111], v11 offset:28672\n\t"
    "v_cvt_pk_bf16_f32 v35, v38, v39\n\t"
    "v_mfma_f32_16x16x32_bf16 a[24:27], a[144:147], v[72:75], a[24:27]\n\t"
    "v_exp_f32 v41, v41\n\t"
    "v_mfma_f32_16x16x32_bf16 a[28:31], a[148:151], v[72:75], a[28:31]\n\t"
    "v_exp_f32 v42, v42\n\t"
    "v_mfma_f32_16x16x32_bf16 a[76:79], v[216:219], v[72:75], a[76:79]\n\t"
    "ds_read_b128 a[112:115], v12 offset:28672\n\t"
    "v_cvt_pk_bf16_f32 v40, v40, v41\n\t"
    "v_mfma_f32_16x16x32_bf16 v[192:195], a[120:123], v[80:83], v[192:195]\n\t"
    "v_exp_f32 v43, v43\n\t"
    "v_mfma_f32_16x16x32_bf16 v[196:199], a[124:127], v[80:83], v[196:199]\n\t"
    "v_exp_f32 v44, v44\n\t"
    "v_mfma_f32_16x16x32_bf16 v[200:203], a[128:131], v[80:83], v[200:203]\n\t"
    "buffer_load_dwordx4 v6, s[44:47], s60 offen offset:0 nt lds\n\t"
    "v_mfma_f32_16x16x32_bf16 v[204:207], a[132:135], v[80:83], v[204:207]\n\t"
    "ds_read_b128 a[116:119], v13 offset:28672\n\t"
    "v_cvt_pk_bf16_f32 v41, v42, v43\n\t"
    "v_mfma_f32_16x16x32_bf16 a[32:35], a[136:139], v[80:83], a[32:35]\n\t"
    "v_exp_f32 v45, v45\n\t"
    "v_mfma_f32_16x16x32_bf16 a[36:39], a[140:143], v[80:83], a[36:39]\n\t"
    "v_exp_f32 v46, v46\n\t"
    "v_mfma_f32_16x16x32_bf16 a[40:43], a[144:147], v[80:83], a[40:43]\n\t"
    "v_exp_f32 v47, v47\n\t"
    "v_mfma_f32_16x16x32_bf16 a[44:47], a[148:151], v[80:83], a[44:47]\n\t"
    "v_cvt_pk_bf16_f32 v42, v44, v45\n\t"
    "v_cvt_pk_bf16_f32 v43, v46, v47\n\t"
    "v_mfma_f32_16x16x32_bf16 a[80:83], v[216:219], v[80:83], a[80:83]\n\t"
    "buffer_load_dwordx4 v7, s[44:47], s60 offen offset:1024 nt lds\n\t"
    "v_mfma_f32_16x16x32_bf16 v[208:211], a[120:123], v[88:91], v[208:211]\n\t"
    "v_exp_f32 v48, v48\n\t"
    "v_mfma_f32_16x16x32_bf16 v[212:215], a[124:127], v[88:91], v[212:215]\n\t"
    "v_exp_f32 v49, v49\n\t"
    "v_mfma_f32_16x16x32_bf16 a[48:51], a[128:131], v[88:91], a[48:51]\n\t"
    "v_exp_f32 v50, v50\n\t"
    "v_mfma_f32_16x16x32_bf16 a[52:55], a[132:135], v[88:91], a[52:55]\n\t"
    "v_exp_f32 v51, v51\n\t"
    "v_mfma_f32_16x16x32_bf16 a[56:59], a[136:139], v[88:91], a[56:59]\n\t"
    "v_exp_f32 v52, v52\n\t"
    "v_mfma_f32_16x16x32_bf16 a[60:63], a[140:143], v[88:91], a[60:63]\n\t"
    "buffer_load_dwordx4 v8, s[44:47], s60 offen offset:2048 nt lds\n\t"
    "v_mfma_f32_16x16x32_bf16 a[64:67], a[144:147], v[88:91], a[64:67]\n\t"
    "v_exp_f32 v53, v53\n\t"
    "v_mfma_f32_16x16x32_bf16 a[68:71], a[148:151], v[88:91], a[68:71]\n\t"
    "v_cvt_pk_bf16_f32 v48, v48, v49\n\t"
    "v_cvt_pk_bf16_f32 v49, v50, v51\n\t"
    "v_mfma_f32_16x16x32_bf16 a[84:87], v[216:219], v[88:91], a[84:87]\n\t"
    "s_waitcnt vmcnt(11) lgkmcnt(0)\n\t"
    "s_barrier\n\t"
    "v_mfma_f32_16x16x32_bf16 v[64:67], a[88:91], v[96:99], 0\n\t"
    "ds_read_b64_tr_b16 a[120:121], v14 offset:33280\n\t"
    "v_cvt_pk_bf16_f32 v50, v52, v53\n\t"
    "v_mfma_f32_16x16x32_bf16 v[72:75], a[88:91], v[112:115], 0\n\t"
    "v_exp_f32 v54, v54\n\t"
    "v_mfma_f32_16x16x32_bf16 v[80:83], a[88:91], v[128:131], 0\n\t"
    "buffer_load_dwordx4 v9, s[44:47], s60 offen offset:3072 nt lds\n\t"
    "v_mfma_f32_16x16x32_bf16 v[88:91], a[88:91], v[144:147], 0\n\t"
    "s_add_u32 s61, s54, 122880\n\t"
    "v_exp_f32 v55, v55\n\t"
    "v_mfma_f32_16x16x32_bf16 v[64:67], a[92:95], v[100:103], v[64:67]\n\t"
    "s_add_u32 m0, s53, 16384\n\t"
    "ds_read_b64_tr_b16 a[122:123], v14 offset:37376\n\t"
    "ds_read_b64_tr_b16 a[124:125], v15 offset:33280\n\t"
    "v_mfma_f32_16x16x32_bf16 v[72:75], a[92:95], v[116:119], v[72:75]\n\t"
    "v_cvt_pk_bf16_f32 v51, v54, v55\n\t"
    "v_mfma_f32_16x16x32_bf16 v[80:83], a[92:95], v[132:135], v[80:83]\n\t"
    "ds_read_b64_tr_b16 a[126:127], v15 offset:37376\n\t"
    "v_mfma_f32_16x16x32_bf16 v[88:91], a[92:95], v[148:151], v[88:91]\n\t"
    "v_mfma_f32_16x16x32_bf16 v[64:67], a[96:99], v[104:107], v[64:67]\n\t"
    "ds_read_b64_tr_b16 a[128:129], v14 offset:32768\n\t"
    "v_mfma_f32_16x16x32_bf16 v[72:75], a[96:99], v[120:123], v[72:75]\n\t"
    "v_mfma_f32_16x16x32_bf16 v[80:83], a[96:99], v[136:139], v[80:83]\n\t"
    "ds_read_b64_tr_b16 a[130:131], v14 offset:36864\n\t"
    "v_mfma_f32_16x16x32_bf16 v[88:91], a[96:99], v[152:155], v[88:91]\n\t"
    "v_mfma_f32_16x16x32_bf16 v[64:67], a[100:103], v[108:111], v[64:67]\n\t"
    "ds_read_b64_tr_b16 a[132:133], v15 offset:32768\n\t"
    "v_mfma_f32_16x16x32_bf16 v[72:75], a[100:103], v[124:127], v[72:75]\n\t"
    "v_mfma_f32_16x16x32_bf16 v[80:83], a[100:103], v[140:143], v[80:83]\n\t"
    "ds_read_b64_tr_b16 a[134:135], v15 offset:36864\n\t"
    "v_mfma_f32_16x16x32_bf16 v[88:91], a[100:103], v[156:159], v[88:91]\n\t"
    "v_mfma_f32_16x16x32_bf16 v[68:71], a[104:107], v[96:99], 0\n\t"
    "ds_read_b64_tr_b16 a[136:137], v16 offset:34304\n\t"
    "v_mfma_f32_16x16x32_bf16 v[76:79], a[104:107], v[112:115], 0\n\t"
    "v_exp_f32 v64, v64\n\t"
    "v_mfma_f32_16x16x32_bf16 v[84:87], a[104:107], v[128:131], 0\n\t"
    "v_exp_f32 v65, v65\n\t"
    "v_mfma_f32_16x16x32_bf16 v[92:95], a[104:107], v[144:147], 0\n\t"
    "ds_read_b64_tr_b16 a[138:139], v16 offset:38400\n\t"
    "v_cvt_pk_bf16_f32 v64, v64, v65\n\t"
    "v_mfma_f32_16x16x32_bf16 v[68:71], a[108:111], v[100:103], v[68:71]\n\t"
    "v_exp_f32 v66, v66\n\t"
    "v_mfma_f32_16x16x32_bf16 v[76:79], a[108:111], v[116:119], v[76:79]\n\t"
    "v_exp_f32 v67, v67\n\t"
    "v_mfma_f32_16x16x32_bf16 v[84:87], a[108:111], v[132:135], v[84:87]\n\t"
    "ds_read_b64_tr_b16 a[140:141], v17 offset:34304\n\t"
    "ds_read_b64_tr_b16 a[142:143], v17 offset:38400\n\t"
    "v_mfma_f32_16x16x32_bf16 v[92:95], a[108:111], v[148:151], v[92:95]\n\t"
    "v_exp_f32 v72, v72\n\t"
    "v_mfma_f32_16x16x32_bf16 v[68:71], a[112:115], v[104:107], v[68:71]\n\t"
    "ds_read_b64_tr_b16 a[144:145], v16 offset:33792\n\t"
    "v_cvt_pk_bf16_f32 v65, v66, v67\n\t"
    "v_mfma_f32_16x16x32_bf16 v[76:79], a[112:115], v[120:123], v[76:79]\n\t"
    "v_exp_f32 v73, v73\n\t"
    "v_mfma_f32_16x16x32_bf16 v[84:87], a[112:115], v[136:139], v[84:87]\n\t"
    "ds_read_b64_tr_b16 a[146:147], v16 offset:37888\n\t"
    "v_cvt_pk_bf16_f32 v72, v72, v73\n\t"
    "v_mfma_f32_16x16x32_bf16 v[92:95], a[112:115], v[152:155], v[92:95]\n\t"
    "v_exp_f32 v74, v74\n\t"
    "v_mfma_f32_16x16x32_bf16 v[68:71], a[116:119], v[108:111], v[68:71]\n\t"
    "ds_read_b64_tr_b16 a[148:149], v17 offset:33792\n\t"
    "ds_read_b128 a[88:91], v10 offset:32768\n\t"
    "v_mfma_f32_16x16x32_bf16 v[76:79], a[116:119], v[124:127], v[76:79]\n\t"
    "v_exp_f32 v75, v75\n\t"
    "v_mfma_f32_16x16x32_bf16 v[84:87], a[116:119], v[140:143], v[84:87]\n\t"
    "ds_read_b64_tr_b16 a[150:151], v17 offset:37888\n\t"
    "ds_read_b128 a[92:95], v11 offset:32768\n\t"
    "v_mfma_f32_16x16x32_bf16 v[92:95], a[116:119], v[156:159], v[92:95]\n\t"
    "s_waitcnt lgkmcnt(6)\n\t"
    "v_exp_f32 v80, v80\n\t"
    "v_mfma_f32_16x16x32_bf16 v[160:163], a[120:123], v[32:35], v[160:163]\n\t"
    "v_exp_f32 v81, v81\n\t"
    "v_mfma_f32_16x16x32_bf16 v[164:167], a[124:127], v[32:35], v[164:167]\n\t"
    "ds_read_b128 a[96:99], v12 offset:32768\n\t"
    "v_cvt_pk_bf16_f32 v73, v74, v75\n\t"
    "v_mfma_f32_16x16x32_bf16 v[168:171], a[128:131], v[32:35], v[168:171]\n\t"
    "v_exp_f32 v68, v68\n\t"
    "v_mfma_f32_16x16x32_bf16 v[172:175], a[132:135], v[32:35], v[172:175]\n\t"
    "ds_read_b128 a[100:103], v13 offset:32768\n\t"
    "v_cvt_pk_bf16_f32 v80, v80, v81\n\t"
    "v_mfma_f32_16x16x32_bf16 a[0:3], a[136:139], v[32:35], a[0:3]\n\t"
    "v_exp_f32 v69, v69\n\t"
    "v_mfma_f32_16x16x32_bf16 a[4:7], a[140:143], v[32:35], a[4:7]\n\t"
    "s_waitcnt lgkmcnt(4)\n\t"
    "v_exp_f32 v70, v70\n\t"
    "v_mfma_f32_16x16x32_bf16 a[8:11], a[144:147], v[32:35], a[8:11]\n\t"
    "s_waitcnt lgkmcnt(2)\n\t"
    "ds_read_b128 a[104:107], v10 offset:36864\n\t"
    "v_cvt_pk_bf16_f32 v66, v68, v69\n\t"
    "v_mfma_f32_16x16x32_bf16 a[12:15], a[148:151], v[32:35], a[12:15]\n\t"
    "v_exp_f32 v71, v71\n\t"
    "v_mfma_f32_16x16x32_bf16 a[72:75], v[216:219], v[32:35], a[72:75]\n\t"
    "ds_read_b128 a[108:111], v11 offset:36864\n\t"
    "v_cvt_pk_bf16_f32 v67, v70, v71\n\t"
    "v_mfma_f32_16x16x32_bf16 v[176:179], a[120:123], v[40:43], v[176:179]\n\t"
    "v_exp_f32 v76, v76\n\t"
    "v_mfma_f32_16x16x32_bf16 v[180:183], a[124:127], v[40:43], v[180:183]\n\t"
    "v_exp_f32 v77, v77\n\t"
    "v_mfma_f32_16x16x32_bf16 v[184:187], a[128:131], v[40:43], v[184:187]\n\t"
    "ds_read_b128 a[112:115], v12 offset:36864\n\t"
    "v_cvt_pk_bf16_f32 v74, v76, v77\n\t"
    "v_mfma_f32_16x16x32_bf16 v[188:191], a[132:135], v[40:43], v[188:191]\n\t"
    "buffer_load_dwordx4 v6, s[44:47], s61 offen offset:0 nt lds\n\t"
    "v_mfma_f32_16x16x32_bf16 a[16:19], a[136:139], v[40:43], a[16:19]\n\t"
    "v_exp_f32 v78, v78\n\t"
    "v_mfma_f32_16x16x32_bf16 a[20:23], a[140:143], v[40:43], a[20:23]\n\t"
    "v_exp_f32 v79, v79\n\t"
    "v_mfma_f32_16x16x32_bf16 a[24:27], a[144:147], v[40:43], a[24:27]\n\t"
    "ds_read_b128 a[116:119], v13 offset:36864\n\t"
    "v_cvt_pk_bf16_f32 v75, v78, v79\n\t"
    "v_mfma_f32_16x16x32_bf16 a[28:31], a[148:151], v[40:43], a[28:31]\n\t"
    "v_exp_f32 v82, v82\n\t"
    "v_mfma_f32_16x16x32_bf16 a[76:79], v[216:219], v[40:43], a[76:79]\n\t"
    "v_exp_f32 v83, v83\n\t"
    "v_mfma_f32_16x16x32_bf16 v[192:195], a[120:123], v[48:51], v[192:195]\n\t"
    "buffer_load_dwordx4 v7, s[44:47], s61 offen offset:1024 nt lds\n\t"
    "v_mfma_f32_16x16x32_bf16 v[196:199], a[124:127], v[48:51], v[196:199]\n\t"
    "v_exp_f32 v84, v84\n\t"
    "v_mfma_f32_16x16x32_bf16 v[200:203], a[128:131], v[48:51], v[200:203]\n\t"
    "v_exp_f32 v85, v85\n\t"
    "v_mfma_f32_16x16x32_bf16 v[204:207], a[132:135], v[48:51], v[204:207]\n\t"
    "v_cvt_pk_bf16_f32 v81, v82, v83\n\t"
    "v_cvt_pk_bf16_f32 v82, v84, v85\n\t"
    "v_mfma_f32_16x16x32_bf16 a[32:35], a[136:139], v[48:51], a[32:35]\n\t"
    "v_exp_f32 v86, v86\n\t"
    "v_mfma_f32_16x16x32_bf16 a[36:39], a[140:143], v[48:51], a[36:39]\n\t"
    "v_exp_f32 v87, v87\n\t"
    "v_mfma_f32_16x16x32_bf16 a[40:43], a[144:147], v[48:51], a[40:43]\n\t"
    "buffer_load_dwordx4 v8, s[44:47], s61 offen offset:2048 nt lds\n\t"
    "v_mfma_f32_16x16x32_bf16 a[44:47], a[148:151], v[48:51], a[44:47]\n\t"
    "v_exp_f32 v88, v88\n\t"
    "v_mfma_f32_16x16x32_bf16 a[80:83], v[216:219], v[48:51], a[80:83]\n\t"
    "s_waitcnt vmcnt(7) lgkmcnt(0)\n\t"
    "s_barrier\n\t"
    "v_mfma_f32_16x16x32_bf16 v[32:35], a[88:91], v[96:99], 0\n\t"
    "ds_read_b64_tr_b16 a[120:121], v14 offset:512\n\t"
    "v_cvt_pk_bf16_f32 v83, v86, v87\n\t"
    "v_mfma_f32_16x16x32_bf16 v[40:43], a[88:91], v[112:115], 0\n\t"
    "v_exp_f32 v89, v89\n\t"
    "v_mfma_f32_16x16x32_bf16 v[48:51], a[88:91], v[128:131], 0\n\t"
    "ds_read_b64_tr_b16 a[122:123], v14 offset:4608\n\t"
    "v_cvt_pk_bf16_f32 v88, v88, v89\n\t"
    "v_mfma_f32_16x16x32_bf16 v[32:35], a[92:95], v[100:103], v[32:35]\n\t"
    "buffer_load_dwordx4 v9, s[44:47], s61 offen offset:3072 nt lds\n\t"
    "v_mfma_f32_16x16x32_bf16 v[40:43], a[92:95], v[116:119], v[40:43]\n\t"
    "ds_read_b64_tr_b16 a[124:125], v15 offset:512\n\t"
    "ds_read_b64_tr_b16 a[126:127], v15 offset:4608\n\t"
    "v_mfma_f32_16x16x32_bf16 v[48:51], a[92:95], v[132:135], v[48:51]\n\t"
    "v_exp_f32 v90, v90\n\t"
    "v_mfma_f32_16x16x32_bf16 v[32:35], a[96:99], v[104:107], v[32:35]\n\t"
    "v_exp_f32 v91, v91\n\t"
    "v_mfma_f32_16x16x32_bf16 v[40:43], a[96:99], v[120:123], v[40:43]\n\t"
    "ds_read_b64_tr_b16 a[128:129], v14 offset:0\n\t"
    "ds_read_b64_tr_b16 a[130:131], v14 offset:4096\n\t"
    "v_mfma_f32_16x16x32_bf16 v[48:51], a[96:99], v[136:139], v[48:51]\n\t"
    "v_exp_f32 v92, v92\n\t"
    "v_mfma_f32_16x16x32_bf16 v[32:35], a[100:103], v[108:111], v[32:35]\n\t"
    "ds_read_b64_tr_b16 a[132:133], v15 offset:0\n\t"
    "v_cvt_pk_bf16_f32 v89, v90, v91\n\t"
    "v_mfma_f32_16x16x32_bf16 v[40:43], a[100:103], v[124:127], v[40:43]\n\t"
    "v_exp_f32 v93, v93\n\t"
    "v_mfma_f32_16x16x32_bf16 v[48:51], a[100:103], v[140:143], v[48:51]\n\t"
    "ds_read_b64_tr_b16 a[134:135], v15 offset:4096\n\t"
    "v_cvt_pk_bf16_f32 v90, v92, v93\n\t"
    "v_mfma_f32_16x16x32_bf16 v[36:39], a[104:107], v[96:99], 0\n\t"
    "v_exp_f32 v94, v94\n\t"
    "v_mfma_f32_16x16x32_bf16 v[44:47], a[104:107], v[112:115], 0\n\t"
    "ds_read_b64_tr_b16 a[136:137], v16 offset:1536\n\t"
    "ds_read_b64_tr_b16 a[138:139], v16 offset:5632\n\t"
    "v_mfma_f32_16x16x32_bf16 v[52:55], a[104:107], v[128:131], 0\n\t"
    "v_exp_f32 v95, v95\n\t"
    "v_mfma_f32_16x16x32_bf16 v[36:39], a[108:111], v[100:103], v[36:39]\n\t"
    "ds_read_b64_tr_b16 a[140:141], v17 offset:1536\n\t"
    "v_cvt_pk_bf16_f32 v91, v94, v95\n\t"
    "v_mfma_f32_16x16x32_bf16 v[44:47], a[108:111], v[116:119], v[44:47]\n\t"
    "v_exp_f32 v32, v32\n\t"
    "v_mfma_f32_16x16x32_bf16 v[52:55], a[108:111], v[132:135], v[52:55]\n\t"
    "v_exp_f32 v33, v33\n\t"
    "v_mfma_f32_16x16x32_bf16 v[36:39], a[112:115], v[104:107], v[36:39]\n\t"
    "ds_read_b64_tr_b16 a[142:143], v17 offset:5632\n\t"
    "ds_read_b64_tr_b16 a[144:145], v16 offset:1024\n\t"
    "v_mfma_f32_16x16x32_bf16 v[44:47], a[112:115], v[120:123], v[44:47]\n\t"
    "ds_read_b64_tr_b16 a[146:147], v16 offset:5120\n\t"
    "v_cvt_pk_bf16_f32 v32, v32, v33\n\t"
    "v_mfma_f32_16x16x32_bf16 v[52:55], a[112:115], v[136:139], v[52:55]\n\t"
    "v_exp_f32 v34, v34\n\t"
    "v_mfma_f32_16x16x32_bf16 v[36:39], a[116:119], v[108:111], v[36:39]\n\t"
    "ds_read_b64_tr_b16 a[148:149], v17 offset:1024\n\t"
    "ds_read_b128 a[88:91], v10 offset:0\n\t"
    "v_mfma_f32_16x16x32_bf16 v[44:47], a[116:119], v[124:127], v[44:47]\n\t"
    "ds_read_b64_tr_b16 a[150:151], v17 offset:5120\n\t"
    "v_mfma_f32_16x16x32_bf16 v[52:55], a[116:119], v[140:143], v[52:55]\n\t"
    "s_waitcnt lgkmcnt(6)\n\t"
    "v_exp_f32 v35, v35\n\t"
    "v_mfma_f32_16x16x32_bf16 v[160:163], a[120:123], v[64:67], v[160:163]\n\t"
    "ds_read_b128 a[92:95], v11 offset:0\n\t"
    "v_cvt_pk_bf16_f32 v33, v34, v35\n\t"
    "v_mfma_f32_16x16x32_bf16 v[164:167], a[124:127], v[64:67], v[164:167]\n\t"
    "v_exp_f32 v40, v40\n\t"
    "v_mfma_f32_16x16x32_bf16 v[168:171], a[128:131], v[64:67], v[168:171]\n\t"
    "v_exp_f32 v36, v36\n\t"
    "v_mfma_f32_16x16x32_bf16 v[172:175], a[132:135], v[64:67], v[172:175]\n\t"
    "v_exp_f32 v37, v37\n\t"
    "v_mfma_f32_16x16x32_bf16 a[0:3], a[136:139], v[64:67], a[0:3]\n\t"
    "s_waitcnt lgkmcnt(4)\n\t"
    "ds_read_b128 a[96:99], v12 offset:0\n\t"
    "v_cvt_pk_bf16_f32 v34, v36, v37\n\t"
    "v_mfma_f32_16x16x32_bf16 a[4:7], a[140:143], v[64:67], a[4:7]\n\t"
    "v_exp_f32 v38, v38\n\t"
    "v_mfma_f32_16x16x32_bf16 a[8:11], a[144:147], v[64:67], a[8:11]\n\t"
    "s_waitcnt lgkmcnt(2)\n\t"
    "v_exp_f32 v39, v39\n\t"
    "v_mfma_f32_16x16x32_bf16 a[12:15], a[148:151], v[64:67], a[12:15]\n\t"
    "ds_read_b128 a[100:103], v13 offset:0\n\t"
    "v_cvt_pk_bf16_f32 v35, v38, v39\n\t"
    "v_mfma_f32_16x16x32_bf16 a[72:75], v[216:219], v[64:67], a[72:75]\n\t"
    "v_exp_f32 v41, v41\n\t"
    "v_mfma_f32_16x16x32_bf16 v[176:179], a[120:123], v[72:75], v[176:179]\n\t"
    "v_exp_f32 v42, v42\n\t"
    "v_mfma_f32_16x16x32_bf16 v[180:183], a[124:127], v[72:75], v[180:183]\n\t"
    "ds_read_b128 a[104:107], v10 offset:4096\n\t"
    "v_cvt_pk_bf16_f32 v40, v40, v41\n\t"
    "v_mfma_f32_16x16x32_bf16 v[184:187], a[128:131], v[72:75], v[184:187]\n\t"
    "v_exp_f32 v43, v43\n\t"
    "v_mfma_f32_16x16x32_bf16 v[188:191], a[132:135], v[72:75], v[188:191]\n\t"
    "v_exp_f32 v44, v44\n\t"
    "v_mfma_f32_16x16x32_bf16 a[16:19], a[136:139], v[72:75], a[16:19]\n\t"
    "v_exp_f32 v45, v45\n\t"
    "v_mfma_f32_16x16x32_bf16 a[20:23], a[140:143], v[72:75], a[20:23]\n\t"
    "ds_read_b128 a[108:111], v11 offset:4096\n\t"
    "v_cvt_pk_bf16_f32 v41, v42, v43\n\t"
    "v_mfma_f32_16x16x32_bf16 a[24:27], a[144:147], v[72:75], a[24:27]\n\t"
    "v_exp_f32 v46, v46\n\t"
    "v_mfma_f32_16x16x32_bf16 a[28:31], a[148:151], v[72:75], a[28:31]\n\t"
    "v_exp_f32 v47, v47\n\t"
    "v_mfma_f32_16x16x32_bf16 a[76:79], v[216:219], v[72:75], a[76:79]\n\t"
    "ds_read_b128 a[112:115], v12 offset:4096\n\t"
    "v_cvt_pk_bf16_f32 v42, v44, v45\n\t"
    "v_mfma_f32_16x16x32_bf16 v[192:195], a[120:123], v[80:83], v[192:195]\n\t"
    "v_exp_f32 v48, v48\n\t"
    "v_mfma_f32_16x16x32_bf16 v[196:199], a[124:127], v[80:83], v[196:199]\n\t"
    "v_exp_f32 v49, v49\n\t"
    "v_mfma_f32_16x16x32_bf16 v[200:203], a[128:131], v[80:83], v[200:203]\n\t"
    "v_cvt_pk_bf16_f32 v43, v46, v47\n\t"
    "v_cvt_pk_bf16_f32 v48, v48, v49\n\t"
    "v_mfma_f32_16x16x32_bf16 v[204:207], a[132:135], v[80:83], v[204:207]\n\t"
    "v_exp_f32 v50, v50\n\t"
    "v_mfma_f32_16x16x32_bf16 a[32:35], a[136:139], v[80:83], a[32:35]\n\t"
    "v_exp_f32 v51, v51\n\t"
    "v_mfma_f32_16x16x32_bf16 a[36:39], a[140:143], v[80:83], a[36:39]\n\t"
    "ds_read_b128 a[116:119], v13 offset:4096\n\t"
    "v_cvt_pk_bf16_f32 v49, v50, v51\n\t"
    "v_mfma_f32_16x16x32_bf16 a[40:43], a[144:147], v[80:83], a[40:43]\n\t"
    "v_exp_f32 v52, v52\n\t"
    "v_mfma_f32_16x16x32_bf16 a[44:47], a[148:151], v[80:83], a[44:47]\n\t"
    "v_exp_f32 v53, v53\n\t"
    "v_mfma_f32_16x16x32_bf16 a[80:83], v[216:219], v[80:83], a[80:83]\n\t"
    "v_exp_f32 v54, v54\n\t"
    "v_mfma_f32_16x16x32_bf16 v[208:211], a[120:123], v[88:91], v[208:211]\n\t"
    "v_exp_f32 v55, v55\n\t"
    "v_mfma_f32_16x16x32_bf16 v[212:215], a[124:127], v[88:91], v[212:215]\n\t"
    "v_cvt_pk_bf16_f32 v50, v52, v53\n\t"
    "v_cvt_pk_bf16_f32 v51, v54, v55\n\t"
    "v_mfma_f32_16x16x32_bf16 a[48:51], a[128:131], v[88:91], a[48:51]\n\t"
    "v_mfma_f32_16x16x32_bf16 a[52:55], a[132:135], v[88:91], a[52:55]\n\t"
    "v_mfma_f32_16x16x32_bf16 a[56:59], a[136:139], v[88:91], a[56:59]\n\t"
    "v_mfma_f32_16x16x32_bf16 a[60:63], a[140:143], v[88:91], a[60:63]\n\t"
    "v_mfma_f32_16x16x32_bf16 a[64:67], a[144:147], v[88:91], a[64:67]\n\t"
    "v_mfma_f32_16x16x32_bf16 a[68:71], a[148:151], v[88:91], a[68:71]\n\t"
    "v_mfma_f32_16x16x32_bf16 a[84:87], v[216:219], v[88:91], a[84:87]\n\t"
    "s_waitcnt vmcnt(0) lgkmcnt(0)\n\t"
    "s_barrier\n\t"
    "v_mfma_f32_16x16x32_bf16 v[64:67], a[88:91], v[96:99], 0\n\t"
    "ds_read_b64_tr_b16 a[120:121], v14 offset:8704\n\t"
    "v_mfma_f32_16x16x32_bf16 v[68:71], a[104:107], v[96:99], 0\n\t"
    "v_mfma_f32_16x16x32_bf16 v[64:67], a[92:95], v[100:103], v[64:67]\n\t"
    "ds_read_b64_tr_b16 a[122:123], v14 offset:12800\n\t"
    "v_mfma_f32_16x16x32_bf16 v[68:71], a[108:111], v[100:103], v[68:71]\n\t"
    "v_mfma_f32_16x16x32_bf16 v[64:67], a[96:99], v[104:107], v[64:67]\n\t"
    "ds_read_b64_tr_b16 a[124:125], v15 offset:8704\n\t"
    "v_mfma_f32_16x16x32_bf16 v[68:71], a[112:115], v[104:107], v[68:71]\n\t"
    "v_mfma_f32_16x16x32_bf16 v[64:67], a[100:103], v[108:111], v[64:67]\n\t"
    "ds_read_b64_tr_b16 a[126:127], v15 offset:12800\n\t"
    "v_mfma_f32_16x16x32_bf16 v[68:71], a[116:119], v[108:111], v[68:71]\n\t"
    "v_mfma_f32_16x16x32_bf16 v[72:75], a[88:91], v[112:115], 0\n\t"
    "ds_read_b64_tr_b16 a[128:129], v14 offset:8192\n\t"
    "v_mfma_f32_16x16x32_bf16 v[76:79], a[104:107], v[112:115], 0\n\t"
    "v_mfma_f32_16x16x32_bf16 v[72:75], a[92:95], v[116:119], v[72:75]\n\t"
    "ds_read_b64_tr_b16 a[130:131], v14 offset:12288\n\t"
    "v_mfma_f32_16x16x32_bf16 v[76:79], a[108:111], v[116:119], v[76:79]\n\t"
    "v_exp_f32 v64, v64\n\t"
    "v_mfma_f32_16x16x32_bf16 v[72:75], a[96:99], v[120:123], v[72:75]\n\t"
    "v_exp_f32 v65, v65\n\t"
    "v_mfma_f32_16x16x32_bf16 v[76:79], a[112:115], v[120:123], v[76:79]\n\t"
    "ds_read_b64_tr_b16 a[132:133], v15 offset:8192\n\t"
    "v_cvt_pk_bf16_f32 v64, v64, v65\n\t"
    "v_mfma_f32_16x16x32_bf16 v[72:75], a[100:103], v[124:127], v[72:75]\n\t"
    "v_exp_f32 v66, v66\n\t"
    "v_mfma_f32_16x16x32_bf16 v[76:79], a[116:119], v[124:127], v[76:79]\n\t"
    "v_exp_f32 v67, v67\n\t"
    "v_mfma_f32_16x16x32_bf16 v[80:83], a[88:91], v[128:131], 0\n\t"
    "ds_read_b64_tr_b16 a[134:135], v15 offset:12288\n\t"
    "ds_read_b64_tr_b16 a[136:137], v16 offset:9728\n\t"
    "v_mfma_f32_16x16x32_bf16 v[84:87], a[104:107], v[128:131], 0\n\t"
    "v_exp_f32 v68, v68\n\t"
    "v_mfma_f32_16x16x32_bf16 v[80:83], a[92:95], v[132:135], v[80:83]\n\t"
    "ds_read_b64_tr_b16 a[138:139], v16 offset:13824\n\t"
    "v_cvt_pk_bf16_f32 v65, v66, v67\n\t"
    "v_mfma_f32_16x16x32_bf16 v[84:87], a[108:111], v[132:135], v[84:87]\n\t"
    "v_exp_f32 v69, v69\n\t"
    "v_mfma_f32_16x16x32_bf16 v[80:83], a[96:99], v[136:139], v[80:83]\n\t"
    "ds_read_b64_tr_b16 a[140:141], v17 offset:9728\n\t"
    "v_cvt_pk_bf16_f32 v66, v68, v69\n\t"
    "v_mfma_f32_16x16x32_bf16 v[84:87], a[112:115], v[136:139], v[84:87]\n\t"
    "v_exp_f32 v70, v70\n\t"
    "v_mfma_f32_16x16x32_bf16 v[80:83], a[100:103], v[140:143], v[80:83]\n\t"
    "v_exp_f32 v71, v71\n\t"
    "v_mfma_f32_16x16x32_bf16 v[84:87], a[116:119], v[140:143], v[84:87]\n\t"
    "ds_read_b64_tr_b16 a[142:143], v17 offset:13824\n\t"
    "v_cvt_pk_bf16_f32 v67, v70, v71\n\t"
    "v_mfma_f32_16x16x32_bf16 v[88:91], a[88:91], v[144:147], 0\n\t"
    "ds_read_b64_tr_b16 a[144:145], v16 offset:9216\n\t"
    "ds_read_b64_tr_b16 a[152:153], v14 offset:16896\n\t"
    "v_mfma_f32_16x16x32_bf16 v[92:95], a[104:107], v[144:147], 0\n\t"
    "v_exp_f32 v72, v72\n\t"
    "v_mfma_f32_16x16x32_bf16 v[88:91], a[92:95], v[148:151], v[88:91]\n\t"
    "ds_read_b64_tr_b16 a[146:147], v16 offset:13312\n\t"
    "ds_read_b64_tr_b16 a[154:155], v14 offset:20992\n\t"
    "v_mfma_f32_16x16x32_bf16 v[92:95], a[108:111], v[148:151], v[92:95]\n\t"
    "v_exp_f32 v73, v73\n\t"
    "v_mfma_f32_16x16x32_bf16 v[88:91], a[96:99], v[152:155], v[88:91]\n\t"
    "ds_read_b64_tr_b16 a[148:149], v17 offset:9216\n\t"
    "v_cvt_pk_bf16_f32 v72, v72, v73\n\t"
    "v_mfma_f32_16x16x32_bf16 v[92:95], a[112:115], v[152:155], v[92:95]\n\t"
    "v_exp_f32 v74, v74\n\t"
    "v_mfma_f32_16x16x32_bf16 v[88:91], a[100:103], v[156:159], v[88:91]\n\t"
    "ds_read_b64_tr_b16 a[150:151], v17 offset:13312\n\t"
    "ds_read_b64_tr_b16 a[156:157], v15 offset:16896\n\t"
    "v_mfma_f32_16x16x32_bf16 v[92:95], a[116:119], v[156:159], v[92:95]\n\t"
    "s_waitcnt lgkmcnt(7)\n\t"
    "v_exp_f32 v75, v75\n\t"
    "v_mfma_f32_16x16x32_bf16 v[160:163], a[120:123], v[32:35], v[160:163]\n\t"
    "v_exp_f32 v80, v80\n\t"
    "v_mfma_f32_16x16x32_bf16 v[164:167], a[124:127], v[32:35], v[164:167]\n\t"
    "v_exp_f32 v81, v81\n\t"
    "v_mfma_f32_16x16x32_bf16 v[168:171], a[128:131], v[32:35], v[168:171]\n\t"
    "v_exp_f32 v76, v76\n\t"
    "v_mfma_f32_16x16x32_bf16 v[172:175], a[132:135], v[32:35], v[172:175]\n\t"
    "v_exp_f32 v77, v77\n\t"
    "v_mfma_f32_16x16x32_bf16 a[0:3], a[136:139], v[32:35], a[0:3]\n\t"
    "v_exp_f32 v82, v82\n\t"
    "v_mfma_f32_16x16x32_bf16 a[4:7], a[140:143], v[32:35], a[4:7]\n\t"
    "s_waitcnt lgkmcnt(2)\n\t"
    "v_exp_f32 v83, v83\n\t"
    "v_mfma_f32_16x16x32_bf16 a[8:11], a[144:147], v[32:35], a[8:11]\n\t"
    "s_waitcnt lgkmcnt(0)\n\t"
    "ds_read_b64_tr_b16 a[168:169], v16 offset:17920\n\t"
    "v_cvt_pk_bf16_f32 v73, v74, v75\n\t"
    "v_mfma_f32_16x16x32_bf16 a[12:15], a[148:151], v[32:35], a[12:15]\n\t"
    "ds_read_b64_tr_b16 a[170:171], v16 offset:22016\n\t"
    "v_cvt_pk_bf16_f32 v74, v76, v77\n\t"
    "v_mfma_f32_16x16x32_bf16 a[72:75], v[216:219], v[32:35], a[72:75]\n\t"
    "v_exp_f32 v88, v88\n\t"
    "v_mfma_f32_16x16x32_bf16 v[176:179], a[120:123], v[40:43], v[176:179]\n\t"
    "ds_read_b64_tr_b16 a[172:173], v17 offset:17920\n\t"
    "v_cvt_pk_bf16_f32 v80, v80, v81\n\t"
    "v_mfma_f32_16x16x32_bf16 v[180:183], a[124:127], v[40:43], v[180:183]\n\t"
    "v_exp_f32 v89, v89\n\t"
    "v_mfma_f32_16x16x32_bf16 v[184:187], a[128:131], v[40:43], v[184:187]\n\t"
    "ds_read_b64_tr_b16 a[174:175], v17 offset:22016\n\t"
    "v_cvt_pk_bf16_f32 v81, v82, v83\n\t"
    "v_mfma_f32_16x16x32_bf16 v[188:191], a[132:135], v[40:43], v[188:191]\n\t"
    "v_exp_f32 v78, v78\n\t"
    "v_mfma_f32_16x16x32_bf16 a[16:19], a[136:139], v[40:43], a[16:19]\n\t"
    "ds_read_b64_tr_b16 a[176:177], v16 offset:17408\n\t"
    "v_cvt_pk_bf16_f32 v88, v88, v89\n\t"
    "v_mfma_f32_16x16x32_bf16 a[20:23], a[140:143], v[40:43], a[20:23]\n\t"
    "ds_read_b64_tr_b16 a[178:179], v16 offset:21504\n\t"
    "ds_read_b64_tr_b16 a[160:161], v14 offset:16384\n\t"
    "v_mfma_f32_16x16x32_bf16 a[24:27], a[144:147], v[40:43], a[24:27]\n\t"
    "v_exp_f32 v79, v79\n\t"
    "v_mfma_f32_16x16x32_bf16 a[28:31], a[148:151], v[40:43], a[28:31]\n\t"
    "ds_read_b64_tr_b16 a[180:181], v17 offset:17408\n\t"
    "v_cvt_pk_bf16_f32 v75, v78, v79\n\t"
    "v_mfma_f32_16x16x32_bf16 a[76:79], v[216:219], v[40:43], a[76:79]\n\t"
    "v_exp_f32 v84, v84\n\t"
    "v_mfma_f32_16x16x32_bf16 v[192:195], a[120:123], v[48:51], v[192:195]\n\t"
    "ds_read_b64_tr_b16 a[182:183], v17 offset:21504\n\t"
    "ds_read_b64_tr_b16 a[162:163], v14 offset:20480\n\t"
    "v_mfma_f32_16x16x32_bf16 v[196:199], a[124:127], v[48:51], v[196:199]\n\t"
    "v_exp_f32 v85, v85\n\t"
    "v_mfma_f32_16x16x32_bf16 v[200:203], a[128:131], v[48:51], v[200:203]\n\t"
    "v_exp_f32 v90, v90\n\t"
    "v_mfma_f32_16x16x32_bf16 v[204:207], a[132:135], v[48:51], v[204:207]\n\t"
    "v_exp_f32 v91, v91\n\t"
    "v_mfma_f32_16x16x32_bf16 a[32:35], a[136:139], v[48:51], a[32:35]\n\t"
    "v_exp_f32 v86, v86\n\t"
    "v_mfma_f32_16x16x32_bf16 a[36:39], a[140:143], v[48:51], a[36:39]\n\t"
    "v_exp_f32 v87, v87\n\t"
    "v_mfma_f32_16x16x32_bf16 a[40:43], a[144:147], v[48:51], a[40:43]\n\t"
    "v_exp_f32 v92, v92\n\t"
    "v_mfma_f32_16x16x32_bf16 a[44:47], a[148:151], v[48:51], a[44:47]\n\t"
    "v_exp_f32 v93, v93\n\t"
    "v_mfma_f32_16x16x32_bf16 a[80:83], v[216:219], v[48:51], a[80:83]\n\t"
    "v_cvt_pk_bf16_f32 v82, v84, v85\n\t"
    "v_cvt_pk_bf16_f32 v83, v86, v87\n\t"
    "v_mfma_f32_16x16x32_bf16 a[72:75], v[216:219], v[64:67], a[72:75]\n\t"
    "s_waitcnt lgkmcnt(0)\n\t"
    "v_cvt_pk_bf16_f32 v89, v90, v91\n\t"
    "v_cvt_pk_bf16_f32 v90, v92, v93\n\t"
    "v_mfma_f32_16x16x32_bf16 a[0:3], a[168:171], v[64:67], a[0:3]\n\t"
    "v_exp_f32 v94, v94\n\t"
    "v_mfma_f32_16x16x32_bf16 a[4:7], a[172:175], v[64:67], a[4:7]\n\t"
    "v_exp_f32 v95, v95\n\t"
    "v_mfma_f32_16x16x32_bf16 a[8:11], a[176:179], v[64:67], a[8:11]\n\t"
    "ds_read_b64_tr_b16 a[164:165], v15 offset:16384\n\t"
    "v_cvt_pk_bf16_f32 v91, v94, v95\n\t"
    "v_mfma_f32_16x16x32_bf16 a[12:15], a[180:183], v[64:67], a[12:15]\n\t"
    "ds_write_b128 v220, a[0:3] offset:0\n\t"
    "v_mfma_f32_16x16x32_bf16 a[76:79], v[216:219], v[72:75], a[76:79]\n\t"
    "ds_read_b64_tr_b16 a[166:167], v15 offset:20480\n\t"
    "ds_write_b32 v221, a72 offset:0\n\t"
    "v_mfma_f32_16x16x32_bf16 a[80:83], v[216:219], v[80:83], a[80:83]\n\t"
    "ds_write_b128 v220, a[4:7] offset:1024\n\t"
    "v_mfma_f32_16x16x32_bf16 a[84:87], v[216:219], v[88:91], a[84:87]\n\t"
    "ds_read_b64_tr_b16 a[158:159], v15 offset:20992\n\t"
    "v_mfma_f32_16x16x32_bf16 a[16:19], a[168:171], v[72:75], a[16:19]\n\t"
    "ds_write_b128 v220, a[8:11] offset:2048\n\t"
    "v_mfma_f32_16x16x32_bf16 a[20:23], a[172:175], v[72:75], a[20:23]\n\t"
    "ds_write_b32 v221, a76 offset:256\n\t"
    "ds_write_b32 v221, a80 offset:512\n\t"
    "v_mfma_f32_16x16x32_bf16 a[24:27], a[176:179], v[72:75], a[24:27]\n\t"
    "ds_write_b128 v220, a[12:15] offset:3072\n\t"
    "v_mfma_f32_16x16x32_bf16 a[28:31], a[180:183], v[72:75], a[28:31]\n\t"
    "ds_write_b32 v221, a84 offset:768\n\t"
    "v_accvgpr_read_b32 v232, a72\n\t"
    "v_mfma_f32_16x16x32_bf16 a[32:35], a[168:171], v[80:83], a[32:35]\n\t"
    "ds_write_b128 v220, a[16:19] offset:4096\n\t"
    "v_mfma_f32_16x16x32_bf16 a[36:39], a[172:175], v[80:83], a[36:39]\n\t"
    "v_accvgpr_read_b32 v233, a76\n\t"
    "v_accvgpr_read_b32 v234, a80\n\t"
    "v_mfma_f32_16x16x32_bf16 a[40:43], a[176:179], v[80:83], a[40:43]\n\t"
    "ds_write_b128 v220, a[20:23] offset:5120\n\t"
    "v_mfma_f32_16x16x32_bf16 a[44:47], a[180:183], v[80:83], a[44:47]\n\t"
    "s_waitcnt lgkmcnt(2)\n\t"
    "s_barrier\n\t"
    "v_mfma_f32_16x16x32_bf16 a[48:51], a[160:163], v[88:91], a[48:51]\n\t"
    "s_waitcnt lgkmcnt(6)\n\t"
    "ds_write_b128 v220, a[24:27] offset:6144\n\t"
    "v_mfma_f32_16x16x32_bf16 a[52:55], a[164:167], v[88:91], a[52:55]\n\t"
    "ds_read_b32 v236, v226 offset:0\n\t"
    "ds_read_b32 v237, v226 offset:256\n\t"
    "ds_read_b32 v238, v226 offset:512\n\t"
    "v_mfma_f32_16x16x32_bf16 a[56:59], a[168:171], v[88:91], a[56:59]\n\t"
    "ds_write_b128 v220, a[28:31] offset:7168\n\t"
    "ds_read_b32 v239, v225 offset:768\n\t"
    "ds_read_b32 v240, v226 offset:768\n\t"
    "ds_read_b32 v241, v227 offset:768\n\t"
    "v_accvgpr_read_b32 v235, a84\n\t"
    "v_mfma_f32_16x16x32_bf16 a[60:63], a[172:175], v[88:91], a[60:63]\n\t"
    "v_mfma_f32_16x16x32_bf16 a[64:67], a[176:179], v[88:91], a[64:67]\n\t"
    "ds_write_b128 v220, a[32:35] offset:8192\n\t"
    "v_mfma_f32_16x16x32_bf16 a[68:71], a[180:183], v[88:91], a[68:71]\n\t"
    "v_mfma_f32_16x16x32_bf16 v[160:163], a[152:155], v[64:67], v[160:163]\n\t"
    "s_waitcnt lgkmcnt(1)\n\t"
    "ds_write_b128 v220, a[36:39] offset:9216\n\t"
    "v_mfma_f32_16x16x32_bf16 v[176:179], a[152:155], v[72:75], v[176:179]\n\t"
    "v_add_f32 v232, v232, v236\n\t"
    "v_add_f32 v233, v233, v237\n\t"
    "v_mfma_f32_16x16x32_bf16 v[192:195], a[152:155], v[80:83], v[192:195]\n\t"
    "ds_write_b128 v220, a[40:43] offset:10240\n\t"
    "v_mfma_f32_16x16x32_bf16 v[208:211], a[152:155], v[88:91], v[208:211]\n\t"
    "v_add_f32 v234, v234, v238\n\t"
    "v_add_f32 v235, v235, v239\n\t"
    "v_mfma_f32_16x16x32_bf16 v[164:167], a[156:159], v[64:67], v[164:167]\n\t"
    "ds_write_b128 v220, a[44:47] offset:11264\n\t"
    "v_mfma_f32_16x16x32_bf16 v[180:183], a[156:159], v[72:75], v[180:183]\n\t"
    "v_add_f32 v240, v240, v241\n\t"
    "v_mfma_f32_16x16x32_bf16 v[196:199], a[156:159], v[80:83], v[196:199]\n\t"
    "ds_write_b128 v220, a[48:51] offset:12288\n\t"
    "v_mfma_f32_16x16x32_bf16 v[212:215], a[156:159], v[88:91], v[212:215]\n\t"
    "v_rcp_f32 v244, v232\n\t"
    "v_mfma_f32_16x16x32_bf16 v[168:171], a[160:163], v[64:67], v[168:171]\n\t"
    "ds_write_b128 v220, a[52:55] offset:13312\n\t"
    "v_mfma_f32_16x16x32_bf16 v[184:187], a[160:163], v[72:75], v[184:187]\n\t"
    "v_add_f32 v235, v235, v240\n\t"
    "v_mov_b32 v245, v244\n\t"
    "v_mfma_f32_16x16x32_bf16 v[200:203], a[160:163], v[80:83], v[200:203]\n\t"
    "ds_write_b128 v220, a[56:59] offset:14336\n\t"
    "v_mfma_f32_16x16x32_bf16 v[172:175], a[164:167], v[64:67], v[172:175]\n\t"
    "v_rcp_f32 v246, v233\n\t"
    "v_mfma_f32_16x16x32_bf16 v[188:191], a[164:167], v[72:75], v[188:191]\n\t"
    "ds_write_b128 v220, a[60:63] offset:15360\n\t"
    "v_mfma_f32_16x16x32_bf16 v[204:207], a[164:167], v[80:83], v[204:207]\n\t"
    "v_rcp_f32 v248, v234\n\t"
    "s_mov_b32 s60, 16384\n\t"
    "s_mov_b32 s61, 20480\n\t"
    "s_mov_b32 s62, 24576\n\t"
    "s_mov_b32 s63, 12288\n\t"
    "ds_write_b128 v220, a[64:67] offset:16384\n\t"
    "ds_write_b128 v220, a[68:71] offset:17408\n\t"
    "s_nop 3\n\t"
    "v_rcp_f32 v250, v235\n\t"
    "s_nop 0\n\t"
    "v_mov_b32 v247, v246\n\t"
    "v_mov_b32 v249, v248\n\t"
    "v_mov_b32 v251, v250\n\t"
    "v_pk_mul_f32 v[160:161], v[160:161], v[244:245]\n\t"
    "v_pk_mul_f32 v[162:163], v[162:163], v[244:245]\n\t"
    "v_pk_mul_f32 v[176:177], v[176:177], v[246:247]\n\t"
    "v_pk_mul_f32 v[178:179], v[178:179], v[246:247]\n\t"
    "v_pk_mul_f32 v[192:193], v[192:193], v[248:249]\n\t"
    "v_pk_mul_f32 v[194:195], v[194:195], v[248:249]\n\t"
    "v_pk_mul_f32 v[208:209], v[208:209], v[250:251]\n\t"
    "v_pk_mul_f32 v[210:211], v[210:211], v[250:251]\n\t"
    "v_pk_mul_f32 v[164:165], v[164:165], v[244:245]\n\t"
    "v_pk_mul_f32 v[166:167], v[166:167], v[244:245]\n\t"
    "v_pk_mul_f32 v[180:181], v[180:181], v[246:247]\n\t"
    "v_pk_mul_f32 v[182:183], v[182:183], v[246:247]\n\t"
    "v_pk_mul_f32 v[196:197], v[196:197], v[248:249]\n\t"
    "v_pk_mul_f32 v[198:199], v[198:199], v[248:249]\n\t"
    "v_pk_mul_f32 v[212:213], v[212:213], v[250:251]\n\t"
    "v_pk_mul_f32 v[214:215], v[214:215], v[250:251]\n\t"
    "v_pk_mul_f32 v[168:169], v[168:169], v[244:245]\n\t"
    "v_pk_mul_f32 v[170:171], v[170:171], v[244:245]\n\t"
    "v_pk_mul_f32 v[184:185], v[184:185], v[246:247]\n\t"
    "v_pk_mul_f32 v[186:187], v[186:187], v[246:247]\n\t"
    "v_pk_mul_f32 v[200:201], v[200:201], v[248:249]\n\t"
    "v_pk_mul_f32 v[202:203], v[202:203], v[248:249]\n\t"
    "v_pk_mul_f32 v[172:173], v[172:173], v[244:245]\n\t"
    "v_pk_mul_f32 v[174:175], v[174:175], v[244:245]\n\t"
    "v_pk_mul_f32 v[188:189], v[188:189], v[246:247]\n\t"
    "v_pk_mul_f32 v[190:191], v[190:191], v[246:247]\n\t"
    "v_pk_mul_f32 v[204:205], v[204:205], v[248:249]\n\t"
    "v_pk_mul_f32 v[206:207], v[206:207], v[248:249]\n\t"
    "s_waitcnt lgkmcnt(0)\n\t"
    "s_barrier\n\t"
    "ds_read_b128 v[32:35], v223 offset:0\n\t"
    "ds_read_b128 v[36:39], v223 offset:1024\n\t"
    "ds_read_b128 v[40:43], v223 offset:2048\n\t"
    "ds_read_b128 v[44:47], v223 offset:3072\n\t"
    "ds_read_b128 v[48:51], v223 offset:4096\n\t"
    "ds_read_b128 v[52:55], v223 offset:5120\n\t"
    "s_waitcnt lgkmcnt(4)\n\t"
    "v_pk_fma_f32 v[160:161], v[32:33], v[244:245], v[160:161]\n\t"
    "v_pk_fma_f32 v[162:163], v[34:35], v[244:245], v[162:163]\n\t"
    "ds_read_b128 v[56:59], v223 offset:6144\n\t"
    "ds_read_b128 v[60:63], v223 offset:7168\n\t"
    "v_pk_fma_f32 v[164:165], v[36:37], v[244:245], v[164:165]\n\t"
    "v_pk_fma_f32 v[166:167], v[38:39], v[244:245], v[166:167]\n\t"
    "v_cvt_pk_bf16_f32 v112, v160, v161\n\t"
    "ds_read_b128 v[64:67], v223 offset:8192\n\t"
    "ds_read_b128 v[68:71], v223 offset:9216\n\t"
    "v_cvt_pk_bf16_f32 v113, v162, v163\n\t"
    "v_cvt_pk_bf16_f32 v114, v164, v165\n\t"
    "v_cvt_pk_bf16_f32 v115, v166, v167\n\t"
    "ds_read_b128 v[72:75], v223 offset:10240\n\t"
    "ds_read_b128 v[76:79], v223 offset:11264\n\t"
    "s_waitcnt lgkmcnt(8)\n\t"
    "v_pk_fma_f32 v[168:169], v[40:41], v[244:245], v[168:169]\n\t"
    "v_pk_fma_f32 v[170:171], v[42:43], v[244:245], v[170:171]\n\t"
    "ds_read_b128 v[80:83], v222 offset:12288\n\t"
    "ds_read_b128 v[84:87], v222 offset:13312\n\t"
    "v_pk_fma_f32 v[172:173], v[44:45], v[244:245], v[172:173]\n\t"
    "v_pk_fma_f32 v[174:175], v[46:47], v[244:245], v[174:175]\n\t"
    "v_cvt_pk_bf16_f32 v116, v168, v169\n\t"
    "ds_read_b128 v[88:91], v223 offset:14336\n\t"
    "ds_read_b128 v[92:95], v223 offset:15360\n\t"
    "v_cvt_pk_bf16_f32 v117, v170, v171\n\t"
    "v_cvt_pk_bf16_f32 v118, v172, v173\n\t"
    "v_cvt_pk_bf16_f32 v119, v174, v175\n\t"
    "ds_read_b128 v[96:99], v224 offset:16384\n\t"
    "ds_read_b128 v[100:103], v224 offset:17408\n\t"
    "v_permlane16_swap_b32 v112, v114\n\t"
    "v_permlane16_swap_b32 v113, v115\n\t"
    "s_waitcnt lgkmcnt(12)\n\t"
    "v_pk_fma_f32 v[176:177], v[48:49], v[246:247], v[176:177]\n\t"
    "v_pk_fma_f32 v[178:179], v[50:51], v[246:247], v[178:179]\n\t"
    "v_pk_fma_f32 v[180:181], v[52:53], v[246:247], v[180:181]\n\t"
    "v_pk_fma_f32 v[182:183], v[54:55], v[246:247], v[182:183]\n\t"
    "v_permlane16_swap_b32 v116, v118\n\t"
    "v_permlane16_swap_b32 v117, v119\n\t"
    "v_cvt_pk_bf16_f32 v120, v176, v177\n\t"
    "v_cvt_pk_bf16_f32 v121, v178, v179\n\t"
    "v_cvt_pk_bf16_f32 v122, v180, v181\n\t"
    "v_cvt_pk_bf16_f32 v123, v182, v183\n\t"
    "s_waitcnt lgkmcnt(10)\n\t"
    "v_pk_fma_f32 v[184:185], v[56:57], v[246:247], v[184:185]\n\t"
    "v_pk_fma_f32 v[186:187], v[58:59], v[246:247], v[186:187]\n\t"
    "v_pk_fma_f32 v[188:189], v[60:61], v[246:247], v[188:189]\n\t"
    "v_pk_fma_f32 v[190:191], v[62:63], v[246:247], v[190:191]\n\t"
    "v_mov_b32 v140, v116\n\t"
    "v_mov_b32 v141, v117\n\t"
    "v_mov_b32 v142, v118\n\t"
    "v_mov_b32 v143, v119\n\t"
    "v_mov_b32_dpp v116, v112 row_ror:8 row_mask:0xf bank_mask:0x3\n\t"
    "v_mov_b32_dpp v117, v113 row_ror:8 row_mask:0xf bank_mask:0x3\n\t"
    "v_mov_b32_dpp v118, v114 row_ror:8 row_mask:0xf bank_mask:0x3\n\t"
    "v_mov_b32_dpp v119, v115 row_ror:8 row_mask:0xf bank_mask:0x3\n\t"
    "v_mov_b32_dpp v112, v140 row_ror:8 row_mask:0xf bank_mask:0xc\n\t"
    "v_mov_b32_dpp v113, v141 row_ror:8 row_mask:0xf bank_mask:0xc\n\t"
    "v_mov_b32_dpp v114, v142 row_ror:8 row_mask:0xf bank_mask:0xc\n\t"
    "v_mov_b32_dpp v115, v143 row_ror:8 row_mask:0xf bank_mask:0xc\n\t"
    "buffer_store_dwordx4 v[112:115], v229, s[48:51], s60 offen offset:0 sc1\n\t"
    "v_cvt_pk_bf16_f32 v124, v184, v185\n\t"
    "v_cvt_pk_bf16_f32 v125, v186, v187\n\t"
    "v_cvt_pk_bf16_f32 v126, v188, v189\n\t"
    "v_cvt_pk_bf16_f32 v127, v190, v191\n\t"
    "v_permlane16_swap_b32 v120, v122\n\t"
    "v_permlane16_swap_b32 v121, v123\n\t"
    "s_waitcnt lgkmcnt(8)\n\t"
    "v_pk_fma_f32 v[192:193], v[64:65], v[248:249], v[192:193]\n\t"
    "v_pk_fma_f32 v[194:195], v[66:67], v[248:249], v[194:195]\n\t"
    "v_pk_fma_f32 v[196:197], v[68:69], v[248:249], v[196:197]\n\t"
    "v_pk_fma_f32 v[198:199], v[70:71], v[248:249], v[198:199]\n\t"
    "buffer_store_dwordx4 v[116:119], v229, s[48:51], s60 offen offset:2048 sc1\n\t"
    "v_permlane16_swap_b32 v124, v126\n\t"
    "v_permlane16_swap_b32 v125, v127\n\t"
    "v_cvt_pk_bf16_f32 v128, v192, v193\n\t"
    "v_cvt_pk_bf16_f32 v129, v194, v195\n\t"
    "v_cvt_pk_bf16_f32 v130, v196, v197\n\t"
    "v_cvt_pk_bf16_f32 v131, v198, v199\n\t"
    "s_waitcnt lgkmcnt(6)\n\t"
    "v_pk_fma_f32 v[200:201], v[72:73], v[248:249], v[200:201]\n\t"
    "v_pk_fma_f32 v[202:203], v[74:75], v[248:249], v[202:203]\n\t"
    "v_pk_fma_f32 v[204:205], v[76:77], v[248:249], v[204:205]\n\t"
    "v_pk_fma_f32 v[206:207], v[78:79], v[248:249], v[206:207]\n\t"
    "v_mov_b32 v140, v124\n\t"
    "v_mov_b32 v141, v125\n\t"
    "v_mov_b32 v142, v126\n\t"
    "v_mov_b32 v143, v127\n\t"
    "v_mov_b32_dpp v124, v120 row_ror:8 row_mask:0xf bank_mask:0x3\n\t"
    "v_mov_b32_dpp v125, v121 row_ror:8 row_mask:0xf bank_mask:0x3\n\t"
    "v_mov_b32_dpp v126, v122 row_ror:8 row_mask:0xf bank_mask:0x3\n\t"
    "v_mov_b32_dpp v127, v123 row_ror:8 row_mask:0xf bank_mask:0x3\n\t"
    "v_mov_b32_dpp v120, v140 row_ror:8 row_mask:0xf bank_mask:0xc\n\t"
    "v_mov_b32_dpp v121, v141 row_ror:8 row_mask:0xf bank_mask:0xc\n\t"
    "v_mov_b32_dpp v122, v142 row_ror:8 row_mask:0xf bank_mask:0xc\n\t"
    "v_mov_b32_dpp v123, v143 row_ror:8 row_mask:0xf bank_mask:0xc\n\t"
    "buffer_store_dwordx4 v[120:123], v229, s[48:51], s61 offen offset:0 sc1\n\t"
    "v_cvt_pk_bf16_f32 v132, v200, v201\n\t"
    "v_cvt_pk_bf16_f32 v133, v202, v203\n\t"
    "v_cvt_pk_bf16_f32 v134, v204, v205\n\t"
    "v_cvt_pk_bf16_f32 v135, v206, v207\n\t"
    "v_permlane16_swap_b32 v128, v130\n\t"
    "v_permlane16_swap_b32 v129, v131\n\t"
    "s_waitcnt lgkmcnt(0)\n\t"
    "v_pk_fma_f32 v[208:209], v[80:81], v[250:251], v[208:209]\n\t"
    "v_pk_fma_f32 v[210:211], v[82:83], v[250:251], v[210:211]\n\t"
    "v_pk_fma_f32 v[208:209], v[88:89], v[250:251], v[208:209]\n\t"
    "v_pk_fma_f32 v[210:211], v[90:91], v[250:251], v[210:211]\n\t"
    "v_pk_fma_f32 v[208:209], v[96:97], v[250:251], v[208:209]\n\t"
    "buffer_store_dwordx4 v[124:127], v229, s[48:51], s61 offen offset:2048 sc1\n\t"
    "v_pk_fma_f32 v[210:211], v[98:99], v[250:251], v[210:211]\n\t"
    "v_pk_fma_f32 v[212:213], v[84:85], v[250:251], v[212:213]\n\t"
    "v_pk_fma_f32 v[214:215], v[86:87], v[250:251], v[214:215]\n\t"
    "v_pk_fma_f32 v[212:213], v[92:93], v[250:251], v[212:213]\n\t"
    "v_pk_fma_f32 v[214:215], v[94:95], v[250:251], v[214:215]\n\t"
    "v_pk_fma_f32 v[212:213], v[100:101], v[250:251], v[212:213]\n\t"
    "v_pk_fma_f32 v[214:215], v[102:103], v[250:251], v[214:215]\n\t"
    "v_permlane16_swap_b32 v132, v134\n\t"
    "v_permlane16_swap_b32 v133, v135\n\t"
    "v_cvt_pk_bf16_f32 v136, v208, v209\n\t"
    "v_cvt_pk_bf16_f32 v137, v210, v211\n\t"
    "v_cvt_pk_bf16_f32 v138, v212, v213\n\t"
    "v_cvt_pk_bf16_f32 v139, v214, v215\n\t"
    "v_mov_b32 v140, v132\n\t"
    "v_mov_b32 v141, v133\n\t"
    "v_mov_b32 v142, v134\n\t"
    "v_mov_b32 v143, v135\n\t"
    "v_mov_b32_dpp v132, v128 row_ror:8 row_mask:0xf bank_mask:0x3\n\t"
    "v_mov_b32_dpp v133, v129 row_ror:8 row_mask:0xf bank_mask:0x3\n\t"
    "v_mov_b32_dpp v134, v130 row_ror:8 row_mask:0xf bank_mask:0x3\n\t"
    "v_mov_b32_dpp v135, v131 row_ror:8 row_mask:0xf bank_mask:0x3\n\t"
    "v_mov_b32_dpp v128, v140 row_ror:8 row_mask:0xf bank_mask:0xc\n\t"
    "v_mov_b32_dpp v129, v141 row_ror:8 row_mask:0xf bank_mask:0xc\n\t"
    "v_mov_b32_dpp v130, v142 row_ror:8 row_mask:0xf bank_mask:0xc\n\t"
    "v_mov_b32_dpp v131, v143 row_ror:8 row_mask:0xf bank_mask:0xc\n\t"
    "buffer_store_dwordx4 v[128:131], v229, s[48:51], s62 offen offset:0 sc1\n\t"
    "v_permlane16_swap_b32 v136, v138\n\t"
    "v_permlane16_swap_b32 v137, v139\n\t"
    "buffer_store_dwordx4 v[132:135], v229, s[48:51], s62 offen offset:2048 sc1\n\t"
    "s_nop 0\n\t"
    "buffer_store_dwordx4 v[136:139], v228, s[48:51], s63 offen sc1\n\t"
    "v_mov_b32 v242, 0\n\t"
    "v_mov_b32 v243, 0\n\t"
    "v_pk_fma_f32 v[242:243], v[160:161], 0, v[242:243]\n\t"
    "v_pk_fma_f32 v[242:243], v[162:163], 0, v[242:243]\n\t"
    "v_pk_fma_f32 v[242:243], v[164:165], 0, v[242:243]\n\t"
    "v_pk_fma_f32 v[242:243], v[166:167], 0, v[242:243]\n\t"
    "v_pk_fma_f32 v[242:243], v[168:169], 0, v[242:243]\n\t"
    "v_pk_fma_f32 v[242:243], v[170:171], 0, v[242:243]\n\t"
    "v_pk_fma_f32 v[242:243], v[172:173], 0, v[242:243]\n\t"
    "v_pk_fma_f32 v[242:243], v[174:175], 0, v[242:243]\n\t"
    "v_pk_fma_f32 v[242:243], v[176:177], 0, v[242:243]\n\t"
    "v_pk_fma_f32 v[242:243], v[178:179], 0, v[242:243]\n\t"
    "v_pk_fma_f32 v[242:243], v[180:181], 0, v[242:243]\n\t"
    "v_pk_fma_f32 v[242:243], v[182:183], 0, v[242:243]\n\t"
    "v_pk_fma_f32 v[242:243], v[184:185], 0, v[242:243]\n\t"
    "v_pk_fma_f32 v[242:243], v[186:187], 0, v[242:243]\n\t"
    "v_pk_fma_f32 v[242:243], v[188:189], 0, v[242:243]\n\t"
    "v_pk_fma_f32 v[242:243], v[190:191], 0, v[242:243]\n\t"
    "v_pk_fma_f32 v[242:243], v[192:193], 0, v[242:243]\n\t"
    "v_pk_fma_f32 v[242:243], v[194:195], 0, v[242:243]\n\t"
    "v_pk_fma_f32 v[242:243], v[196:197], 0, v[242:243]\n\t"
    "v_pk_fma_f32 v[242:243], v[198:199], 0, v[242:243]\n\t"
    "v_pk_fma_f32 v[242:243], v[200:201], 0, v[242:243]\n\t"
    "v_pk_fma_f32 v[242:243], v[202:203], 0, v[242:243]\n\t"
    "v_pk_fma_f32 v[242:243], v[204:205], 0, v[242:243]\n\t"
    "v_pk_fma_f32 v[242:243], v[206:207], 0, v[242:243]\n\t"
    "v_pk_fma_f32 v[242:243], v[208:209], 0, v[242:243]\n\t"
    "v_pk_fma_f32 v[242:243], v[210:211], 0, v[242:243]\n\t"
    "v_pk_fma_f32 v[242:243], v[212:213], 0, v[242:243]\n\t"
    "v_pk_fma_f32 v[242:243], v[214:215], 0, v[242:243]\n\t"
    "v_add_f32 v242, v242, v243\n\t"
    "v_min_f32 v236, v232, v233\n\t"
    "v_min_f32 v237, v234, v235\n\t"
    "v_min_f32 v236, v236, v237\n\t"
    "v_add_f32 v237, v232, v233\n\t"
    "v_add_f32 v238, v234, v235\n\t"
    "v_add_f32 v237, v237, v238\n\t"
    "v_add_f32 v242, v242, v237\n\t"
    "s_mov_b32 s64, 0x17800000\n\t"
    "s_mov_b32 s65, 0x67800000\n\t"
    "v_cmp_gt_f32 s[72:73], s64, v236\n\t"
    "v_cmp_nge_f32 vcc, s65, v242\n\t"
    "s_nop 1\n\t"
    "s_or_b64 s[72:73], s[72:73], vcc\n\t"
    "s_or_b32 s70, s72, s73\n\t"
    "L_end_%=:\n\t"
    "s_mov_b32 %0, s70"
    : "=s"(bad)
    : "s"(__builtin_amdgcn_kernarg_segment_ptr()), "s"(0), "s"(0), "s"(0), "s"(bid), "v"(tid), "s"(ldsb)
    : "v1", "v2", "v3", "v4", "v5", "v6", "v7", "v8", "v9", "v10", "v11", "v12", "v13", "v14", "v15", "v16", "v17", "v18", "v19", "v20", "v21", "v22", "v23", "v24", "v25", "v26", "v27", "v28", "v29", "v30", "v31", "v32", "v33", "v34", "v35", "v36", "v37", "v38", "v39", "v40", "v41", "v42", "v43", "v44", "v45", "v46", "v47", "v48", "v49", "v50", "v51", "v52", "v53", "v54", "v55", "v56", "v57", "v58", "v59", "v60", "v61", "v62", "v63", "v64", "v65", "v66", "v67", "v68", "v69", "v70", "v71", "v72", "v73", "v74", "v75", "v76", "v77", "v78", "v79", "v80", "v81", "v82", "v83", "v84", "v85", "v86", "v87", "v88", "v89", "v90", "v91", "v92", "v93", "v94", "v95", "v96", "v97", "v98", "v99", "v100", "v101", "v102", "v103", "v104", "v105", "v106", "v107", "v108", "v109", "v110", "v111", "v112", "v113", "v114", "v115", "v116", "v117", "v118", "v119", "v120", "v121", "v122", "v123", "v124", "v125", "v126", "v127", "v128", "v129", "v130", "v131", "v132", "v133", "v134", "v135", "v136", "v137", "v138", "v139", "v140", "v141", "v142", "v143", "v144", "v145", "v146", "v147", "v148", "v149", "v150", "v151", "v152", "v153", "v154", "v155", "v156", "v157", "v158", "v159", "v160", "v161", "v162", "v163", "v164", "v165", "v166", "v167", "v168", "v169", "v170", "v171", "v172", "v173", "v174", "v175", "v176", "v177", "v178", "v179", "v180", "v181", "v182", "v183", "v184", "v185", "v186", "v187", "v188", "v189", "v190", "v191", "v192", "v193", "v194", "v195", "v196", "v197", "v198", "v199", "v200", "v201", "v202", "v203", "v204", "v205", "v206", "v207", "v208", "v209", "v210", "v211", "v212", "v213", "v214", "v215", "v216", "v217", "v218", "v219", "v220", "v221", "v222", "v223", "v224", "v225", "v226", "v227", "v228", "v229", "v230", "v231", "v232", "v233", "v234", "v235", "v236", "v237", "v238", "v239", "v240", "v241", "v242", "v243", "v244", "v245", "v246", "v247", "v248", "v249", "v250", "v251", "v252", "v253", "v254", "v255", "a0", "a1", "a2", "a3", "a4", "a5", "a6", "a7", "a8", "a9", "a10", "a11", "a12", "a13", "a14", "a15", "a16", "a17", "a18", "a19", "a20", "a21", "a22", "a23", "a24", "a25", "a26", "a27", "a28", "a29", "a30", "a31", "a32", "a33", "a34", "a35", "a36", "a37", "a38", "a39", "a40", "a41", "a42", "a43", "a44", "a45", "a46", "a47", "a48", "a49", "a50", "a51", "a52", "a53", "a54", "a55", "a56", "a57", "a58", "a59", "a60", "a61", "a62", "a63", "a64", "a65", "a66", "a67", "a68", "a69", "a70", "a71", "a72", "a73", "a74", "a75", "a76", "a77", "a78", "a79", "a80", "a81", "a82", "a83", "a84", "a85", "a86", "a87", "a88", "a89", "a90", "a91", "a92", "a93", "a94", "a95", "a96", "a97", "a98", "a99", "a100", "a101", "a102", "a103", "a104", "a105", "a106", "a107", "a108", "a109", "a110", "a111", "a112", "a113", "a114", "a115", "a116", "a117", "a118", "a119", "a120", "a121", "a122", "a123", "a124", "a125", "a126", "a127", "a128", "a129", "a130", "a131", "a132", "a133", "a134", "a135", "a136", "a137", "a138", "a139", "a140", "a141", "a142", "a143", "a144", "a145", "a146", "a147", "a148", "a149", "a150", "a151", "a152", "a153", "a154", "a155", "a156", "a157", "a158", "a159", "a160", "a161", "a162", "a163", "a164", "a165", "a166", "a167", "a168", "a169", "a170", "a171", "a172", "a173", "a174", "a175", "a176", "a177", "a178", "a179", "a180", "a181", "a182", "a183", "a184", "a185", "a186", "a187", "a188", "a189", "a190", "a191", "a192", "a193", "a194", "a195", "a196", "a197", "a198", "a199", "a200", "a201", "a202", "a203", "a204", "a205", "a206", "a207", "a208", "a209", "a210", "a211", "a212", "a213", "a214", "a215", "a216", "a217", "a218", "a219", "a220", "a221", "a222", "a223", "a224", "a225", "a226", "a227", "a228", "a229", "a230", "a231", "a232", "a233", "a234", "a235", "a236", "a237", "a238", "a239", "a240", "a241", "a242", "a243", "a244", "a245", "a246", "a247", "s36", "s37", "s38", "s39", "s40", "s41", "s42", "s43", "s44", "s45", "s46", "s47", "s48", "s49", "s50", "s51", "s52", "s53", "s54", "s55", "s56", "s57", "s58", "s59", "s60", "s61", "s62", "s63", "s64", "s65", "s66", "s67", "s68", "s69", "s70", "s71", "s72", "s73", "s74", "s75", "s76", "s77", "s78", "s79", "s80", "s81", "s82", "s83", "s84", "s85", "s86", "s87", "s88", "s89", "s90", "s91", "s92", "s93", "s94", "s95", "s96", "s97", "s98", "s99", "vcc", "scc", "memory");
  if (bad) slow_block(Q, K, V, O, bid, tid, lds);
}

extern "C" void kernel_launch(void* const* d_in, const int* in_sizes, int n_in, void* d_out, int out_size, void* d_ws,
                              size_t ws_size, hipStream_t stream) {
  gqa_fwd<<<256, 256, 0, stream>>>((const char*)d_in[0], (const char*)d_in[1], (const char*)d_in[2], (char*)d_out);
}
```
